```python
import torch

def _softmax(x, axis=-1):
    return torch.softmax(x, dim=axis)
def _gelu(x):
    return torch.nn.functional.gelu(x, approximate="tanh")
def _silu(x):
    return torch.nn.functional.silu(x)
def _sigmoid(x):
    return torch.sigmoid(x)
def _layernorm(x, axis=-1, eps=1e-5):
    m = x.mean(dim=axis, keepdim=True)
    v = x.var(dim=axis, keepdim=True, unbiased=False)
    return (x - m) / torch.sqrt(v + eps)
def _scatter_add(tgt, idx, src, axis=0):
    return tgt.clone().index_add_(axis, idx, src)
def _segment_sum(src, seg, n):
    out = torch.zeros((n,) + src.shape[1:], dtype=src.dtype)
    return out.index_add_(0, seg, src)
def _pad(x, pw):
    flat = tuple(v for pair in reversed(pw) for v in pair)
    return torch.nn.functional.pad(x, flat)

kernel_name = 'concat_heads__gqa_block_cached__gqa_block_cached__near_ridge'

def setup_inputs():

    _w = (lambda a: (lambda t: t.to(torch.bfloat16) if t.is_floating_point() else t)(torch.as_tensor(a)))
    def _A_setup():
        B, Sq, D = 8, 160, 640
        Hq, Hkv, Sk, Dh = 32, 32, 512, 128
        assert Hq % Hkv == 0, (Hq, Hkv)
        return {
            "x": _w((lambda *s: torch.randn(*s, dtype=torch.bfloat16) * 0.8)(B, Sq, D)),
            "k_cache": _w((lambda *s: torch.randn(*s, dtype=torch.bfloat16) * 0.8)(B, Hkv, Sk, Dh)),
            "v_cache": _w((lambda *s: torch.randn(*s, dtype=torch.bfloat16) * 0.05)(B, Hkv, Sk, Dh)),
            "wq": _w((lambda *s: torch.randn(*s, dtype=torch.bfloat16) * 0.8)(D, Hq * Dh) * (D ** -0.5)),
            "wo": _w((lambda *s: torch.randn(*s, dtype=torch.bfloat16) * 0.05)(Hq * Dh, D) * ((Hq * Dh) ** -0.5)),
        }
    _A_in = _A_setup()

    def _B_setup():
        B, Sq, D = 8, 160, 640
        Hq, Hkv, Sk, Dh = 32, 32, 512, 128
        assert Hq % Hkv == 0, (Hq, Hkv)
        return {
            "x": _w((lambda *s: torch.randn(*s, dtype=torch.bfloat16) * 0.8)(B, Sq, D)),
            "k_cache": _w((lambda *s: torch.randn(*s, dtype=torch.bfloat16) * 0.8)(B, Hkv, Sk, Dh)),
            "v_cache": _w((lambda *s: torch.randn(*s, dtype=torch.bfloat16) * 0.05)(B, Hkv, Sk, Dh)),
            "wq": _w((lambda *s: torch.randn(*s, dtype=torch.bfloat16) * 0.8)(D, Hq * Dh) * (D ** -0.5)),
            "wo": _w((lambda *s: torch.randn(*s, dtype=torch.bfloat16) * 0.05)(Hq * Dh, D) * ((Hq * Dh) ** -0.5)),
        }
    _B_in = _B_setup()

    return {
        "A_x": _A_in["x"],
        "A_k_cache": _A_in["k_cache"],
        "A_v_cache": _A_in["v_cache"],
        "A_wq": _A_in["wq"],
        "A_wo": _A_in["wo"],
        "B_x": _B_in["x"],
        "B_k_cache": _B_in["k_cache"],
        "B_v_cache": _B_in["v_cache"],
        "B_wq": _B_in["wq"],
        "B_wo": _B_in["wo"],
    }

def reference(A_x, A_k_cache, A_v_cache, A_wq, A_wo, B_x, B_k_cache, B_v_cache, B_wq, B_wo):
    def _A_ref(x, k_cache, v_cache, wq, wo):
        B, Sq, D = x.shape
        Hq, Dh = 32, 128
        Hkv = k_cache.shape[1]
        rep = Hq // Hkv
        q = (lambda x, *d: x.permute(*d))((x @ wq).reshape(B, Sq, Hq, Dh), 0, 2, 1, 3)
        k = (lambda x, n, axis: torch.repeat_interleave(x, n, dim=axis))(k_cache, rep, axis=1)
        v = (lambda x, n, axis: torch.repeat_interleave(x, n, dim=axis))(v_cache, rep, axis=1)
        scores = torch.einsum("bhqd,bhkd->bhqk", q, k) * (Dh ** -0.5)
        p = _softmax(scores, axis=-1)
        o = torch.einsum("bhqk,bhkd->bhqd", p, v)
        return (lambda x, *d: x.permute(*d))(o, 0, 2, 1, 3).reshape(B, Sq, Hq * Dh) @ wo

    def _B_ref(x, k_cache, v_cache, wq, wo):
        B, Sq, D = x.shape
        Hq, Dh = 32, 128
        Hkv = k_cache.shape[1]
        rep = Hq // Hkv
        q = (lambda x, *d: x.permute(*d))((x @ wq).reshape(B, Sq, Hq, Dh), 0, 2, 1, 3)
        k = (lambda x, n, axis: torch.repeat_interleave(x, n, dim=axis))(k_cache, rep, axis=1)
        v = (lambda x, n, axis: torch.repeat_interleave(x, n, dim=axis))(v_cache, rep, axis=1)
        scores = torch.einsum("bhqd,bhkd->bhqk", q, k) * (Dh ** -0.5)
        p = _softmax(scores, axis=-1)
        o = torch.einsum("bhqk,bhkd->bhqd", p, v)
        return (lambda x, *d: x.permute(*d))(o, 0, 2, 1, 3).reshape(B, Sq, Hq * Dh) @ wo

    a = _A_ref(A_x, A_k_cache, A_v_cache, A_wq, A_wo)
    b = _B_ref(B_x, B_k_cache, B_v_cache, B_wq, B_wo)
    return torch.concatenate([a, b], axis=-1)
```

```cpp
#include <hip/hip_runtime.h>
#include <hip/hip_bf16.h>
typedef unsigned short u16;
__device__ __forceinline__ float bf2f(u16 v){ return __uint_as_float(((unsigned)v)<<16); }
__device__ __forceinline__ u16 f2bf(float f){ __hip_bfloat16 b = __float2bfloat16(f); return *(u16*)&b; }
__global__ void gemm_naive(const u16* A, const u16* B, u16* C, int M, int N, int K, int ldc){
  __shared__ float As[16][17], Bs[16][17];
  int tx=threadIdx.x, ty=threadIdx.y; int row=blockIdx.y*16+ty, col=blockIdx.x*16+tx; float acc=0;
  for(int k0=0;k0<K;k0+=16){ As[ty][tx]=bf2f(A[(size_t)row*K+k0+tx]); Bs[ty][tx]=bf2f(B[(size_t)(k0+ty)*N+col]); __syncthreads();
    for(int k=0;k<16;k++) acc+=As[ty][k]*Bs[k][tx]; __syncthreads(); }
  C[(size_t)row*ldc+col]=f2bf(acc);
}
__global__ void attn_naive(const u16* Q, const u16* K, const u16* V, u16* O){
  int q=blockIdx.x, h=blockIdx.y, b=blockIdx.z; int t=threadIdx.x;
  __shared__ float qs[128], s[512], red[128];
  qs[t]=bf2f(Q[((size_t)(b*160+q))*4096+h*128+t]); __syncthreads();
  const u16* Kb=K+((size_t)(b*32+h))*512*128; const u16* Vb=V+((size_t)(b*32+h))*512*128;
  float mx=-1e30f;
  for(int i=0;i<4;i++){ int k=t+i*128; float a=0; for(int d=0;d<128;d++) a+=qs[d]*bf2f(Kb[k*128+d]); a*=0.08838834764f; s[k]=a; mx=fmaxf(mx,a);}
  red[t]=mx; __syncthreads(); for(int o=64;o>0;o>>=1){ if(t<o) red[t]=fmaxf(red[t],red[t+o]); __syncthreads(); } mx=red[0]; __syncthreads();
  float sm=0; for(int i=0;i<4;i++){ int k=t+i*128; float e=__expf(s[k]-mx); s[k]=e; sm+=e; }
  red[t]=sm; __syncthreads(); for(int o=64;o>0;o>>=1){ if(t<o) red[t]+=red[t+o]; __syncthreads(); } sm=red[0];
  float a=0; for(int k=0;k<512;k++) a+=s[k]*bf2f(Vb[k*128+t]);
  O[((size_t)(b*160+q))*4096+h*128+t]=f2bf(a/sm);
}
extern "C" void kernel_launch(void* const* d_in, const int* in_sizes, int n_in, void* d_out, int out_size, void* d_ws, size_t ws_size, hipStream_t stream){
  u16* ws=(u16*)d_ws;
  for(int br=0;br<2;br++){
    const u16* x=(const u16*)d_in[br*5+0]; const u16* k=(const u16*)d_in[br*5+1]; const u16* v=(const u16*)d_in[br*5+2];
    const u16* wq=(const u16*)d_in[br*5+3]; const u16* wo=(const u16*)d_in[br*5+4];
    u16* Q=ws+(size_t)br*2*1280*4096; u16* O=Q+(size_t)1280*4096;
    gemm_naive<<<dim3(4096/16,1280/16),dim3(16,16),0,stream>>>(x,wq,Q,1280,4096,640,4096);
    attn_naive<<<dim3(160,32,8),128,0,stream>>>(Q,k,v,O);
    gemm_naive<<<dim3(640/16,1280/16),dim3(16,16),0,stream>>>(O,wo,(u16*)d_out+br*640,1280,640,4096,1280);
  }
}
```

```cpp
#include <hip/hip_runtime.h>
#include <hip/hip_bf16.h>

typedef __bf16 bf16x8 __attribute__((ext_vector_type(8)));
typedef __bf16 bf16x4 __attribute__((ext_vector_type(4)));
typedef __bf16 bf16x2 __attribute__((ext_vector_type(2)));
typedef float f32x4 __attribute__((ext_vector_type(4)));
typedef float f32x2 __attribute__((ext_vector_type(2)));
typedef unsigned u32x4 __attribute__((ext_vector_type(4)));
typedef unsigned short u16;
#define LDS3 __attribute__((address_space(3)))
#define DI __device__ __forceinline__
typedef __amdgpu_buffer_rsrc_t rsrc_t;

constexpr int SQ = 160, DM = 640, NH = 32, SK = 512, DH = 128;
constexpr int LDS_BYTES = 163840;
constexpr int SLOT = 32768;
constexpr int XOFF = 65536, XBUF = 20480, WOFF = XOFF + 2 * XBUF, WBUF = 16384, DUMMY = WOFF + 3 * WBUF;
constexpr float QSCALE = 0.08838834764831845f * 1.4426950408889634f;
constexpr float PLIM = 1024.f;

DI unsigned cvtpk(float lo, float hi) { f32x2 v = {lo, hi}; bf16x2 b = __builtin_convertvector(v, bf16x2); return __builtin_bit_cast(unsigned, b); }
DI f32x4 mfma(bf16x8 a, bf16x8 b, f32x4 c) { return __builtin_amdgcn_mfma_f32_16x16x32_bf16(a, b, c, 0, 0, 0); }
DI bf16x8 ld128(unsigned addr) { return *(const LDS3 bf16x8*)(size_t)addr; }
DI bf16x4 ldtr(unsigned addr) { return __builtin_amdgcn_ds_read_tr16_b64_v4bf16((LDS3 bf16x4*)(size_t)addr); }
DI bf16x8 cat(bf16x4 a, bf16x4 b) { return __builtin_shufflevector(a, b, 0, 1, 2, 3, 4, 5, 6, 7); }
DI void dma16(unsigned ldsaddr, unsigned voff, rsrc_t r, unsigned soff) {
  asm volatile("s_mov_b32 m0, %0\n\ts_nop 0\n\tbuffer_load_dwordx4 %1, %2, %3 offen lds" :: "s"(ldsaddr), "v"(voff), "s"(r), "s"(soff) : "memory");
}
DI void dma16nt(unsigned ldsaddr, unsigned voff, rsrc_t r, unsigned soff) {
  asm volatile("s_mov_b32 m0, %0\n\ts_nop 0\n\tbuffer_load_dwordx4 %1, %2, %3 offen nt lds" :: "s"(ldsaddr), "v"(voff), "s"(r), "s"(soff) : "memory");
}
DI void dma4(unsigned ldsaddr, unsigned voff, rsrc_t r, unsigned soff) {
  asm volatile("s_mov_b32 m0, %0\n\ts_nop 0\n\tbuffer_load_dword %1, %2, %3 offen lds" :: "s"(ldsaddr), "v"(voff), "s"(r), "s"(soff) : "memory");
}
template <int N> DI void wait_vm_bar() { asm volatile("s_waitcnt vmcnt(%0) lgkmcnt(0)\n\ts_barrier" :: "i"(N) : "memory"); }
DI void bar() { asm volatile("s_waitcnt lgkmcnt(0)\n\ts_barrier" ::: "memory"); }
DI float xmax16(float v) { auto r = __builtin_amdgcn_permlane16_swap(__float_as_uint(v), __float_as_uint(v), false, false); return fmaxf(__uint_as_float(r[0]), __uint_as_float(r[1])); }
DI float xmax32(float v) { auto r = __builtin_amdgcn_permlane32_swap(__float_as_uint(v), __float_as_uint(v), false, false); return fmaxf(__uint_as_float(r[0]), __uint_as_float(r[1])); }
DI float xsum16(float v) { auto r = __builtin_amdgcn_permlane16_swap(__float_as_uint(v), __float_as_uint(v), false, false); return __uint_as_float(r[0]) + __uint_as_float(r[1]); }
DI float xsum32(float v) { auto r = __builtin_amdgcn_permlane32_swap(__float_as_uint(v), __float_as_uint(v), false, false); return __uint_as_float(r[0]) + __uint_as_float(r[1]); }

struct Ctx {
  rsrc_t rx, rw, rk, rv;
  unsigned lds0;
  int j, hs, blk0;
  int b, pr, rot;
  u16* of;
  int h;
};

template <int NB> DI void body(const Ctx& c) {
  const int lane = threadIdx.x & 63, l15 = lane & 15, g = lane >> 4;
  const unsigned lds0 = c.lds0;
  unsigned voffA, voffB;
  if constexpr (NB == 3) {
    voffA = (lane >> 3) * 1280 + (((lane & 7) ^ (4 * (c.j & 1) + (lane >> 4))) * 16);
    const int f = (2 * (c.j & 1) + (lane >> 5)) | ((c.j >> 1) << 2);
    voffB = (lane >> 5) * 8192 + (((lane & 31) ^ (f << 1)) * 16);
  } else {
    voffA = (lane >> 4) * 256 + (((lane & 15) ^ (4 * c.j + (lane >> 4))) * 16);
    voffB = ((lane >> 1) & 7) * 256 + (lane >> 4) * 32 + (lane & 1) * 16;
  }
  auto dma_x = [&](int ch) {
#pragma unroll
    for (int n = 0; n < 5; ++n) { const int i = c.j + 4 * n; dma16(lds0 + XOFF + (ch & 1) * XBUF + i * 1024, voffA, c.rx, i * (8 * 1280) + ch * 128); }
  };
  auto dma_w = [&](int s, int buf) {
#pragma unroll
    for (int n = 0; n < 4; ++n) { const int i = (c.j & 1) + 4 * (c.j >> 1) + 2 * (n & 1) + 8 * (n >> 1); dma16(lds0 + WOFF + buf * WBUF + i * 1024, voffB, c.rw, (s * 32 + 2 * i) * 8192); }
  };
  auto dma_kv = [&](int t, int slot) {
    const int tt = (t + c.rot) & 15;
#pragma unroll
    for (int hh = 0; hh < 2; ++hh)
#pragma unroll
      for (int n = 0; n < 2; ++n) { const int i = c.j + 4 * n; dma16nt(lds0 + slot * SLOT + hh * 8192 + i * 1024, voffA, c.rk, hh * 131072 + tt * 8192 + i * 1024); }
#pragma unroll
    for (int hh = 0; hh < 2; ++hh)
#pragma unroll
      for (int n = 0; n < 2; ++n) { const int i = c.j + 4 * n;
        dma16nt(lds0 + slot * SLOT + 16384 + hh * 8192 + i * 1024, voffB, c.rv, hh * 131072 + tt * 8192 + (i >> 1) * 2048 + (i & 1) * 128); }
  };
  if constexpr (NB == 3) { dma_x(0); dma_w(0, 0); dma_w(1, 1); }
  else {
    {
      const int idx = (c.pr * 4 + c.j) * 50 + lane;
      const unsigned o = lane < 50 ? (unsigned)((idx / 5) * 1280 + (idx % 5) * 128) : 0x7fffff00u;
      const int idw = (c.b * 4 + c.j) * 40 + lane;
      const unsigned ow = lane < 40 ? (unsigned)((idw >> 2) * 8192 + (idw & 3) * 128) : 0x7fffff00u;
      dma4(lds0 + DUMMY, ow, c.rw, 0); dma4(lds0 + DUMMY, o, c.rx, 0);
      dma4(lds0 + DUMMY, ow, c.rw, 320 * 8192); dma4(lds0 + DUMMY, o, c.rx, 640);
    }
    dma_kv(0, 0); dma_kv(1, 1);
  }
  f32x4 acc[NB][8];
#pragma unroll
  for (int nb = 0; nb < NB; ++nb)
#pragma unroll
    for (int db = 0; db < 8; ++db) acc[nb][db] = f32x4{0.f, 0.f, 0.f, 0.f};
  {
    unsigned wa[8];
    const int qq = l15 >> 2, p = lane & 3, f = qq | ((g & 1) << 2);
#pragma unroll
    for (int db = 0; db < 8; ++db) wa[db] = lds0 + WOFF + (8 * g + qq) * 512 + c.hs * 256 + ((db ^ f) * 32) + p * 8;
    unsigned xa[2];
#pragma unroll
    for (int kk = 0; kk < 2; ++kk) xa[kk] = lds0 + XOFF + (c.blk0 * 16 + l15) * 128 + (((4 * kk + g) ^ ((l15 >> 1) & 7)) * 16);
#pragma unroll
    for (int s = 0; s < 20; ++s) {
      if constexpr (NB == 3) { if (s == 19) wait_vm_bar<0>(); else if (s & 1) wait_vm_bar<9>(); else wait_vm_bar<4>(); }
      else bar();
      if constexpr (NB == 3) {
        if (!(s & 1) && (s >> 1) + 1 < 10) dma_x((s >> 1) + 1);
        if (s + 2 < 20) dma_w(s + 2, (s + 2) % 3);
      }
      bf16x8 xf[NB];
#pragma unroll
      for (int nb = 0; nb < NB; ++nb) xf[nb] = ld128(xa[s & 1] + ((s >> 1) & 1) * XBUF + nb * 2048);
#pragma unroll
      for (int db = 0; db < 8; ++db) {
        const bf16x8 wf = cat(ldtr(wa[db] + (s % 3) * WBUF), ldtr(wa[db] + (s % 3) * WBUF + 2048));
#pragma unroll
        for (int nb = 0; nb < NB; ++nb) acc[nb][db] = mfma(wf, xf[nb], acc[nb][db]);
      }
    }
  }
  bf16x8 qf[NB][4];
#pragma unroll
  for (int nb = 0; nb < NB; ++nb)
#pragma unroll
    for (int e = 0; e < 4; ++e) {
      const f32x4 a0 = acc[nb][2 * e] * QSCALE, a1 = acc[nb][2 * e + 1] * QSCALE;
      auto r0 = __builtin_amdgcn_permlane16_swap(cvtpk(a0[0], a0[1]), cvtpk(a1[0], a1[1]), false, false);
      auto r1 = __builtin_amdgcn_permlane16_swap(cvtpk(a0[2], a0[3]), cvtpk(a1[2], a1[3]), false, false);
      u32x4 w = {r0[0], r1[0], r0[1], r1[1]};
      qf[nb][e] = __builtin_bit_cast(bf16x8, w);
    }
  f32x4 o[NB][8], negm[NB];
  float lsum[NB];
#pragma unroll
  for (int nb = 0; nb < NB; ++nb) {
    negm[nb] = f32x4{0.f, 0.f, 0.f, 0.f}; lsum[nb] = 0.f;
#pragma unroll
    for (int db = 0; db < 8; ++db) o[nb][db] = f32x4{0.f, 0.f, 0.f, 0.f};
  }
  unsigned ka[4];
  {
    const int pg = ((g & 1) << 1) | (g >> 1);
#pragma unroll
    for (int s = 0; s < 4; ++s) ka[s] = lds0 + c.hs * 8192 + l15 * 256 + (((4 * s + pg) ^ l15) * 16);
  }
  const unsigned va = lds0 + 16384 + c.hs * 8192 + (g >> 1) * 2048 + (4 * (g & 1) + (l15 >> 2)) * 32 + (lane & 3) * 8;
  int slot = 0;
#pragma unroll 1
  for (int t = 0; t < 16; ++t) {
    if constexpr (NB == 2) {
      if (t == 0) wait_vm_bar<8>(); else if (t <= 12) wait_vm_bar<24>(); else if (t == 13) wait_vm_bar<16>(); else if (t == 14) wait_vm_bar<8>(); else wait_vm_bar<0>();
      if (t == 0) { dma_kv(2, 2); dma_kv(3, 3); dma_kv(4, 4); }
      else if (t + 4 < 16) dma_kv(t + 4, slot == 0 ? 4 : slot - 1);
    } else bar();
    const unsigned so = slot * SLOT;
    f32x4 S[NB][2];
    auto qk = [&]() {
#pragma unroll
      for (int kt = 0; kt < 2; ++kt)
#pragma unroll
        for (int s = 0; s < 4; ++s) {
          const bf16x8 kf = ld128(ka[s] + so + kt * 4096);
#pragma unroll
          for (int nb = 0; nb < NB; ++nb) S[nb][kt] = mfma(kf, qf[nb][s], s == 0 ? negm[nb] : S[nb][kt]);
        }
    };
    auto fix = [&](bool first) {
#pragma unroll
      for (int nb = 0; nb < NB; ++nb) {
        float mx = fmaxf(fmaxf(fmaxf(S[nb][0][0], S[nb][0][1]), fmaxf(S[nb][0][2], S[nb][0][3])), fmaxf(fmaxf(S[nb][1][0], S[nb][1][1]), fmaxf(S[nb][1][2], S[nb][1][3])));
        mx = xmax32(xmax16(mx));
        const float d = first ? mx : fmaxf(mx, 0.f);
        negm[nb] -= d; S[nb][0] -= d; S[nb][1] -= d;
        if (!first) {
          const float f = __builtin_amdgcn_exp2f(-d);
          lsum[nb] *= f;
#pragma unroll
          for (int db = 0; db < 8; ++db) o[nb][db] *= f;
        }
      }
    };
    float ts[NB];
    auto ex = [&]() {
#pragma unroll
      for (int nb = 0; nb < NB; ++nb) {
#pragma unroll
        for (int kt = 0; kt < 2; ++kt)
#pragma unroll
          for (int r = 0; r < 4; ++r) S[nb][kt][r] = __builtin_amdgcn_exp2f(S[nb][kt][r]);
        ts[nb] = ((S[nb][0][0] + S[nb][0][1]) + (S[nb][0][2] + S[nb][0][3])) + ((S[nb][1][0] + S[nb][1][1]) + (S[nb][1][2] + S[nb][1][3]));
      }
    };
    qk();
    if (t == 0) fix(true);
    ex();
    float tm = ts[0];
#pragma unroll
    for (int nb = 1; nb < NB; ++nb) tm = fmaxf(tm, ts[nb]);
    if (__builtin_expect(__any(!(tm <= PLIM)), 0)) { qk(); fix(false); ex(); }
    bf16x8 pf[NB];
#pragma unroll
    for (int nb = 0; nb < NB; ++nb) {
      lsum[nb] += ts[nb];
      u32x4 w = {cvtpk(S[nb][0][0], S[nb][0][1]), cvtpk(S[nb][0][2], S[nb][0][3]), cvtpk(S[nb][1][0], S[nb][1][1]), cvtpk(S[nb][1][2], S[nb][1][3])};
      pf[nb] = __builtin_bit_cast(bf16x8, w);
    }
#pragma unroll
    for (int db = 0; db < 8; ++db) {
      const unsigned a = va + so + (db >> 2) * 1024 + (db & 3) * 256;
      const bf16x8 vf = cat(ldtr(a), ldtr(a + 4096));
#pragma unroll
      for (int nb = 0; nb < NB; ++nb) o[nb][db] = mfma(vf, pf[nb], o[nb][db]);
    }
    slot = slot == 4 ? 0 : slot + 1;
  }
#pragma unroll
  for (int nb = 0; nb < NB; ++nb) {
    const float inv = 1.f / xsum32(xsum16(lsum[nb]));
    u16* dst = c.of + ((size_t)((c.blk0 + nb) * 128 + c.h * 4) * 64 + lane) * 8;
#pragma unroll
    for (int e = 0; e < 4; ++e) {
      const f32x4 a0 = o[nb][2 * e] * inv, a1 = o[nb][2 * e + 1] * inv;
      u32x4 w = {cvtpk(a0[0], a0[1]), cvtpk(a0[2], a0[3]), cvtpk(a1[0], a1[1]), cvtpk(a1[2], a1[3])};
      *(u32x4*)(dst + e * 512) = w;
    }
  }
}

__global__ __launch_bounds__(512, 2) void k1(const u16* xA, const u16* kA, const u16* vA, const u16* wqA,
                                              const u16* xB, const u16* kB, const u16* vB, const u16* wqB, u16* ofrag) {
  extern __shared__ __attribute__((aligned(16))) char smem[];
  const int id = blockIdx.x, xg = id & 7, cc = id >> 3, br = xg >> 2, hg = xg & 3, b = cc >> 2, pr = cc & 3, h0 = hg * 8 + pr * 2;
  const int w = __builtin_amdgcn_readfirstlane(threadIdx.x >> 6);
  asm volatile("s_nop 4");
  Ctx c;
  const u16* x = br ? xB : xA; const u16* k = br ? kB : kA; const u16* v = br ? vB : vA; const u16* wq = br ? wqB : wqA;
  c.rx = __builtin_amdgcn_make_buffer_rsrc((void*)(x + (size_t)b * SQ * DM), (short)0, SQ * DM * 2, 0x00020000);
  c.rw = __builtin_amdgcn_make_buffer_rsrc((void*)(wq + h0 * DH), (short)0, DM * NH * DH * 2 - h0 * DH * 2, 0x00020000);
  c.rk = __builtin_amdgcn_make_buffer_rsrc((void*)(k + (size_t)(b * NH + h0) * SK * DH), (short)0, 2 * SK * DH * 2, 0x00020000);
  c.rv = __builtin_amdgcn_make_buffer_rsrc((void*)(v + (size_t)(b * NH + h0) * SK * DH), (short)0, 2 * SK * DH * 2, 0x00020000);
  c.lds0 = (unsigned)(size_t)(LDS3 char*)smem;
  c.hs = w >> 2; c.j = w & 3; c.b = b; c.pr = pr; c.rot = (id * 5) & 15;
  c.h = h0 + c.hs;
  c.of = ofrag + (size_t)(br * 80 + b * 10) * 128 * 512;
  const bool big = (c.j < 2) == (c.hs == 0);
  c.blk0 = c.hs == 0 ? (c.j < 2 ? 3 * c.j : 2 + 2 * c.j) : (c.j < 2 ? 2 * c.j : 3 * c.j - 2);
  if (big) body<3>(c); else body<2>(c);
}

__global__ void k2_naive(const u16* of, const u16* woA, const u16* woB, u16* out) {
  const int br = blockIdx.z; const u16* wo = br ? woB : woA;
  __shared__ float As[16][33], Bs[32][17];
  const int tx = threadIdx.x, ty = threadIdx.y, rb = blockIdx.y, col = blockIdx.x * 16 + tx; float acc = 0;
  for (int ks = 0; ks < 128; ++ks) {
    for (int i = ty * 16 + tx; i < 512; i += 256) { int lane = i >> 3, j = i & 7, m = lane & 15, g = lane >> 4; int k = j < 4 ? 4 * g + j : 16 + 4 * g + j - 4;
      As[m][k] = __uint_as_float((unsigned)of[((size_t)(br * 80 + rb) * 128 + ks) * 512 + i] << 16); }
    for (int i = ty; i < 32; i += 16) Bs[i][tx] = __uint_as_float((unsigned)wo[(size_t)(ks * 32 + i) * 640 + col] << 16);
    __syncthreads();
    for (int k = 0; k < 32; ++k) acc += As[ty][k] * Bs[k][tx];
    __syncthreads();
  }
  __hip_bfloat16 r = __float2bfloat16(acc);
  out[(size_t)(rb * 16 + ty) * 1280 + br * 640 + col] = *(u16*)&r;
}

extern "C" void kernel_launch(void* const* d_in, const int* in_sizes, int n_in, void* d_out, int out_size, void* d_ws, size_t ws_size, hipStream_t stream) {
  hipFuncSetAttribute((const void*)k1, hipFuncAttributeMaxDynamicSharedMemorySize, LDS_BYTES);
  u16* of = (u16*)d_ws;
  k1<<<256, 512, LDS_BYTES, stream>>>((const u16*)d_in[0], (const u16*)d_in[1], (const u16*)d_in[2], (const u16*)d_in[3],
                                      (const u16*)d_in[5], (const u16*)d_in[6], (const u16*)d_in[7], (const u16*)d_in[8], of);
  k2_naive<<<dim3(40, 80, 2), dim3(16, 16), 0, stream>>>(of, (const u16*)d_in[4], (const u16*)d_in[9], (u16*)d_out);
}
```

```cpp
#include <hip/hip_runtime.h>
#include <hip/hip_bf16.h>

typedef __bf16 bf16x8 __attribute__((ext_vector_type(8)));
typedef __bf16 bf16x4 __attribute__((ext_vector_type(4)));
typedef __bf16 bf16x2 __attribute__((ext_vector_type(2)));
typedef float f32x4 __attribute__((ext_vector_type(4)));
typedef float f32x2 __attribute__((ext_vector_type(2)));
typedef unsigned u32x4 __attribute__((ext_vector_type(4)));
typedef unsigned short u16;
#define LDS3 __attribute__((address_space(3)))
#define DI __device__ __forceinline__
#define SB() __builtin_amdgcn_sched_barrier(0)
typedef __amdgpu_buffer_rsrc_t rsrc_t;

constexpr int SQ = 160, DM = 640, NH = 32, SK = 512, DH = 128;
constexpr int LDS_BYTES = 163840;
constexpr int KV_HS = 32768, V_OFF = 65536;
__host__ __device__ constexpr int xoff(int b) { return b * 65536 + 73728; }
__host__ __device__ constexpr int woff(int b) { return b == 0 ? 16384 : b == 1 ? 49152 : b == 2 ? 106496 : 122880; }
constexpr int DUMMY = 159744;
constexpr float QSCALE = 0.08838834764831845f * 1.4426950408889634f;
constexpr float BIAS = 10.f;
constexpr unsigned OOB = 0x7ff00000u;

DI unsigned cvtpk(float lo, float hi) { f32x2 v = {lo, hi}; bf16x2 b = __builtin_convertvector(v, bf16x2); return __builtin_bit_cast(unsigned, b); }
DI f32x4 mfma(bf16x8 a, bf16x8 b, f32x4 c) { return __builtin_amdgcn_mfma_f32_16x16x32_bf16(a, b, c, 0, 0, 0); }
DI bf16x8 ld128(unsigned addr) { return *(const LDS3 bf16x8*)(size_t)addr; }
DI bf16x4 ldtr(unsigned addr) { return __builtin_amdgcn_ds_read_tr16_b64_v4bf16((LDS3 bf16x4*)(size_t)addr); }
DI bf16x8 cat(bf16x4 a, bf16x4 b) { return __builtin_shufflevector(a, b, 0, 1, 2, 3, 4, 5, 6, 7); }
DI bf16x8 asb(u32x4 w) { return __builtin_bit_cast(bf16x8, w); }
template <bool NT> DI void dma16(unsigned ldsaddr, unsigned voff, rsrc_t r, unsigned soff) {
  if constexpr (NT) asm volatile("s_mov_b32 m0, %0\n\ts_nop 0\n\tbuffer_load_dwordx4 %1, %2, %3 offen nt lds" :: "s"(ldsaddr), "v"(voff), "s"(r), "s"(soff) : "memory");
  else              asm volatile("s_mov_b32 m0, %0\n\ts_nop 0\n\tbuffer_load_dwordx4 %1, %2, %3 offen lds" :: "s"(ldsaddr), "v"(voff), "s"(r), "s"(soff) : "memory");
}
DI void touch(unsigned ldsaddr, unsigned voff, rsrc_t r, unsigned soff) {
  asm volatile("s_mov_b32 m0, %0\n\ts_nop 0\n\tbuffer_load_dword %1, %2, %3 offen nt lds" :: "s"(ldsaddr), "v"(voff), "s"(r), "s"(soff) : "memory");
}
template <int N> DI void wait_vm_bar() { asm volatile("s_waitcnt vmcnt(%0) lgkmcnt(0)\n\ts_barrier" :: "i"(N) : "memory"); }
DI float xmax16(float v) { auto r = __builtin_amdgcn_permlane16_swap(__float_as_uint(v), __float_as_uint(v), false, false); return fmaxf(__uint_as_float(r[0]), __uint_as_float(r[1])); }
DI float xmax32(float v) { auto r = __builtin_amdgcn_permlane32_swap(__float_as_uint(v), __float_as_uint(v), false, false); return fmaxf(__uint_as_float(r[0]), __uint_as_float(r[1])); }
DI void amfma(f32x4& acc, bf16x8 a, bf16x8 b) { asm volatile("v_mfma_f32_16x16x32_bf16 %0, %1, %2, %0" : "+v"(acc) : "v"(a), "v"(b)); }
DI void amfma_c(f32x4& d, bf16x8 a, bf16x8 b, f32x4 cc) { asm volatile("v_mfma_f32_16x16x32_bf16 %0, %1, %2, %3" : "=&v"(d) : "v"(a), "v"(b), "v"(cc)); }
DI float aexp(float x) { asm volatile("v_exp_f32 %0, %0" : "+v"(x)); return x; }
DI unsigned aor3(unsigned a, unsigned b, unsigned c) { unsigned r; asm volatile("v_or3_b32 %0, %1, %2, %3" : "=v"(r) : "v"(a), "v"(b), "v"(c)); return r; }
DI void use(bf16x8 f) { asm volatile("" :: "v"(f)); }
DI void aaddu(unsigned& a, int d) { asm volatile("v_add_u32 %0, %1, %0" : "+v"(a) : "s"(d)); }
DI unsigned acvt(float lo, float hi) { unsigned r; asm volatile("v_cvt_pk_bf16_f32 %0, %1, %2" : "=v"(r) : "v"(lo), "v"(hi)); return r; }

#define STAMP(i)
#define XNT false
#define WNT true
#define NTOUCH 0
#define KVN0 1
#define KROT 1
#define PRIOQ 2
#define PRIOA 2

struct Ctx {
  rsrc_t rx, rw, rk, rv;
  unsigned lds0;
  int j, hs, blk0, rot, krot;
  u16* of;
  unsigned long long* dbg;
};

template <int NB> struct St {
  f32x4 o[NB][8], negm[NB], S0[NB], S1[NB], l[NB];
  bf16x8 qf[NB][4], ones;
  u32x4 pf[NB];
  unsigned orv;
};
template <int NB, int E> DI void exp1(f32x4 (&S)[NB]) { if constexpr (E >= 0 && E < 4 * NB) S[E % NB][E / NB] = aexp(S[E % NB][E / NB]); }
template <int NB, int LO, int HI> DI void exps(f32x4 (&S)[NB]) { if constexpr (LO < HI) { exp1<NB, LO>(S); exps<NB, LO + 1, HI>(S); } }
DI float max8(f32x4 a, f32x4 b) { return fmaxf(fmaxf(fmaxf(a[0], a[1]), fmaxf(a[2], a[3])), fmaxf(fmaxf(b[0], b[1]), fmaxf(b[2], b[3]))); }

template <int NB, bool HAS_NEXT> DI void refix(St<NB>& s, rsrc_t rk, int ksoff) {
  unsigned ln; asm volatile("v_mbcnt_lo_u32_b32 %0, -1, 0\n\tv_mbcnt_hi_u32_b32 %0, -1, %0" : "=v"(ln));
  const int kvo = (ln & 15) * 256 + ((((ln >> 4) & 1) << 1) | (ln >> 5)) * 16;
  asm volatile("s_nop 7");
#pragma unroll
  for (int nb = 0; nb < NB; ++nb) {
    f32x4 a = s.negm[nb], b = s.negm[nb];
#pragma unroll
    for (int st = 0; st < 4; ++st) {
      a = mfma(asb(__builtin_amdgcn_raw_buffer_load_b128(rk, kvo + st * 64, ksoff, 0)), s.qf[nb][st], a);
      SB();
      b = mfma(asb(__builtin_amdgcn_raw_buffer_load_b128(rk, kvo + st * 64, ksoff + 4096, 0)), s.qf[nb][st], b);
      SB();
    }
    const float d = fmaxf(xmax32(xmax16(max8(a, b))) + BIAS, 0.f);
    const float f = __builtin_amdgcn_exp2f(-d);
    s.negm[nb] -= d; if (HAS_NEXT) s.S0[nb] -= d;
    s.l[nb] *= f;
#pragma unroll
    for (int db = 0; db < 8; ++db) s.o[nb][db] *= f;
#pragma unroll
    for (int r = 0; r < 4; ++r) { a[r] = __builtin_amdgcn_exp2f(a[r] - d); b[r] = __builtin_amdgcn_exp2f(b[r] - d); }
    u32x4 w = {cvtpk(a[0], a[1]), cvtpk(a[2], a[3]), cvtpk(b[0], b[1]), cvtpk(b[2], b[3])};
    s.pf[nb] = w;
    SB();
  }
  asm volatile("s_nop 1");
}
template <int NB, bool HAS_NEXT> DI void check(St<NB>& s, rsrc_t rk, int ksoff) {
  if (__builtin_expect(__any((s.orv & 0x40004000u) != 0u), 0)) refix<NB, HAS_NEXT>(s, rk, ksoff);
}

template <int NB> DI void body(const Ctx& c) {
  const int lane = threadIdx.x & 63, l15 = lane & 15, g = lane >> 4;
  const unsigned lds0 = c.lds0;
  const unsigned voffK = (lane >> 4) * 256 + (((lane & 15) ^ (4 * c.j + (lane >> 4))) * 16);
  const unsigned voffV = ((lane >> 1) & 7) * 256 + (lane >> 4) * 32 + (lane & 1) * 16;
  auto dma_k1 = [&](int t, int q) {
    const int hh = q >> 1, i = c.j + 4 * (q & 1);
    dma16<true>(lds0 + hh * KV_HS + (t & 3) * 8192 + i * 1024, voffK, c.rk, t < 16 ? hh * 131072 + ((t + c.rot) & 15) * 8192 + i * 1024 : OOB);
  };
  auto dma_v1 = [&](int t, int q) {
    const int hh = q >> 1, i = c.j + 4 * (q & 1);
    dma16<true>(lds0 + V_OFF + hh * KV_HS + (t & 3) * 8192 + i * 1024, voffV, c.rv, t < 16 ? hh * 131072 + ((t + c.rot) & 15) * 8192 + (i >> 1) * 2048 + (i & 1) * 128 : OOB);
  };
  STAMP(0);
  St<NB> s;
  auto N0 = [] {};
  {
    unsigned voffS;
    if constexpr (NB == 3) { const int f = (2 * (c.j & 1) + (lane >> 5)) | ((c.j >> 1) << 2); voffS = (lane >> 5) * 8192 + (((lane & 31) ^ (f << 1)) * 16); }
    else voffS = (lane >> 3) * 1280 + (((lane & 7) ^ (4 * (c.j & 1) + g)) * 16);
    const unsigned voffT = lane * 128;
    auto dma_w = [&](int st, int buf, int n) {
      int ks = st + 2 * c.krot; ks = ks >= 20 ? ks - 20 : ks;
      const int i = (c.j & 1) + 4 * (c.j >> 1) + 2 * (n & 1) + 8 * (n >> 1);
      dma16<WNT>(lds0 + woff(buf) + i * 1024, voffS, c.rw, st < 20 ? (ks * 32 + 2 * i) * 8192 : OOB);
    };
    auto dma_x = [&](int ch, int n) {
      int kc = ch + c.krot; kc = kc >= 10 ? kc - 10 : kc;
      const int i = c.j + 4 * n;
      dma16<XNT>(lds0 + xoff(ch & 1) + i * 1024, voffS, c.rx, ch < 10 ? i * (8 * 1280) + kc * 128 : OOB);
    };
    auto extra = [&](int n, int q) {
      const int m = n - KVN0;
      if (m >= 0 && m < 2) dma_k1(m, q);
      else if (m == 2) dma_v1(0, q);
      else {
        const int tau = (m - 3) * 2 + (c.j & 1), u = tau >> 1, tile = 2 + (u >> 1) - (u & 1);
        const unsigned so = (q == 0 && c.j >= 2 && m >= 3 && tau < NTOUCH) ? (tau & 1) * 131072 + ((tile + c.rot) & 15) * 8192 : OOB;
        if (u & 1) touch(lds0 + DUMMY, voffT, c.rv, so); else touch(lds0 + DUMMY, voffT, c.rk, so);
      }
    };
    if constexpr (NB == 3) {
#pragma unroll
      for (int st = 0; st < 3; ++st)
#pragma unroll
        for (int n = 0; n < 4; ++n) dma_w(st, st, n);
    } else {
#pragma unroll
      for (int n = 0; n < 5; ++n) dma_x(0, n);
#pragma unroll
      for (int q = 0; q < 4; ++q) touch(lds0 + DUMMY, voffT, c.rk, OOB);
    }
    if constexpr (PRIOQ == NB) __builtin_amdgcn_s_setprio(1);
#pragma unroll
    for (int nb = 0; nb < NB; ++nb)
#pragma unroll
      for (int db = 0; db < 8; ++db) s.o[nb][db] = f32x4{0.f, 0.f, 0.f, 0.f};
    unsigned wa[2][8], xa[4];
    const int qq = l15 >> 2, p = lane & 3, f = qq | ((g & 1) << 2);
#pragma unroll
    for (int db = 0; db < 8; ++db) { wa[0][db] = lds0 + woff(0) + (8 * g + qq) * 512 + c.hs * 256 + ((db ^ f) * 32) + p * 8; wa[1][db] = wa[0][db] + (woff(2) - woff(0)); asm volatile("" : "+v"(wa[1][db])); }
#pragma unroll
    for (int u = 0; u < 4; ++u) { xa[u] = lds0 + xoff(u >> 1) + (c.blk0 * 16 + l15) * 128 + (((4 * (u & 1) + g) ^ ((l15 >> 1) & 7)) * 16); asm volatile("" : "+v"(xa[u])); }
    const bf16x8 zf = {};
    bf16x8 w4 = zf, w5 = zf, w6 = zf, w7 = zf, xp[NB];
#pragma unroll
    for (int nb = 0; nb < NB; ++nb) xp[nb] = zf;
    STAMP(1); STAMP(2); STAMP(3);
    SB();
#pragma unroll 1
    for (int s4 = 0; s4 < 20; s4 += 4) {
#pragma unroll
      for (int u = 0; u < 4; ++u) {
        const int st = s4 + u;
        if constexpr (NB == 3) wait_vm_bar<8>();
        else { if (u & 1) wait_vm_bar<63>(); else wait_vm_bar<4>(); }
        auto rw = [&](int db) { const unsigned a = wa[u >> 1][db] + (woff(u) - woff(u & 2)); return cat(ldtr(a), ldtr(a + 2048)); };
        auto rx = [&](int nb) { return ld128(xa[u] + nb * 2048); };
        auto D = [&](int n) { return [&, n] {
          if constexpr (NB == 3) { if (n < 4) dma_w(st + 3, (u + 3) & 3, n); }
          else { if (u & 1) { if (n < 4) extra(st >> 1, n); } else dma_x((st >> 1) + 1, n); } }; };
        auto mm = [&](int db, bf16x8 wf, bf16x8 (&xf)[NB], auto g0, auto g1, auto g2) {
          amfma(s.o[0][db], wf, xf[0]); SB(); g0(); SB();
          amfma(s.o[1][db], wf, xf[1]); SB(); g1(); SB();
          if constexpr (NB == 3) { amfma(s.o[2][db], wf, xf[2]); SB(); }
          g2(); SB();
        };
        bf16x8 xf[NB], w0, w1, w2, w3;
        if constexpr (NB == 3) {
          mm(4, w4, xp, [&] { w0 = rw(0); }, [&] { xf[0] = rx(0); xf[1] = rx(1); }, [&] { xf[2] = rx(2); });
          mm(5, w5, xp, [&] { w1 = rw(1); }, D(0), N0);
          mm(6, w6, xp, [&] { w2 = rw(2); }, [&] { w3 = rw(3); }, N0);
          mm(7, w7, xp, [&] { w4 = rw(4); }, D(1), [&] { use(xf[NB - 1]); });
          mm(0, w0, xf, [&] { w5 = rw(5); }, [&] { use(w1); w6 = rw(6); }, N0);
          mm(1, w1, xf, [&] { w7 = rw(7); }, [&] { use(w2); D(2)(); }, N0);
          mm(2, w2, xf, [&] { use(w3); }, D(3), N0);
          mm(3, w3, xf, [&] { use(w7); }, N0, N0);
        } else {
          mm(4, w4, xp, [&] { w0 = rw(0); }, [&] { xf[0] = rx(0); xf[1] = rx(1); }, N0);
          mm(5, w5, xp, [&] { w1 = rw(1); }, D(0), N0);
          mm(6, w6, xp, [&] { w2 = rw(2); }, [&] { w3 = rw(3); D(1)(); }, N0);
          mm(7, w7, xp, [&] { w4 = rw(4); }, [&] { D(2)(); use(xf[NB - 1]); }, N0);
          mm(0, w0, xf, [&] { w5 = rw(5); }, [&] { use(w1); w6 = rw(6); D(3)(); }, N0);
          mm(1, w1, xf, [&] { w7 = rw(7); }, [&] { use(w2); D(4)(); }, N0);
          mm(2, w2, xf, [&] { use(w3); }, N0, N0);
          mm(3, w3, xf, [&] { use(w7); }, N0, N0);
        }
#pragma unroll
        for (int nb = 0; nb < NB; ++nb) xp[nb] = xf[nb];
      }
    }
#pragma unroll
    for (int nb = 0; nb < NB; ++nb) { amfma(s.o[nb][4], w4, xp[nb]); amfma(s.o[nb][5], w5, xp[nb]); amfma(s.o[nb][6], w6, xp[nb]); amfma(s.o[nb][7], w7, xp[nb]); }
    asm volatile("s_nop 7");
  }
  STAMP(4);
  wait_vm_bar<0>();
#pragma unroll
  for (int q = 0; q < 4; ++q) { if constexpr (NB == 3) dma_k1(2, q); else dma_v1(1, q); }
  STAMP(5);
  if constexpr (PRIOQ != PRIOA) { if constexpr (PRIOA == NB) __builtin_amdgcn_s_setprio(1); else __builtin_amdgcn_s_setprio(0); }
  const int pg = ((g & 1) << 1) | (g >> 1);
  unsigned ka[4];
#pragma unroll
  for (int st = 0; st < 4; ++st) ka[st] = lds0 + c.hs * KV_HS + l15 * 256 + (((4 * st + pg) ^ l15) * 16);
  unsigned va = lds0 + V_OFF + c.hs * KV_HS + (g >> 1) * 2048 + (4 * (g & 1) + (l15 >> 2)) * 32 + (lane & 3) * 8 - 8192;
  auto rk = [&](int kt, int st) { return ld128(ka[st] + kt * 4096); };
  auto rv = [&](int db) { const unsigned a = va + (db >> 2) * 1024 + (db & 3) * 256; return cat(ldtr(a), ldtr(a + 4096)); };
  auto kv = [&](int t, int n) { if constexpr (NB == 2) { if (n < 4) dma_k1(t + 3, n); else dma_v1(t + 2, n - 4); } };
  {
    bf16x8 kf0[4], kf1[4];
#pragma unroll
    for (int st = 0; st < 4; ++st) { kf0[st] = rk(0, st); kf1[st] = rk(1, st); }
#pragma unroll
    for (int nb = 0; nb < NB; ++nb)
#pragma unroll
      for (int e = 0; e < 4; ++e) {
        const f32x4 a0 = s.o[nb][2 * e] * QSCALE, a1 = s.o[nb][2 * e + 1] * QSCALE;
        auto r0 = __builtin_amdgcn_permlane16_swap(cvtpk(a0[0], a0[1]), cvtpk(a1[0], a1[1]), false, false);
        auto r1 = __builtin_amdgcn_permlane16_swap(cvtpk(a0[2], a0[3]), cvtpk(a1[2], a1[3]), false, false);
        u32x4 w = {r0[0], r1[0], r0[1], r1[1]};
        s.qf[nb][e] = asb(w);
        if (nb < 2) { SB(); kv(0, nb * 4 + e); SB(); }
      }
    const f32x4 z4 = {0.f, 0.f, 0.f, 0.f};
#pragma unroll
    for (int st = 0; st < 4; ++st)
#pragma unroll
      for (int nb = 0; nb < NB; ++nb) { s.S0[nb] = mfma(kf0[st], s.qf[nb][st], st == 0 ? z4 : s.S0[nb]); s.S1[nb] = mfma(kf1[st], s.qf[nb][st], st == 0 ? z4 : s.S1[nb]); }
#pragma unroll
    for (int nb = 0; nb < NB; ++nb) {
      s.l[nb] = z4; s.pf[nb] = u32x4{0u, 0u, 0u, 0u};
#pragma unroll
      for (int db = 0; db < 8; ++db) s.o[nb][db] = z4;
      const float m = xmax32(xmax16(max8(s.S0[nb], s.S1[nb]))) + BIAS;
      s.negm[nb] = z4 - m; s.S0[nb] -= m; s.S1[nb] -= m;
    }
    s.ones = asb(u32x4{0x3f803f80u, 0x3f803f80u, 0x3f803f80u, 0x3f803f80u});
    asm volatile("" : "+v"(s.ones));
  }
  exps<NB, 0, 4 * NB>(s.S0); exps<NB, 0, 2 * NB>(s.S1);
  bf16x8 v5h = {}, v6 = {}, v7 = {};
  SB();
  auto top = [&]() { if constexpr (NB == 2) wait_vm_bar<16>(); else wait_vm_bar<0>(); };
#pragma unroll
  for (int st = 0; st < 4; ++st) ka[st] += 8192;
  va += 8192;
  auto us = [&](auto m, auto g0, auto g1, auto g2) {
    m(0); SB(); g0(); SB(); m(1); SB(); g1(); SB();
    if constexpr (NB == 3) { m(2); SB(); g2(); SB(); }
  };
  auto PV = [&](int db, bf16x8 fr) { return [&s, db, fr](int nb) { amfma(s.o[nb][db], fr, asb(s.pf[nb])); }; };
  auto QK0 = [&](int st, bf16x8 fr) { return [&s, st, fr](int nb) { if (st == 0) amfma_c(s.S0[nb], fr, s.qf[nb][0], s.negm[nb]); else amfma(s.S0[nb], fr, s.qf[nb][st]); }; };
  auto QK1 = [&](int st, bf16x8 fr) { return [&s, st, fr](int nb) { if (st == 0) amfma_c(s.S1[nb], fr, s.qf[nb][0], s.negm[nb]); else amfma(s.S1[nb], fr, s.qf[nb][st]); }; };
  auto SUM = [&](int nb) { amfma(s.l[nb], s.ones, asb(s.pf[nb])); };
  auto N = [] {};
#define E0(e) [&] { exp1<NB, (e)>(s.S0); }
#define E1(e) [&] { exp1<NB, (e)>(s.S1); }
#define CLO(nb) [&] { if constexpr ((nb) < NB) { s.pf[nb][0] = acvt(s.S0[nb][0], s.S0[nb][1]); s.pf[nb][1] = acvt(s.S0[nb][2], s.S0[nb][3]); } }
#define CHI(nb) [&] { if constexpr ((nb) < NB) { s.pf[nb][2] = acvt(s.S1[nb][0], s.S1[nb][1]); s.pf[nb][3] = acvt(s.S1[nb][2], s.S1[nb][3]); } }
#define U(f, ...) [&] { use(f); SB(); (__VA_ARGS__)(); }
  STAMP(6); STAMP(7); STAMP(8);
#pragma unroll 1
  for (int t = 1; t < 16; ++t) {
    const int dk = (t & 3) == 3 ? -24576 : 8192, dv = (t & 3) == 0 ? -24576 : 8192;
    top();
    bf16x8 a0, a1, a2, a3, b0, b1, b2, b3, v0, v1, v2, v3, v4;
    unsigned o1, o2;
    if constexpr (NB == 3) {
      bf16x8 v5;
      us(PV(6, v6), [&] { a0 = rk(0, 0); a1 = rk(0, 1); }, [&] { a2 = rk(0, 2); a3 = rk(0, 3); }, E1(6));
      us(PV(7, v7), CLO(0), CLO(1), U(a0, CLO(2)));
      us(QK0(0, a0), [&] { b0 = rk(1, 0); b1 = rk(1, 1); }, E1(7), U(a1, E1(8)));
      us(QK0(1, a1), E1(9), E1(10), U(a2, E1(11)));
      us(QK0(2, a2), [&] { o1 = aor3(s.pf[0][0], s.pf[0][1], s.pf[1][0]); o2 = aor3(s.pf[1][1], s.pf[2][0], s.pf[2][1]); }, CHI(0), U(a3, CHI(1)));
      us(QK0(3, a3), CHI(2), [&] { o1 = aor3(o1, s.pf[0][2], s.pf[0][3]); o2 = aor3(o2, s.pf[1][2], s.pf[1][3]); },
                     U(b0, [&] { o1 = aor3(o1, s.pf[2][2], s.pf[2][3]); s.orv = o1 | o2; }));
      check<NB, true>(s, c.rk, c.hs * 131072 + ((t - 1 + c.rot) & 15) * 8192); SB();
      us(QK1(0, b0), [&] { b2 = rk(1, 2); b3 = rk(1, 3); }, [&] { v0 = rv(0); }, U(b1, [&] { aaddu(ka[0], dk); aaddu(ka[1], dk); }));
      us(QK1(1, b1), [&] { v1 = rv(1); }, E0(0), U(b2, E0(1)));
      us(QK1(2, b2), [&] { v2 = rv(2); }, E0(2), U(b3, E0(3)));
      us(QK1(3, b3), [&] { v3 = rv(3); }, E0(4), E0(5));
      us(SUM, E0(6), E0(7), U(v0, E0(8)));
      us(PV(0, v0), [&] { v4 = rv(4); }, E0(9), U(v1, E0(10)));
      us(PV(1, v1), [&] { v5 = rv(5); }, E0(11), U(v2, E1(0)));
      us(PV(2, v2), [&] { v6 = rv(6); }, E1(1), U(v3, E1(2)));
      us(PV(3, v3), [&] { v7 = rv(7); }, E1(3), U(v4, E1(4)));
      us(PV(4, v4), E1(5), [&] { aaddu(ka[2], dk); aaddu(ka[3], dk); }, U(v5, [&] { aaddu(va, dv); }));
      us(PV(5, v5), N, [&] { use(v7); }, N);
    } else {
      us(PV(5, v5h), [&] { a0 = rk(0, 0); a1 = rk(0, 1); }, [&] { a2 = rk(0, 2); a3 = rk(0, 3); }, N);
      us(PV(6, v6), E1(4), [&] { kv(t, 0); }, N);
      us(PV(7, v7), CLO(0), U(a0, CLO(1)), N);
      us(QK0(0, a0), [&] { b0 = rk(1, 0); b1 = rk(1, 1); }, U(a1, E1(5)), N);
      us(QK0(1, a1), E1(6), U(a2, E1(7)), N);
      us(QK0(2, a2), [&] { o1 = aor3(s.pf[0][0], s.pf[0][1], s.pf[1][0]); kv(t, 1); }, U(a3, CHI(0)), N);
      us(QK0(3, a3), CHI(1), U(b0, [&] { o1 = aor3(o1, s.pf[1][1], s.pf[0][2]); o2 = aor3(s.pf[0][3], s.pf[1][2], s.pf[1][3]); s.orv = o1 | o2; }), N);
      check<NB, true>(s, c.rk, c.hs * 131072 + ((t - 1 + c.rot) & 15) * 8192); SB();
      us(QK1(0, b0), [&] { b2 = rk(1, 2); b3 = rk(1, 3); }, U(b1, [&] { v0 = rv(0); kv(t, 2); }), N);
      us(QK1(1, b1), [&] { v1 = rv(1); }, U(b2, [&] { aaddu(ka[0], dk); aaddu(ka[1], dk); kv(t, 3); }), N);
      us(QK1(2, b2), [&] { v2 = rv(2); }, U(b3, E0(0)), N);
      us(QK1(3, b3), [&] { v3 = rv(3); }, [&] { exp1<NB, 1>(s.S0); kv(t, 4); }, N);
      us(SUM, E0(2), U(v0, E0(3)), N);
      us(PV(0, v0), [&] { v4 = rv(4); }, U(v1, [&] { exp1<NB, 4>(s.S0); kv(t, 5); }), N);
      us(PV(1, v1), [&] { v5h = rv(5); }, U(v2, E0(5)), N);
      us(PV(2, v2), [&] { v6 = rv(6); aaddu(ka[2], dk); aaddu(ka[3], dk); }, U(v3, [&] { exp1<NB, 6>(s.S0); kv(t, 6); }), N);
      us(PV(3, v3), [&] { v7 = rv(7); exp1<NB, 7>(s.S0); }, U(v4, [&] { exp1<NB, 0>(s.S1); kv(t, 7); }), N);
      us(PV(4, v4), [&] { exp1<NB, 1>(s.S1); exp1<NB, 2>(s.S1); }, [&] { exp1<NB, 3>(s.S1); aaddu(va, dv); use(v7); }, N);
    }
  }
  STAMP(9);
  top();
  {
    bf16x8 vf[8];
#pragma unroll
    for (int db = 0; db < 4; ++db) vf[db] = rv(db);
    SB();
    if constexpr (NB == 2) us(PV(5, v5h), N, N, N);
    us(PV(6, v6), N, N, N);
    exps<NB, 2 * NB, 4 * NB>(s.S1);
    us(PV(7, v7), CLO(0), CLO(1), CLO(2));
    CHI(0)(); CHI(1)(); CHI(2)();
    s.orv = 0;
#pragma unroll
    for (int nb = 0; nb < NB; ++nb) { s.orv = aor3(s.orv, s.pf[nb][0], s.pf[nb][1]); s.orv = aor3(s.orv, s.pf[nb][2], s.pf[nb][3]); }
    SB();
    check<NB, false>(s, c.rk, c.hs * 131072 + ((15 + c.rot) & 15) * 8192);
    SB();
#pragma unroll
    for (int db = 4; db < 8; ++db) vf[db] = rv(db);
#pragma unroll
    for (int nb = 0; nb < NB; ++nb) s.l[nb] = mfma(s.ones, asb(s.pf[nb]), s.l[nb]);
#pragma unroll
    for (int db = 0; db < 8; ++db)
#pragma unroll
      for (int nb = 0; nb < NB; ++nb) s.o[nb][db] = mfma(vf[db], asb(s.pf[nb]), s.o[nb][db]);
  }
  STAMP(10);
#pragma unroll
  for (int nb = 0; nb < NB; ++nb) {
    const float inv = 1.f / s.l[nb][0];
    u16* dst = c.of + ((size_t)nb * 128 * 64 + lane) * 8;
#pragma unroll
    for (int e = 0; e < 4; ++e) {
      const f32x4 a0 = s.o[nb][2 * e] * inv, a1 = s.o[nb][2 * e + 1] * inv;
      u32x4 w = {cvtpk(a0[0], a0[1]), cvtpk(a0[2], a0[3]), cvtpk(a1[0], a1[1]), cvtpk(a1[2], a1[3])};
      *(u32x4*)(dst + e * 512) = w;
    }
  }
}

__global__ __launch_bounds__(512, 2) void k1(const u16* xA, const u16* kA, const u16* vA, const u16* wqA,
                                              const u16* xB, const u16* kB, const u16* vB, const u16* wqB, u16* ofrag) {
  extern __shared__ __attribute__((aligned(16))) char smem[];
  const int id = blockIdx.x, xg = id & 7, cc = id >> 3, br = xg >> 2, hg = xg & 3, b = cc >> 2, pr = cc & 3, h0 = hg * 8 + pr * 2;
  const int w = __builtin_amdgcn_readfirstlane(threadIdx.x >> 6);
  asm volatile("s_nop 4");
  Ctx c;
  const u16* x = br ? xB : xA; const u16* k = br ? kB : kA; const u16* v = br ? vB : vA; const u16* wq = br ? wqB : wqA;
  c.rx = __builtin_amdgcn_make_buffer_rsrc((void*)(x + (size_t)b * SQ * DM), (short)0, SQ * DM * 2, 0x00020000);
  c.rw = __builtin_amdgcn_make_buffer_rsrc((void*)(wq + h0 * DH), (short)0, DM * NH * DH * 2 - h0 * DH * 2, 0x00020000);
  c.rk = __builtin_amdgcn_make_buffer_rsrc((void*)(k + (size_t)(b * NH + h0) * SK * DH), (short)0, 2 * SK * DH * 2, 0x00020000);
  c.rv = __builtin_amdgcn_make_buffer_rsrc((void*)(v + (size_t)(b * NH + h0) * SK * DH), (short)0, 2 * SK * DH * 2, 0x00020000);
  c.lds0 = (unsigned)(size_t)(LDS3 char*)smem;
  c.hs = w >> 2; c.j = w & 3; c.rot = (id * 5) & 15; c.krot = KROT ? ((10 * pr + 5 * b) >> 2) % 10 : 0;
  const bool big = (c.j < 2) == (c.hs == 0);
  c.blk0 = c.hs == 0 ? (c.j < 2 ? 3 * c.j : 2 + 2 * c.j) : (c.j < 2 ? 2 * c.j : 3 * c.j - 2);
  c.of = ofrag + ((size_t)(br * 80 + b * 10 + c.blk0) * 128 + (h0 + c.hs) * 4) * 512;
  c.dbg = (unsigned long long*)((char*)ofrag + (128u << 20));
  if (threadIdx.x == 0) *(unsigned*)((char*)ofrag + (64u << 20) + id * 256) = 0u;
  if (big) body<3>(c); else body<2>(c);
}

typedef __attribute__((address_space(1))) unsigned gu32;
typedef unsigned u32x2 __attribute__((ext_vector_type(2)));
constexpr size_t WS_SLAB = 32u << 20, WS_FLAG = 64u << 20;
#define K2NT true
__global__ __launch_bounds__(512, 2) void k2(const u16* ofrag, const u16* woA, const u16* woB, u16* out, char* ws) {
  extern __shared__ __attribute__((aligned(16))) char smem[];
  STAMP(0);
  const int id = blockIdx.x, xg = id & 7, cc = id >> 3, br = xg >> 2, hg = xg & 3, b = cc >> 2, cq = cc & 3;
  const int w = __builtin_amdgcn_readfirstlane(threadIdx.x >> 6);
  asm volatile("s_nop 4");
  const int lane = threadIdx.x & 63, l15 = lane & 15, g = lane >> 4;
  const int q = w & 3, qm = q & 1, qn = q >> 1, kh = w >> 2;
  const unsigned lds0 = (unsigned)(size_t)(LDS3 char*)smem;
  const int dkh = q >> 1, dh = q & 1;
  rsrc_t rs; unsigned voff[5], sinc, sstep, ldst;
  if (w < 4) {
    const u16* wo = (br ? woB : woA) + (size_t)(hg * 1024 + dkh * 512 + dh * 16) * 640 + cq * 160;
    rs = __builtin_amdgcn_make_buffer_rsrc((void*)wo, (short)0, 512 * 1280 - dh * 16 * 1280 - cq * 320, 0x00020000);
    sinc = 32 * 1280; sstep = 0; ldst = dkh * 20480 + 10240 + dh * 5120;
  } else {
    const u16* o = ofrag + ((size_t)(br * 80 + b * 10 + dh * 5) * 128 + hg * 32 + dkh * 16) * 512;
    rs = __builtin_amdgcn_make_buffer_rsrc((void*)o, (short)0, 5 * 128 * 1024, 0x00020000);
    sinc = 1024; sstep = 128 * 1024; ldst = dkh * 20480 + dh * 5120;
  }
#pragma unroll
  for (int n = 0; n < 5; ++n) {
    const int e = 64 * n + lane, row = e / 20, ch = e - row * 20;
    voff[n] = w < 4 ? row * 1280 + ((ch ^ (((row >> 2) & 1) << 1)) * 16) : lane * 16;
  }
  const int rot = 2 * b;
  auto dma = [&](int i, int stg, int n) { dma16<K2NT>(lds0 + stg * 40960 + ldst + n * 1024, voff[n], rs, i < 16 ? ((i + rot) & 15) * sinc + n * sstep : OOB); };
#pragma unroll
  for (int i = 0; i < 3; ++i)
#pragma unroll
    for (int n = 0; n < 5; ++n) dma(i, i, n);
  unsigned oa[2], wa[2][2];
  {
    const int bit = g & 1;
    oa[0] = lds0 + kh * 20480 + qm * 5120 + lane * 16; oa[1] = oa[0] + 81920;
    const unsigned wb = lds0 + kh * 20480 + 10240 + (4 * g + (l15 >> 2)) * 320 + (lane & 3) * 8 + qn * 160;
    const int sg = (qn ? -32 : 32) * bit;
    wa[0][0] = wb + sg; wa[0][1] = wb - sg; wa[1][0] = wa[0][0] + 81920; wa[1][1] = wa[0][1] + 81920;
    asm volatile("" : "+v"(oa[1]), "+v"(wa[1][0]), "+v"(wa[1][1]));
  }
  f32x4 acc[5][5];
  const f32x4 z4 = {0.f, 0.f, 0.f, 0.f};
#pragma unroll
  for (int m = 0; m < 5; ++m)
#pragma unroll
    for (int n = 0; n < 5; ++n) acc[m][n] = z4;
  const bf16x8 zf = {};
  bf16x8 w3 = zf, w4 = zf, op[5] = {zf, zf, zf, zf, zf};
  auto N0 = [] {};
  STAMP(1);
  SB();
#pragma unroll 1
  for (int i4 = 0; i4 < 16; i4 += 4) {
#pragma unroll
    for (int u = 0; u < 4; ++u) {
      wait_vm_bar<10>();
      auto ro = [&](int m) { return ld128(oa[u >> 1] + (u & 1) * 40960 + m * 1024); };
      auto rw = [&](int n) { const unsigned a = wa[u >> 1][n & 1] + (u & 1) * 40960 + n * 32; return cat(ldtr(a), ldtr(a + 5120)); };
      auto D = [&](int n) { return [&, n] { dma(i4 + u + 3, (u + 3) & 3, n); }; };
      auto mm = [&](int n, bf16x8 wf, bf16x8 (&of)[5], auto g0, auto g1, auto g2, auto g3, auto g4) {
        amfma(acc[0][n], wf, of[0]); SB(); g0(); SB(); amfma(acc[1][n], wf, of[1]); SB(); g1(); SB(); amfma(acc[2][n], wf, of[2]); SB(); g2(); SB();
        amfma(acc[3][n], wf, of[3]); SB(); g3(); SB(); amfma(acc[4][n], wf, of[4]); SB(); g4(); SB();
      };
      bf16x8 of[5], w0, w1, w2;
      mm(3, w3, op, [&] { w0 = rw(0); }, [&] { of[0] = ro(0); of[1] = ro(1); }, [&] { of[2] = ro(2); of[3] = ro(3); }, [&] { of[4] = ro(4); }, D(0));
      mm(4, w4, op, [&] { w1 = rw(1); }, D(1), [&] { w2 = rw(2); }, D(2), [&] { use(of[4]); });
      mm(0, w0, of, [&] { w3 = rw(3); }, D(3), [&] { w4 = rw(4); }, D(4), [&] { use(w1); });
      mm(1, w1, of, N0, N0, N0, N0, [&] { use(w2); });
      mm(2, w2, of, [&] { use(w4); }, N0, N0, N0, N0);
#pragma unroll
      for (int m = 0; m < 5; ++m) op[m] = of[m];
    }
  }
#pragma unroll
  for (int m = 0; m < 5; ++m) { amfma(acc[m][3], w3, op[m]); amfma(acc[m][4], w4, op[m]); }
  asm volatile("s_nop 7");
  STAMP(2);
  wait_vm_bar<0>();
  LDS3 char* xq = (LDS3 char*)smem + q * 25600 + lane * 16;
#pragma unroll
  for (int e = 0; e < 25; ++e) if ((e < 13) == (kh == 1)) *(LDS3 f32x4*)(xq + e * 1024) = acc[e / 5][e % 5];
  __syncthreads();
  const int tile = (br * 8 + b) * 4 + cq;
  const rsrc_t rslab = __builtin_amdgcn_make_buffer_rsrc(ws + WS_SLAB + (size_t)tile * (12 * 25600), (short)0, 12 * 25600, 0x00020000);
  if (q != hg) {
    const int so = (q * 3 + ((hg - q - 1) & 3)) * 25600;
#pragma unroll
    for (int e = 0; e < 25; ++e) if ((e < 13) == (kh == 0)) {
      const f32x4 v = acc[e / 5][e % 5] + *(const LDS3 f32x4*)(xq + e * 1024);
      __builtin_amdgcn_raw_buffer_store_b128(__builtin_bit_cast(u32x4, v), rslab, lane * 16 + e * 1024, so, 16);
    }
  } else {
#pragma unroll
    for (int e = 0; e < 25; ++e) if ((e < 13) == (kh == 0)) *(LDS3 f32x4*)(xq + e * 1024) = acc[e / 5][e % 5] + *(const LDS3 f32x4*)(xq + e * 1024);
  }
  STAMP(3);
  asm volatile("s_waitcnt vmcnt(0) lgkmcnt(0)\n\ts_barrier" ::: "memory");
  STAMP(4);
  gu32* flags = (gu32*)(ws + WS_FLAG) + tile * 4 * 64;
  if (w == 0) {
    if (lane < 4 && lane != hg) __hip_atomic_fetch_add(flags + lane * 64, 1u, __ATOMIC_RELAXED, __HIP_MEMORY_SCOPE_AGENT);
    for (unsigned spins = 0; spins < (1u << 20); ++spins) {
      if ((unsigned)__builtin_amdgcn_readfirstlane(__hip_atomic_load(flags + hg * 64, __ATOMIC_RELAXED, __HIP_MEMORY_SCOPE_AGENT)) >= 3u) break;
      __builtin_amdgcn_s_sleep(1);
    }
  }
  asm volatile("s_waitcnt vmcnt(0)\n\ts_barrier" ::: "memory");
  STAMP(5);
  {
    u32x4 r[4][3];
#pragma unroll
    for (int k = 0; k < 4; ++k)
#pragma unroll
      for (int sl = 0; sl < 3; ++sl) r[k][sl] = __builtin_amdgcn_raw_buffer_load_b128(rslab, (k == 3 && w != 0) ? OOB : lane * 16 + (w + 8 * k) * 1024, (hg * 3 + sl) * 25600, 16);
    u16* ob = out + (size_t)(b * 160 + (hg & 1) * 80 + l15) * 1280 + br * 640 + cq * 160 + (hg >> 1) * 80 + 4 * g;
#pragma unroll
    for (int k = 0; k < 4; ++k) {
      const int e = w + 8 * k;
      if (e < 25) {
        const f32x4 v = *(const LDS3 f32x4*)((LDS3 char*)smem + hg * 25600 + lane * 16 + e * 1024) +
                        __builtin_bit_cast(f32x4, r[k][0]) + __builtin_bit_cast(f32x4, r[k][1]) + __builtin_bit_cast(f32x4, r[k][2]);
        const int m = e / 5, n = e - m * 5;
        *(u32x2*)(ob + m * (16 * 1280) + n * 16) = u32x2{cvtpk(v[0], v[1]), cvtpk(v[2], v[3])};
      }
    }
  }
}

extern "C" void kernel_launch(void* const* d_in, const int* in_sizes, int n_in, void* d_out, int out_size, void* d_ws, size_t ws_size, hipStream_t stream) {
  (void)hipFuncSetAttribute((const void*)k1, hipFuncAttributeMaxDynamicSharedMemorySize, LDS_BYTES);
  (void)hipFuncSetAttribute((const void*)k2, hipFuncAttributeMaxDynamicSharedMemorySize, LDS_BYTES);
  u16* of = (u16*)d_ws;
  k1<<<256, 512, LDS_BYTES, stream>>>((const u16*)d_in[0], (const u16*)d_in[1], (const u16*)d_in[2], (const u16*)d_in[3],
                                      (const u16*)d_in[5], (const u16*)d_in[6], (const u16*)d_in[7], (const u16*)d_in[8], of);
  k2<<<256, 512, LDS_BYTES, stream>>>(of, (const u16*)d_in[4], (const u16*)d_in[9], (u16*)d_out, (char*)d_ws);
}
```

```cpp
#include <hip/hip_runtime.h>
#include <hip/hip_bf16.h>
#include <type_traits>

typedef __bf16 bf16x8 __attribute__((ext_vector_type(8)));
typedef __bf16 bf16x4 __attribute__((ext_vector_type(4)));
typedef __bf16 bf16x2 __attribute__((ext_vector_type(2)));
typedef float f32x4 __attribute__((ext_vector_type(4)));
typedef float f32x2 __attribute__((ext_vector_type(2)));
typedef unsigned u32x4 __attribute__((ext_vector_type(4)));
typedef unsigned u32x2 __attribute__((ext_vector_type(2)));
typedef unsigned short u16;
typedef __attribute__((address_space(1))) unsigned long long gu64;
typedef __amdgpu_buffer_rsrc_t rsrc_t;
#define LDS3 __attribute__((address_space(3)))
#define DI __device__ __forceinline__
#define SB() __builtin_amdgcn_sched_barrier(0)
#define RLX_AGENT __ATOMIC_RELAXED, __HIP_MEMORY_SCOPE_AGENT
template <int N> using IC = std::integral_constant<int, N>;

constexpr int SQ = 160, DM = 640, NH = 32, SK = 512, DH = 128;
constexpr int LDS_BYTES = 163840;
constexpr int KV_HS = 32768, V_OFF = 65536;
__host__ __device__ constexpr int xoff(int b) { return b * 65536 + 73728; }
__host__ __device__ constexpr int woff(int b) { return b == 0 ? 16384 : b == 1 ? 49152 : b == 2 ? 106496 : 122880; }
constexpr float QSCALE = 0.08838834764831845f * 1.4426950408889634f;
constexpr float BIAS = 10.f;
constexpr unsigned OOB = 0x7ff00000u;
constexpr size_t WS_SLAB = 32u << 20, WS_SYNC = 64u << 20;
constexpr int FS = 32;
constexpr int QB = 13 * 1024;
#define KVN0 1
#define XNT false
#define WNT true
#define WONT false

DI unsigned cvtpk(float lo, float hi) { f32x2 v = {lo, hi}; bf16x2 b = __builtin_convertvector(v, bf16x2); return __builtin_bit_cast(unsigned, b); }
DI f32x4 mfma(bf16x8 a, bf16x8 b, f32x4 c) { return __builtin_amdgcn_mfma_f32_16x16x32_bf16(a, b, c, 0, 0, 0); }
DI bf16x8 ld128(unsigned addr) { return *(const LDS3 bf16x8*)(size_t)addr; }
DI void st128(unsigned addr, u32x4 v) { *(LDS3 u32x4*)(size_t)addr = v; }
DI bf16x4 ldtr(unsigned addr) { return __builtin_amdgcn_ds_read_tr16_b64_v4bf16((LDS3 bf16x4*)(size_t)addr); }
DI bf16x8 cat(bf16x4 a, bf16x4 b) { return __builtin_shufflevector(a, b, 0, 1, 2, 3, 4, 5, 6, 7); }
DI bf16x8 asb(u32x4 w) { return __builtin_bit_cast(bf16x8, w); }
DI rsrc_t mkr(const void* p, int bytes) { return __builtin_amdgcn_make_buffer_rsrc((void*)p, (short)0, bytes, 0x00020000); }
template <bool NT> DI void dma16(unsigned ldsaddr, unsigned voff, rsrc_t r, unsigned soff) {
  if constexpr (NT) asm volatile("s_mov_b32 m0, %0\n\ts_nop 0\n\tbuffer_load_dwordx4 %1, %2, %3 offen nt lds" :: "s"(ldsaddr), "v"(voff), "s"(r), "s"(soff) : "memory");
  else              asm volatile("s_mov_b32 m0, %0\n\ts_nop 0\n\tbuffer_load_dwordx4 %1, %2, %3 offen lds" :: "s"(ldsaddr), "v"(voff), "s"(r), "s"(soff) : "memory");
}
template <int N> DI void wait_vm_bar() { asm volatile("s_waitcnt vmcnt(%0) lgkmcnt(0)\n\ts_barrier" :: "i"(N) : "memory"); }
DI float xmax16(float v) { auto r = __builtin_amdgcn_permlane16_swap(__float_as_uint(v), __float_as_uint(v), false, false); return fmaxf(__uint_as_float(r[0]), __uint_as_float(r[1])); }
DI float xmax32(float v) { auto r = __builtin_amdgcn_permlane32_swap(__float_as_uint(v), __float_as_uint(v), false, false); return fmaxf(__uint_as_float(r[0]), __uint_as_float(r[1])); }
DI void amfma(f32x4& acc, bf16x8 a, bf16x8 b) { asm volatile("v_mfma_f32_16x16x32_bf16 %0, %1, %2, %0" : "+v"(acc) : "v"(a), "v"(b)); }
DI void amfma_c(f32x4& d, bf16x8 a, bf16x8 b, f32x4 cc) { asm volatile("v_mfma_f32_16x16x32_bf16 %0, %1, %2, %3" : "=&v"(d) : "v"(a), "v"(b), "v"(cc)); }
DI void amfma_z(f32x4& d, bf16x8 a, bf16x8 b) { asm volatile("v_mfma_f32_16x16x32_bf16 %0, %1, %2, 0" : "=&v"(d) : "v"(a), "v"(b)); }
template <bool Z> DI void amf(f32x4& d, bf16x8 a, bf16x8 b) { if constexpr (Z) amfma_z(d, a, b); else amfma(d, a, b); }
DI float aexp(float x) { asm volatile("v_exp_f32 %0, %0" : "+v"(x)); return x; }
DI unsigned aor3(unsigned a, unsigned b, unsigned c) { unsigned r; asm volatile("v_or3_b32 %0, %1, %2, %3" : "=v"(r) : "v"(a), "v"(b), "v"(c)); return r; }
DI unsigned acvt(float lo, float hi) { unsigned r; asm volatile("v_cvt_pk_bf16_f32 %0, %1, %2" : "=v"(r) : "v"(lo), "v"(hi)); return r; }
DI void use(bf16x8 f) { asm volatile("" :: "v"(f)); }
DI void aaddu(unsigned& a, int d) { asm volatile("v_add_u32 %0, %1, %0" : "+v"(a) : "s"(d)); }
DI unsigned laneid() { unsigned l; asm volatile("v_mbcnt_lo_u32_b32 %0, -1, 0\n\tv_mbcnt_hi_u32_b32 %0, -1, %0" : "=v"(l)); return l; }
template <class T> DI void pin(T& v) { asm volatile("" : "+v"(v)); }

#define STAMP(i)

struct Ctx {
  rsrc_t rx, rw, rk, rv;
  unsigned lds0;
  int j, hs, blk0, rot, krot, w, id;
  const u16* wo; u16* out; char* ws;
};
template <int NB> struct St {
  f32x4 o[NB][8], negm[NB], S0[NB], S1[NB], l[NB];
  bf16x8 qf[NB][4], ones;
  u32x4 pf[NB];
  unsigned orv;
};
template <int NB, int E> DI void exp1(f32x4 (&S)[NB]) { if constexpr (E >= 0 && E < 4 * NB) S[E % NB][E / NB] = aexp(S[E % NB][E / NB]); }
template <int NB, int LO, int HI> DI void exps(f32x4 (&S)[NB]) { if constexpr (LO < HI) { exp1<NB, LO>(S); exps<NB, LO + 1, HI>(S); } }
DI float max8(f32x4 a, f32x4 b) { return fmaxf(fmaxf(fmaxf(a[0], a[1]), fmaxf(a[2], a[3])), fmaxf(fmaxf(b[0], b[1]), fmaxf(b[2], b[3]))); }

template <int NB, bool HAS_NEXT> DI void refix(St<NB>& s, rsrc_t rk, int ksoff) {
  const unsigned ln = laneid();
  const int kvo = (ln & 15) * 256 + ((((ln >> 4) & 1) << 1) | (ln >> 5)) * 16;
  asm volatile("s_nop 7");
#pragma unroll
  for (int nb = 0; nb < NB; ++nb) {
    f32x4 a = s.negm[nb], b = s.negm[nb];
#pragma unroll
    for (int st = 0; st < 4; ++st) {
      a = mfma(asb(__builtin_amdgcn_raw_buffer_load_b128(rk, kvo + st * 64, ksoff, 0)), s.qf[nb][st], a);
      SB();
      b = mfma(asb(__builtin_amdgcn_raw_buffer_load_b128(rk, kvo + st * 64, ksoff + 4096, 0)), s.qf[nb][st], b);
      SB();
    }
    const float d = fmaxf(xmax32(xmax16(max8(a, b))) + BIAS, 0.f);
    const float f = __builtin_amdgcn_exp2f(-d);
    s.negm[nb] -= d; if (HAS_NEXT) s.S0[nb] -= d;
    s.l[nb] *= f;
#pragma unroll
    for (int db = 0; db < 8; ++db) s.o[nb][db] *= f;
#pragma unroll
    for (int r = 0; r < 4; ++r) { a[r] = __builtin_amdgcn_exp2f(a[r] - d); b[r] = __builtin_amdgcn_exp2f(b[r] - d); }
    u32x4 w = {cvtpk(a[0], a[1]), cvtpk(a[2], a[3]), cvtpk(b[0], b[1]), cvtpk(b[2], b[3])};
    s.pf[nb] = w;
    SB();
  }
  asm volatile("s_nop 1");
}
template <int NB, bool HAS_NEXT> DI void check(St<NB>& s, rsrc_t rk, int ksoff) {
  if (__builtin_expect(__any((s.orv & 0x40004000u) != 0u), 0)) refix<NB, HAS_NEXT>(s, rk, ksoff);
}
template <int... I, class F> DI void static_for(std::integer_sequence<int, I...>, F f) { (f(IC<I>{}), ...); }

template <int NB> DI void body(const Ctx& c) {
  const int lane = threadIdx.x & 63, l15 = lane & 15, g = lane >> 4;
  const unsigned lds0 = c.lds0;
  const f32x4 z4 = {0.f, 0.f, 0.f, 0.f};
  auto N0 = [] {};
  const unsigned voffK = (lane >> 4) * 256 + (((lane & 15) ^ (4 * c.j + (lane >> 4))) * 16);
  const unsigned voffV = ((lane >> 1) & 7) * 256 + (lane >> 4) * 32 + (lane & 1) * 16;
  auto kvl = [&](int t, int q) { return lds0 + (q >> 1) * KV_HS + (t & 3) * 8192 + (c.j + 4 * (q & 1)) * 1024; };
  auto kso = [&](int t, int q) { return (q >> 1) * 131072 + ((t + c.rot) & 15) * 8192 + (c.j + 4 * (q & 1)) * 1024; };
  auto vso = [&](int t, int q) { const int i = c.j + 4 * (q & 1); return (q >> 1) * 131072 + ((t + c.rot) & 15) * 8192 + (i >> 1) * 2048 + (i & 1) * 128; };
  auto dma_k1 = [&](int t, int q) { dma16<true>(kvl(t, q), voffK, c.rk, t < 16 ? kso(t, q) : OOB); };
  auto dma_v1 = [&](int t, int q) { dma16<true>(V_OFF + kvl(t, q), voffV, c.rv, t < 16 ? vso(t, q) : OOB); };
  STAMP(0);
  St<NB> s;
  const unsigned long long epoch = __hip_atomic_load((gu64*)(c.ws + WS_SYNC), RLX_AGENT);
  u32x4 kvr[16];
  {
    unsigned voffS;
    if constexpr (NB == 3) { const int f = (2 * (c.j & 1) + (lane >> 5)) | ((c.j >> 1) << 2); voffS = (lane >> 5) * 8192 + (((lane & 31) ^ (f << 1)) * 16); }
    else voffS = (lane >> 3) * 1280 + (((lane & 7) ^ (4 * (c.j & 1) + g)) * 16);
    auto dma_w = [&](int st, int buf, int n) {
      int ks = st + 2 * c.krot; ks = ks >= 20 ? ks - 20 : ks;
      const int i = (c.j & 1) + 4 * (c.j >> 1) + 2 * (n & 1) + 8 * (n >> 1);
      dma16<WNT>(lds0 + woff(buf) + i * 1024, voffS, c.rw, st < 20 ? (ks * 32 + 2 * i) * 8192 : OOB);
    };
    auto dma_x = [&](int ch, int n) {
      int kc = ch + c.krot; kc = kc >= 10 ? kc - 10 : kc;
      const int i = c.j + 4 * n;
      dma16<XNT>(lds0 + xoff(ch & 1) + i * 1024, voffS, c.rx, i * (8 * 1280) + kc * 128);
    };
    auto extra = [&](int m, int q) {
      if (m == 0 || m == 1) dma_k1(m, q);
      else if (m == 2) dma_v1(0, q);
      else if (m == 3 || m == 5) kvr[(m - 3) * 4 + q] = __builtin_amdgcn_raw_buffer_load_b128(c.rk, voffK, kso((m + 1) / 2, q), 2);
      else if (m == 4 || m == 6) kvr[(m - 3) * 4 + q] = __builtin_amdgcn_raw_buffer_load_b128(c.rv, voffV, vso((m - 2) / 2, q), 2);
    };
    if constexpr (NB == 3) {
#pragma unroll
      for (int st = 0; st < 3; ++st)
#pragma unroll
        for (int n = 0; n < 4; ++n) dma_w(st, st, n);
    } else {
#pragma unroll
      for (int n = 0; n < 5; ++n) dma_x(0, n);
      __builtin_amdgcn_s_setprio(1);
    }
    unsigned wa[2][8], xa[4];
    const int qq = l15 >> 2, p = lane & 3, f = qq | ((g & 1) << 2);
#pragma unroll
    for (int db = 0; db < 8; ++db) { wa[0][db] = lds0 + woff(0) + (8 * g + qq) * 512 + c.hs * 256 + ((db ^ f) * 32) + p * 8; wa[1][db] = wa[0][db] + (woff(2) - woff(0)); pin(wa[1][db]); }
#pragma unroll
    for (int u = 0; u < 4; ++u) { xa[u] = lds0 + xoff(u >> 1) + (c.blk0 * 16 + l15) * 128 + (((4 * (u & 1) + g) ^ ((l15 >> 1) & 7)) * 16); pin(xa[u]); }
    bf16x8 w4, w5, w6, w7, xp[NB];
    auto qstep = [&](auto U, auto PH, auto D) {
      constexpr int u = decltype(U)::value, ph = decltype(PH)::value;
      auto rw = [&](int db) { const unsigned a = wa[u >> 1][db] + (woff(u) - woff(u & 2)); return cat(ldtr(a), ldtr(a + 2048)); };
      auto rx = [&](int nb) { return ld128(xa[u] + nb * 2048); };
      auto mm = [&](auto Z, int db, bf16x8& wf, bf16x8 (&xf)[NB], auto g0, auto g1, auto g2) {
        constexpr int z = decltype(Z)::value;
        if constexpr (z == 2) { g0(); g1(); g2(); SB(); }
        else {
          amf<z == 1>(s.o[0][db], wf, xf[0]); SB(); g0(); SB();
          amf<z == 1>(s.o[1][db], wf, xf[1]); SB(); g1(); SB();
          if constexpr (NB == 3) { amf<z == 1>(s.o[2][db], wf, xf[2]); SB(); }
          g2(); SB();
        }
      };
      constexpr IC<ph == 0 ? 2 : ph == 1 ? 1 : 0> H{};
      constexpr IC<ph == 0 ? 1 : 0> C{};
      bf16x8 xf[NB], w0, w1, w2, w3;
      if constexpr (NB == 3) {
        mm(H, 4, w4, xp, [&] { w0 = rw(0); }, [&] { xf[0] = rx(0); xf[1] = rx(1); }, [&] { xf[2] = rx(2); });
        mm(H, 5, w5, xp, [&] { w1 = rw(1); }, [&] { D(0); }, N0);
        mm(H, 6, w6, xp, [&] { w2 = rw(2); }, [&] { w3 = rw(3); }, N0);
        mm(H, 7, w7, xp, [&] { w4 = rw(4); }, [&] { D(1); }, [&] { use(xf[NB - 1]); });
        mm(C, 0, w0, xf, [&] { w5 = rw(5); }, [&] { use(w1); w6 = rw(6); }, N0);
        mm(C, 1, w1, xf, [&] { w7 = rw(7); }, [&] { use(w2); D(2); }, N0);
        mm(C, 2, w2, xf, [&] { use(w3); }, [&] { D(3); }, N0);
        mm(C, 3, w3, xf, [&] { use(w7); }, N0, N0);
      } else {
        mm(H, 4, w4, xp, [&] { w0 = rw(0); }, [&] { xf[0] = rx(0); xf[1] = rx(1); }, N0);
        mm(H, 5, w5, xp, [&] { w1 = rw(1); }, [&] { D(0); }, N0);
        mm(H, 6, w6, xp, [&] { w2 = rw(2); }, [&] { w3 = rw(3); D(1); }, N0);
        mm(H, 7, w7, xp, [&] { w4 = rw(4); }, [&] { D(2); use(xf[NB - 1]); }, N0);
        mm(C, 0, w0, xf, [&] { w5 = rw(5); }, [&] { use(w1); w6 = rw(6); D(3); }, N0);
        mm(C, 1, w1, xf, [&] { w7 = rw(7); }, [&] { use(w2); D(4); }, N0);
        mm(C, 2, w2, xf, [&] { use(w3); }, N0, N0);
        mm(C, 3, w3, xf, [&] { use(w7); }, N0, N0);
      }
#pragma unroll
      for (int nb = 0; nb < NB; ++nb) xp[nb] = xf[nb];
    };
    STAMP(1);
    SB();
    if constexpr (NB == 3) {
      static_for(std::make_integer_sequence<int, 4>{}, [&](auto U) {
        constexpr int u = decltype(U)::value;
        wait_vm_bar<8>(); qstep(U, IC<(u < 2 ? u : 2)>{}, [&](int n) { dma_w(u + 3, (u + 3) & 3, n); });
      });
#pragma unroll 1
      for (int s4 = 4; s4 < 20; s4 += 4)
        static_for(std::make_integer_sequence<int, 4>{}, [&](auto U) {
          constexpr int u = decltype(U)::value;
          wait_vm_bar<8>(); qstep(U, IC<2>{}, [&](int n) { dma_w(s4 + u + 3, (u + 3) & 3, n); });
        });
    } else {
      static_for(std::make_integer_sequence<int, 20>{}, [&](auto ST) {
        constexpr int st = decltype(ST)::value, mp = (st >> 1) - 1 - KVN0;
        if constexpr (st & 1) wait_vm_bar<63>(); else wait_vm_bar<(mp >= 0 && mp < 7) ? 4 : 0>();
        qstep(IC<(st & 3)>{}, IC<(st < 2 ? st : 2)>{}, [&](int n) {
          if constexpr (st & 1) { if (n < 4) extra((st >> 1) - KVN0, n); } else if constexpr ((st >> 1) + 1 < 10) dma_x((st >> 1) + 1, n); });
      });
    }
#pragma unroll
    for (int nb = 0; nb < NB; ++nb) { amfma(s.o[nb][4], w4, xp[nb]); amfma(s.o[nb][5], w5, xp[nb]); amfma(s.o[nb][6], w6, xp[nb]); amfma(s.o[nb][7], w7, xp[nb]); }
    asm volatile("s_nop 7");
  }
  STAMP(2);
  wait_vm_bar<0>();
  const unsigned long long tag = (((unsigned long long)__builtin_amdgcn_readfirstlane((unsigned)(epoch >> 32)) << 32) | __builtin_amdgcn_readfirstlane((unsigned)epoch)) + 1ull;
  const int pg = ((g & 1) << 1) | (g >> 1);
  unsigned ka[4];
#pragma unroll
  for (int st = 0; st < 4; ++st) ka[st] = lds0 + c.hs * KV_HS + l15 * 256 + (((4 * st + pg) ^ l15) * 16);
  unsigned va = lds0 + V_OFF + c.hs * KV_HS + (g >> 1) * 2048 + (4 * (g & 1) + (l15 >> 2)) * 32 + (lane & 3) * 8;
  auto rk = [&](int kt, int st) { return ld128(ka[st] + kt * 4096); };
  auto rv = [&](int db) { const unsigned a = va + (db >> 2) * 1024 + (db & 3) * 256; return cat(ldtr(a), ldtr(a + 4096)); };
  {
    bf16x8 kf0[4], kf1[4];
#pragma unroll
    for (int st = 0; st < 4; ++st) { kf0[st] = rk(0, st); kf1[st] = rk(1, st); }
#pragma unroll
    for (int nb = 0; nb < NB; ++nb)
#pragma unroll
      for (int e = 0; e < 4; ++e) {
        const f32x4 a0 = s.o[nb][2 * e] * QSCALE, a1 = s.o[nb][2 * e + 1] * QSCALE;
        auto r0 = __builtin_amdgcn_permlane16_swap(cvtpk(a0[0], a0[1]), cvtpk(a1[0], a1[1]), false, false);
        auto r1 = __builtin_amdgcn_permlane16_swap(cvtpk(a0[2], a0[3]), cvtpk(a1[2], a1[3]), false, false);
        u32x4 w = {r0[0], r1[0], r0[1], r1[1]};
        s.qf[nb][e] = asb(w);
        if constexpr (NB == 2) {
#pragma unroll
          for (int h = 0; h < 2; ++h) { const int i = (nb * 4 + e) * 2 + h, m = i >> 2; st128(((m & 1) ? V_OFF + kvl(1 + (m >> 1), i & 3) : kvl(2 + (m >> 1), i & 3)) + lane * 16, kvr[i]); }
        }
      }
#pragma unroll
    for (int st = 0; st < 4; ++st)
#pragma unroll
      for (int nb = 0; nb < NB; ++nb) { s.S0[nb] = mfma(kf0[st], s.qf[nb][st], st == 0 ? z4 : s.S0[nb]); s.S1[nb] = mfma(kf1[st], s.qf[nb][st], st == 0 ? z4 : s.S1[nb]); }
#pragma unroll
    for (int nb = 0; nb < NB; ++nb) {
#pragma unroll
      for (int db = (NB == 3 ? 6 : 5); db < 8; ++db) { s.o[nb][db] = z4; pin(s.o[nb][db]); }
      const float m = xmax32(xmax16(max8(s.S0[nb], s.S1[nb]))) + BIAS;
      s.negm[nb] = z4 - m; s.S0[nb] -= m; s.S1[nb] -= m;
      pin(s.negm[nb]);
    }
    s.ones = asb(u32x4{0x3f803f80u, 0x3f803f80u, 0x3f803f80u, 0x3f803f80u});
    pin(s.ones);
  }
  exps<NB, 0, 4 * NB>(s.S0); exps<NB, 0, 2 * NB>(s.S1);
  bf16x8 v5h, v6, v7;
#pragma unroll
  for (int st = 0; st < 4; ++st) ka[st] += 8192;
  SB();
  auto us = [&](auto m, auto g0, auto g1, auto g2) {
    m(0); SB(); g0(); SB(); m(1); SB(); g1(); SB();
    if constexpr (NB == 3) { m(2); SB(); g2(); SB(); }
  };
  auto QK0 = [&](int st, bf16x8 fr) { return [&s, st, fr](int nb) { if (st == 0) amfma_c(s.S0[nb], fr, s.qf[nb][0], s.negm[nb]); else amfma(s.S0[nb], fr, s.qf[nb][st]); }; };
  auto QK1 = [&](int st, bf16x8 fr) { return [&s, st, fr](int nb) { if (st == 0) amfma_c(s.S1[nb], fr, s.qf[nb][0], s.negm[nb]); else amfma(s.S1[nb], fr, s.qf[nb][st]); }; };
  auto PVH = [&](int db, bf16x8& fr) { return [&s, db, &fr](int nb) { amfma(s.o[nb][db], fr, asb(s.pf[nb])); }; };
  auto N = [] {};
#define E0(e) [&] { exp1<NB, (e)>(s.S0); }
#define E1(e) [&] { exp1<NB, (e)>(s.S1); }
#define CLO(nb) [&] { if constexpr ((nb) < NB) { s.pf[nb][0] = acvt(s.S0[nb][0], s.S0[nb][1]); s.pf[nb][1] = acvt(s.S0[nb][2], s.S0[nb][3]); } }
#define CHI(nb) [&] { if constexpr ((nb) < NB) { s.pf[nb][2] = acvt(s.S1[nb][0], s.S1[nb][1]); s.pf[nb][3] = acvt(s.S1[nb][2], s.S1[nb][3]); } }
#define U(f, ...) [&] { use(f); SB(); (__VA_ARGS__)(); }
  auto astep = [&](auto FI, int t, auto kvh) {
    constexpr bool FIRST = decltype(FI)::value;
    const int dk = (t & 3) == 3 ? -24576 : 8192, dv = (t & 3) == 0 ? -24576 : 8192;
    if constexpr (NB == 2) wait_vm_bar<16>(); else wait_vm_bar<63>();
    auto PH = [&](int db, bf16x8& fr) { return [&, db](int nb) { if constexpr (!FIRST) amfma(s.o[nb][db], fr, asb(s.pf[nb])); }; };
    auto PV = [&](int db, bf16x8 fr) { return [&s, db, fr](int nb) { amf<FIRST>(s.o[nb][db], fr, asb(s.pf[nb])); }; };
    auto SUM = [&](int nb) { amf<FIRST>(s.l[nb], s.ones, asb(s.pf[nb])); };
    bf16x8 a0, a1, a2, a3, b0, b1, b2, b3, v0, v1, v2, v3, v4;
    unsigned o1, o2;
    if constexpr (NB == 3) {
      bf16x8 v5;
      us(PH(6, v6), [&] { a0 = rk(0, 0); a1 = rk(0, 1); }, [&] { a2 = rk(0, 2); a3 = rk(0, 3); }, E1(6));
      us(PH(7, v7), CLO(0), CLO(1), U(a0, CLO(2)));
      us(QK0(0, a0), [&] { b0 = rk(1, 0); b1 = rk(1, 1); }, E1(7), U(a1, E1(8)));
      us(QK0(1, a1), E1(9), E1(10), U(a2, E1(11)));
      us(QK0(2, a2), [&] { o1 = aor3(s.pf[0][0], s.pf[0][1], s.pf[1][0]); o2 = aor3(s.pf[1][1], s.pf[2][0], s.pf[2][1]); }, CHI(0), U(a3, CHI(1)));
      us(QK0(3, a3), CHI(2), [&] { o1 = aor3(o1, s.pf[0][2], s.pf[0][3]); o2 = aor3(o2, s.pf[1][2], s.pf[1][3]); },
                     U(b0, [&] { o1 = aor3(o1, s.pf[2][2], s.pf[2][3]); s.orv = o1 | o2; }));
      if constexpr (!FIRST) check<NB, true>(s, c.rk, c.hs * 131072 + ((t - 1 + c.rot) & 15) * 8192);
      SB();
      us(QK1(0, b0), [&] { b2 = rk(1, 2); b3 = rk(1, 3); }, [&] { v0 = rv(0); }, U(b1, [&] { aaddu(ka[0], dk); aaddu(ka[1], dk); }));
      us(QK1(1, b1), [&] { v1 = rv(1); }, E0(0), U(b2, E0(1)));
      us(QK1(2, b2), [&] { v2 = rv(2); }, E0(2), U(b3, E0(3)));
      us(QK1(3, b3), [&] { v3 = rv(3); }, E0(4), E0(5));
      us(SUM, E0(6), E0(7), U(v0, E0(8)));
      us(PV(0, v0), [&] { v4 = rv(4); }, E0(9), U(v1, E0(10)));
      us(PV(1, v1), [&] { v5 = rv(5); }, E0(11), U(v2, E1(0)));
      us(PV(2, v2), [&] { v6 = rv(6); }, E1(1), U(v3, E1(2)));
      us(PV(3, v3), [&] { v7 = rv(7); }, E1(3), U(v4, E1(4)));
      us(PV(4, v4), E1(5), [&] { aaddu(ka[2], dk); aaddu(ka[3], dk); }, U(v5, [&] { aaddu(va, dv); }));
      us(PV(5, v5), N, [&] { use(v7); }, N);
    } else {
      us(PH(5, v5h), [&] { a0 = rk(0, 0); a1 = rk(0, 1); }, [&] { a2 = rk(0, 2); a3 = rk(0, 3); }, N);
      us(PH(6, v6), E1(4), [&] { kvh(0); }, N);
      us(PH(7, v7), CLO(0), U(a0, CLO(1)), N);
      us(QK0(0, a0), [&] { b0 = rk(1, 0); b1 = rk(1, 1); }, U(a1, E1(5)), N);
      us(QK0(1, a1), E1(6), U(a2, E1(7)), N);
      us(QK0(2, a2), [&] { o1 = aor3(s.pf[0][0], s.pf[0][1], s.pf[1][0]); kvh(1); }, U(a3, CHI(0)), N);
      us(QK0(3, a3), CHI(1), U(b0, [&] { o1 = aor3(o1, s.pf[1][1], s.pf[0][2]); o2 = aor3(s.pf[0][3], s.pf[1][2], s.pf[1][3]); s.orv = o1 | o2; }), N);
      if constexpr (!FIRST) check<NB, true>(s, c.rk, c.hs * 131072 + ((t - 1 + c.rot) & 15) * 8192);
      SB();
      us(QK1(0, b0), [&] { b2 = rk(1, 2); b3 = rk(1, 3); }, U(b1, [&] { v0 = rv(0); kvh(2); }), N);
      us(QK1(1, b1), [&] { v1 = rv(1); }, U(b2, [&] { aaddu(ka[0], dk); aaddu(ka[1], dk); kvh(3); }), N);
      us(QK1(2, b2), [&] { v2 = rv(2); }, U(b3, E0(0)), N);
      us(QK1(3, b3), [&] { v3 = rv(3); }, [&] { exp1<NB, 1>(s.S0); kvh(4); }, N);
      us(SUM, E0(2), U(v0, E0(3)), N);
      us(PV(0, v0), [&] { v4 = rv(4); }, U(v1, [&] { exp1<NB, 4>(s.S0); kvh(5); }), N);
      us(PV(1, v1), [&] { v5h = rv(5); }, U(v2, E0(5)), N);
      us(PV(2, v2), [&] { v6 = rv(6); aaddu(ka[2], dk); aaddu(ka[3], dk); }, U(v3, [&] { exp1<NB, 6>(s.S0); kvh(6); }), N);
      us(PV(3, v3), [&] { v7 = rv(7); exp1<NB, 7>(s.S0); }, U(v4, [&] { exp1<NB, 0>(s.S1); kvh(7); }), N);
      us(PV(4, v4), [&] { exp1<NB, 1>(s.S1); exp1<NB, 2>(s.S1); }, [&] { exp1<NB, 3>(s.S1); aaddu(va, dv); use(v7); }, N);
    }
  };
  auto kv = [&](int t, int n) { if constexpr (NB == 2) { if (n < 4) dma_k1(t + 3, n); else dma_v1(t + 2, n - 4); } };
  STAMP(3);
  astep(std::true_type{}, 1, [&](int n) { kv(1, n); });
#pragma unroll 1
  for (int t = 2; t < (NB == 3 ? 16 : 14); ++t) astep(std::false_type{}, t, [&](int n) { kv(t, n); });
  rsrc_t rof, rwo, rslab; int pr, hg, rotb; gu64* sync; u16* ob;
  {
    int id = c.id; asm volatile("" : "+s"(id));
    const int br = (id >> 2) & 1, b = id >> 5;
    hg = id & 3; pr = (id >> 3) & 3; rotb = b & 3;
    rof = mkr(c.ws + (size_t)((br * 8 + b) * 4 + hg) * 327680, 327680);
    rwo = mkr(c.wo + (size_t)hg * 1024 * 640 + pr * 160, 1024 * 1280 - pr * 320);
    rslab = mkr(c.ws + WS_SLAB + (size_t)((br * 8 + b) * 4 + pr) * (12 * QB), 12 * QB);
    sync = (gu64*)(c.ws + WS_SYNC);
    ob = c.out + (size_t)(b * 160 + (hg & 1) * 80) * 1280 + br * 640 + pr * 160 + (hg >> 1) * 80;
  }
  unsigned lz = 0;
  const int dks = c.j >> 1, dh = c.j & 1;
  auto stg = [](int u) { return (u >> 1) * 81920 + ((u ^ (u >> 1)) & 1) * 40960; };
  auto kpair = [&](int i) { return ((pr + (i >> 2)) & 3) * 4 + ((i + rotb) & 3); };
  auto wvo = [&](int n) { const int e = 64 * n + (int)lz, row = e / 20, ch = e - row * 20; return row * 1280 + ((ch ^ (((row >> 2) & 1) << 1)) * 16); };
  auto wso = [&](int i) { return (kpair(i) * 64 + dks * 32 + dh * 16) * 1280; };
  auto wld = [&](int i, int n) { return lds0 + stg(i & 3) + dks * 20480 + 10240 + dh * 5120 + n * 1024; };
  u32x4 wr[15];
  if constexpr (NB == 2) {
    lz = laneid();
    unsigned vw[5];
#pragma unroll
    for (int n = 0; n < 5; ++n) vw[n] = wvo(n);
    auto wl = [&](int p) { if (p < 15) wr[p] = __builtin_amdgcn_raw_buffer_load_b128(rwo, vw[p % 5], wso(p / 5), WONT ? 2 : 0); };
    SB();
    astep(std::false_type{}, 14, [&](int n) { wl(n); });
    astep(std::false_type{}, 15, [&](int n) { wl(8 + n); });
  }
  STAMP(4);
  if constexpr (NB == 3) wait_vm_bar<63>(); else wait_vm_bar<15>();
  {
    bf16x8 vf[8];
#pragma unroll
    for (int db = 0; db < 4; ++db) vf[db] = rv(db);
    SB();
    if constexpr (NB == 2) us(PVH(5, v5h), N, N, N);
    us(PVH(6, v6), N, N, N);
    exps<NB, 2 * NB, 4 * NB>(s.S1);
    us(PVH(7, v7), CLO(0), CLO(1), CLO(2));
    CHI(0)(); CHI(1)(); CHI(2)();
    s.orv = 0;
#pragma unroll
    for (int nb = 0; nb < NB; ++nb) { s.orv = aor3(s.orv, s.pf[nb][0], s.pf[nb][1]); s.orv = aor3(s.orv, s.pf[nb][2], s.pf[nb][3]); }
    SB();
    check<NB, false>(s, c.rk, c.hs * 131072 + ((15 + c.rot) & 15) * 8192);
    SB();
    if constexpr (NB == 3) lz = laneid();
#pragma unroll
    for (int db = 4; db < 8; ++db) vf[db] = rv(db);
#pragma unroll
    for (int nb = 0; nb < NB; ++nb) s.l[nb] = mfma(s.ones, asb(s.pf[nb]), s.l[nb]);
#pragma unroll
    for (int db = 0; db < 8; ++db)
#pragma unroll
      for (int nb = 0; nb < NB; ++nb) {
        s.o[nb][db] = mfma(vf[db], asb(s.pf[nb]), s.o[nb][db]);
        if constexpr (NB == 2) { const int p = db * 2 + nb; if (p < 15) { SB(); st128(wld(p / 5, p % 5) + lz * 16, wr[p]); SB(); } }
      }
  }
  STAMP(5);
  asm volatile("s_waitcnt lgkmcnt(0)\n\ts_barrier" ::: "memory");
#pragma unroll
  for (int nb = 0; nb < NB; ++nb) {
    const float inv = __builtin_amdgcn_rcpf(s.l[nb][0]);
#pragma unroll
    for (int e = 0; e < 4; ++e) {
      const f32x4 a0 = s.o[nb][2 * e] * inv, a1 = s.o[nb][2 * e + 1] * inv;
      const u32x4 w = {cvtpk(a0[0], a0[1]), cvtpk(a0[2], a0[3]), cvtpk(a1[0], a1[1]), cvtpk(a1[2], a1[3])};
      const int u = (c.hs * 2 + (e >> 1) - rotb) & 3;
      st128(lds0 + stg(u) + (e & 1) * 20480 + (c.blk0 + nb) * 1024 + lz * 16, w);
      __builtin_amdgcn_raw_buffer_store_b128(w, rof, lz * 16 + (pr * 8 + c.hs * 4 + e) * 10240 + (c.blk0 + nb) * 1024, 0, 16);
    }
  }
  STAMP(6);
  {
    constexpr int NM = NB, M0 = NM == 3 ? 0 : 3;
    const int q = c.j, qm = q & 1, qn = q >> 1, zg = lz >> 4;
    unsigned vo3[5];
#pragma unroll
    for (int n = 0; n < 5; ++n) vo3[n] = NB == 3 ? wvo(n) : lz * 16;
    auto dmal = [&](int i, int u, int n) {
      if constexpr (NB == 3) dma16<WONT>(wld(u, n), vo3[n], rwo, i < 16 ? wso(i) : OOB);
      else dma16<false>(lds0 + stg(u) + dks * 20480 + dh * 5120 + n * 1024, vo3[0], rof, i < 16 ? (kpair(i) * 2 + dks) * 10240 + (dh * 5 + n) * 1024 : OOB);
    };
    if constexpr (NB == 3) {
#pragma unroll
      for (int n = 0; n < 5; ++n) dmal(3, 3, n);
    }
    unsigned oa[2], wa[2][2];
    {
      oa[0] = lds0 + (qm * 5 + M0) * 1024 + lz * 16; oa[1] = oa[0] + 81920;
      const unsigned wb = lds0 + 10240 + (4 * zg + ((lz & 15) >> 2)) * 320 + (lz & 3) * 8 + qn * 160;
      const int sg = (qn ? -32 : 32) * (zg & 1);
      wa[0][0] = wb + sg; wa[0][1] = wb - sg; wa[1][0] = wa[0][0] + 81920; wa[1][1] = wa[0][1] + 81920;
      pin(oa[1]); pin(wa[1][0]); pin(wa[1][1]);
    }
    f32x4 acc[NM][5];
    bf16x8 h2, h3, h4, op[NM];
    auto interval = [&](auto UU, auto D, auto tail) {
      constexpr bool FIRST = decltype(UU)::value < 0;
      constexpr int u = FIRST ? 0 : decltype(UU)::value, so = ((u ^ (u >> 1)) & 1) * 40960;
      auto ro = [&](int ks, int m) { return ld128(oa[u >> 1] + so + ks * 20480 + m * 1024); };
      auto rw = [&](int ks, int n) { const unsigned a = wa[u >> 1][n & 1] + so + ks * 20480 + n * 32; return cat(ldtr(a), ldtr(a + 5120)); };
      auto mm = [&](auto Z, int n, bf16x8& wf, bf16x8 (&of)[NM], auto g0, auto g1, auto g2) {
        constexpr int z = decltype(Z)::value;
        if constexpr (z == 2) { g0(); g1(); g2(); SB(); }
        else {
          amf<z == 1>(acc[0][n], wf, of[0]); SB(); g0(); SB(); amf<z == 1>(acc[1][n], wf, of[1]); SB(); g1(); SB();
          if constexpr (NM == 3) { amf<z == 1>(acc[2][n], wf, of[2]); SB(); }
          g2(); SB();
        }
      };
      constexpr IC<FIRST ? 2 : 0> H{};
      constexpr IC<FIRST ? 1 : 0> C{};
      constexpr IC<0> P{};
      bf16x8 o0[NM], o1[NM], a0, a1, a2, a3, a4, b0, b1;
      mm(H, 2, h2, op, [&] { a0 = rw(0, 0); }, [&] { o0[0] = ro(0, 0); o0[1] = ro(0, 1); }, [&] { if constexpr (NM == 3) o0[2] = ro(0, 2); });
      mm(H, 3, h3, op, [&] { a1 = rw(0, 1); }, [&] { a2 = rw(0, 2); }, [&] { D(0); });
      mm(H, 4, h4, op, [&] { a3 = rw(0, 3); }, [&] { a4 = rw(0, 4); }, [&] { use(o0[NM - 1]); });
      mm(C, 0, a0, o0, [&] { o1[0] = ro(1, 0); o1[1] = ro(1, 1); }, [&] { use(a1); if constexpr (NM == 3) o1[2] = ro(1, 2); }, [&] { D(1); });
      mm(C, 1, a1, o0, [&] { b0 = rw(1, 0); }, [&] { use(a2); b1 = rw(1, 1); }, N0);
      mm(C, 2, a2, o0, [&] { h2 = rw(1, 2); }, [&] { use(a3); }, [&] { D(2); });
      mm(C, 3, a3, o0, [&] { h3 = rw(1, 3); }, [&] { use(a4); }, N0);
      mm(C, 4, a4, o0, [&] { h4 = rw(1, 4); }, [&] { use(b0); }, [&] { D(3); });
      mm(P, 0, b0, o1, N0, [&] { use(b1); }, [&] { D(4); });
      mm(P, 1, b1, o1, [&] { use(h4); }, N0, N0);
      tail();
#pragma unroll
      for (int m = 0; m < NM; ++m) op[m] = o1[m];
    };
    auto ND = [](int) {};
    SB();
    wait_vm_bar<63>();
    interval(IC<-1>{}, ND, N0);
    wait_vm_bar<63>();
    STAMP(7);
    interval(IC<1>{}, [&](int n) { if constexpr (NB == 3) dmal(4, 0, n); }, N0);
    if constexpr (NB == 3) wait_vm_bar<10>(); else wait_vm_bar<0>();
    if (c.w == 3 && lz == 0) __hip_atomic_store(sync + (1 + c.id) * FS, tag, RLX_AGENT);
    interval(IC<2>{}, [&](int n) { if constexpr (NB == 3) dmal(5, 1, n); }, [&] {
      if (c.w == 2) {
        const int pid = c.id ^ (((lz & 3) ? (lz & 3) : 1) << 3);
        for (unsigned spins = 0; spins < (1u << 14); ++spins) {
          if (__all(__hip_atomic_load(sync + (1 + pid) * FS, RLX_AGENT) == tag)) break;
          __builtin_amdgcn_s_sleep(1);
        }
        __builtin_amdgcn_fence(__ATOMIC_ACQUIRE, "agent");
        asm volatile("s_waitcnt vmcnt(0)" ::: "memory");
      }
    });
    if constexpr (NB == 3) wait_vm_bar<10>(); else wait_vm_bar<63>();
    STAMP(8);
    interval(IC<3>{}, [&](int n) { if constexpr (NB == 3) dmal(6, 2, n); else { dmal(4, 0, n); dmal(5, 1, n); } }, N0);
    STAMP(9);
#pragma unroll 1
    for (int i4 = 4; i4 < 16; i4 += 4)
      static_for(std::make_integer_sequence<int, 4>{}, [&](auto UU) {
        constexpr int u = decltype(UU)::value;
        if constexpr (NB == 3) wait_vm_bar<10>(); else { if (u == 0 && i4 == 4) wait_vm_bar<0>(); else wait_vm_bar<5>(); }
        interval(UU, [&](int n) { if constexpr (NB == 3) dmal(i4 + u + 3, (u + 3) & 3, n); else dmal(i4 + u + 2, (u + 2) & 3, n); }, N0);
      });
#pragma unroll
    for (int m = 0; m < NM; ++m) { amfma(acc[m][2], h2, op[m]); amfma(acc[m][3], h3, op[m]); amfma(acc[m][4], h4, op[m]); }
    asm volatile("s_nop 7");
    STAMP(10);
    if (q != hg) {
      const int so = (q * 3 + ((hg - q - 1) & 3)) * QB;
#pragma unroll
      for (int e = 0; e < NM * 5; e += 2) {
        const f32x4 a = acc[e / 5][e % 5], b = e + 1 < NM * 5 ? acc[(e + 1) / 5][(e + 1) % 5] : z4;
        __builtin_amdgcn_raw_buffer_store_b128(u32x4{cvtpk(a[0], a[1]), cvtpk(a[2], a[3]), cvtpk(b[0], b[1]), cvtpk(b[2], b[3])}, rslab, lz * 16 + ((M0 ? 8 : 0) + e / 2) * 1024 + so, 0, 16);
      }
    }
    asm volatile("s_waitcnt vmcnt(0) lgkmcnt(0)\n\ts_barrier" ::: "memory");
    if (c.w == 0 && lz == 0) __hip_atomic_store(sync + (257 + c.id) * FS, tag, RLX_AGENT);
    STAMP(11);
    if (q == hg) {
#pragma unroll
      for (int e = 0; e < NM * 5; ++e) st128(lds0 + lz * 16 + (M0 * 5 + e) * 1024, __builtin_bit_cast(u32x4, acc[e / 5][e % 5]));
    }
    if (c.w == ((hg + 1) & 3)) {
      const int pid = c.id ^ ((lz & 3) ? (lz & 3) : 1);
      for (unsigned spins = 0; spins < (1u << 14); ++spins) {
        if (__all(__hip_atomic_load(sync + (257 + pid) * FS, RLX_AGENT) == tag)) break;
        __builtin_amdgcn_s_sleep(1);
      }
    }
    if (c.id == 0 && c.w == 7) {
      for (unsigned spins = 0; spins < (1u << 14); ++spins) {
        bool ok = true;
#pragma unroll
        for (int k = 0; k < 4; ++k) ok &= __hip_atomic_load(sync + (1 + lz + 64 * k) * FS, RLX_AGENT) == tag;
        if (__all(ok)) break;
        __builtin_amdgcn_s_sleep(1);
      }
      if (lz == 0) __hip_atomic_store(sync, tag, RLX_AGENT);
    }
    asm volatile("s_waitcnt vmcnt(0) lgkmcnt(0)\n\ts_barrier" ::: "memory");
    STAMP(12);
    u16* op16 = ob + (size_t)(lz & 15) * 1280 + 4 * zg;
    u32x4 r[2][3];
#pragma unroll
    for (int k = 0; k < 2; ++k)
#pragma unroll
      for (int sl = 0; sl < 3; ++sl) r[k][sl] = __builtin_amdgcn_raw_buffer_load_b128(rslab, c.w + 8 * k < 13 ? lz * 16 + (c.w + 8 * k) * 1024 : OOB, (hg * 3 + sl) * QB, 16);
#pragma unroll
    for (int k = 0; k < 2; ++k) {
      const int un = c.w + 8 * k;
#pragma unroll
      for (int hh = 0; hh < 2; ++hh) {
        const int e = un < 8 ? 2 * un + hh : 15 + 2 * (un - 8) + hh;
        if (un < 13 && !(un == 7 && hh == 1)) {
          f32x4 v = *(const LDS3 f32x4*)(size_t)(lds0 + lz * 16 + e * 1024);
#pragma unroll
          for (int sl = 0; sl < 3; ++sl) { const unsigned lo = r[k][sl][2 * hh], hi = r[k][sl][2 * hh + 1];
            v += f32x4{__uint_as_float(lo << 16), __uint_as_float(lo & 0xffff0000u), __uint_as_float(hi << 16), __uint_as_float(hi & 0xffff0000u)}; }
          const int m = e / 5, n = e - m * 5;
          *(u32x2*)(op16 + m * (16 * 1280) + n * 16) = u32x2{cvtpk(v[0], v[1]), cvtpk(v[2], v[3])};
        }
      }
    }
  }
}

__global__ __launch_bounds__(512, 2) void fused(const u16* xA, const u16* kA, const u16* vA, const u16* wqA, const u16* woA,
                                                 const u16* xB, const u16* kB, const u16* vB, const u16* wqB, const u16* woB, u16* out, char* ws) {
  extern __shared__ __attribute__((aligned(16))) char smem[];
  const int id = blockIdx.x, xg = id & 7, cc = id >> 3, br = xg >> 2, hg = xg & 3, b = cc >> 2, pr = cc & 3, h0 = hg * 8 + pr * 2;
  const int w = __builtin_amdgcn_readfirstlane(threadIdx.x >> 6);
  asm volatile("s_nop 4");
  Ctx c;
  const u16* x = br ? xB : xA; const u16* k = br ? kB : kA; const u16* v = br ? vB : vA; const u16* wq = br ? wqB : wqA;
  c.rx = mkr(x + (size_t)b * SQ * DM, SQ * DM * 2);
  c.rw = mkr(wq + h0 * DH, DM * NH * DH * 2 - h0 * DH * 2);
  c.rk = mkr(k + (size_t)(b * NH + h0) * SK * DH, 2 * SK * DH * 2);
  c.rv = mkr(v + (size_t)(b * NH + h0) * SK * DH, 2 * SK * DH * 2);
  c.lds0 = (unsigned)(size_t)(LDS3 char*)smem;
  c.hs = w >> 2; c.j = w & 3; c.rot = (id * 5) & 15; c.krot = ((10 * pr + 5 * b) >> 2) % 10;
  c.w = w; c.id = id; c.wo = br ? woB : woA; c.out = out; c.ws = ws;
  const bool big = (c.j < 2) == (c.hs == 0);
  c.blk0 = c.hs == 0 ? (c.j < 2 ? 3 * c.j : 2 + 2 * c.j) : (c.j < 2 ? 2 * c.j : 3 * c.j - 2);
  if (big) body<3>(c); else body<2>(c);
}

extern "C" void kernel_launch(void* const* d_in, const int* in_sizes, int n_in, void* d_out, int out_size, void* d_ws, size_t ws_size, hipStream_t stream) {
  (void)hipFuncSetAttribute((const void*)fused, hipFuncAttributeMaxDynamicSharedMemorySize, LDS_BYTES);
  fused<<<256, 512, LDS_BYTES, stream>>>((const u16*)d_in[0], (const u16*)d_in[1], (const u16*)d_in[2], (const u16*)d_in[3], (const u16*)d_in[4],
                                         (const u16*)d_in[5], (const u16*)d_in[6], (const u16*)d_in[7], (const u16*)d_in[8], (const u16*)d_in[9], (u16*)d_out, (char*)d_ws);
}
```

```cpp
#define DIAG
#define NODIAGH
#include <cstdio>
#include <hip/hip_runtime.h>
#include <hip/hip_bf16.h>
#include <type_traits>

typedef __bf16 bf16x8 __attribute__((ext_vector_type(8)));
typedef __bf16 bf16x4 __attribute__((ext_vector_type(4)));
typedef __bf16 bf16x2 __attribute__((ext_vector_type(2)));
typedef float f32x4 __attribute__((ext_vector_type(4)));
typedef float f32x2 __attribute__((ext_vector_type(2)));
typedef unsigned u32x4 __attribute__((ext_vector_type(4)));
typedef unsigned u32x2 __attribute__((ext_vector_type(2)));
typedef unsigned short u16;
typedef __attribute__((address_space(1))) unsigned long long gu64;
typedef __amdgpu_buffer_rsrc_t rsrc_t;
#define LDS3 __attribute__((address_space(3)))
#define DI __device__ __forceinline__
#define SB() __builtin_amdgcn_sched_barrier(0)
#define RLX_AGENT __ATOMIC_RELAXED, __HIP_MEMORY_SCOPE_AGENT
template <int N> using IC = std::integral_constant<int, N>;

constexpr int SQ = 160, DM = 640, NH = 32, SK = 512, DH = 128;
constexpr int LDS_BYTES = 163840;
constexpr int KV_HS = 32768, V_OFF = 65536;
__host__ __device__ constexpr int xoff(int b) { return b * 65536 + 73728; }
__host__ __device__ constexpr int woff(int b) { return b == 0 ? 16384 : b == 1 ? 49152 : b == 2 ? 106496 : 122880; }
constexpr float QSCALE = 0.08838834764831845f * 1.4426950408889634f;
constexpr float BIAS = 10.f;
constexpr unsigned OOB = 0x7ff00000u;
constexpr size_t WS_SLAB = 32u << 20, WS_SYNC = 64u << 20;
constexpr int FS = 32;
constexpr int QB = 13 * 1024;
#define KVN0 1
#define XNT false
#define WNT true
#define WONT false

DI unsigned cvtpk(float lo, float hi) { f32x2 v = {lo, hi}; bf16x2 b = __builtin_convertvector(v, bf16x2); return __builtin_bit_cast(unsigned, b); }
DI f32x4 mfma(bf16x8 a, bf16x8 b, f32x4 c) { return __builtin_amdgcn_mfma_f32_16x16x32_bf16(a, b, c, 0, 0, 0); }
DI bf16x8 ld128(unsigned addr) { return *(const LDS3 bf16x8*)(size_t)addr; }
DI void st128(unsigned addr, u32x4 v) { *(LDS3 u32x4*)(size_t)addr = v; }
DI bf16x4 ldtr(unsigned addr) { return __builtin_amdgcn_ds_read_tr16_b64_v4bf16((LDS3 bf16x4*)(size_t)addr); }
DI bf16x8 cat(bf16x4 a, bf16x4 b) { return __builtin_shufflevector(a, b, 0, 1, 2, 3, 4, 5, 6, 7); }
DI bf16x8 asb(u32x4 w) { return __builtin_bit_cast(bf16x8, w); }
DI rsrc_t mkr(const void* p, int bytes) { return __builtin_amdgcn_make_buffer_rsrc((void*)p, (short)0, bytes, 0x00020000); }
template <bool NT> DI void dma16(unsigned ldsaddr, unsigned voff, rsrc_t r, unsigned soff) {
  if constexpr (NT) asm volatile("s_mov_b32 m0, %0\n\ts_nop 0\n\tbuffer_load_dwordx4 %1, %2, %3 offen nt lds" :: "s"(ldsaddr), "v"(voff), "s"(r), "s"(soff) : "memory");
  else              asm volatile("s_mov_b32 m0, %0\n\ts_nop 0\n\tbuffer_load_dwordx4 %1, %2, %3 offen lds" :: "s"(ldsaddr), "v"(voff), "s"(r), "s"(soff) : "memory");
}
template <int N> DI void wait_vm_bar() { asm volatile("s_waitcnt vmcnt(%0) lgkmcnt(0)\n\ts_barrier" :: "i"(N) : "memory"); }
DI float xmax16(float v) { auto r = __builtin_amdgcn_permlane16_swap(__float_as_uint(v), __float_as_uint(v), false, false); return fmaxf(__uint_as_float(r[0]), __uint_as_float(r[1])); }
DI float xmax32(float v) { auto r = __builtin_amdgcn_permlane32_swap(__float_as_uint(v), __float_as_uint(v), false, false); return fmaxf(__uint_as_float(r[0]), __uint_as_float(r[1])); }
DI void amfma(f32x4& acc, bf16x8 a, bf16x8 b) { asm volatile("v_mfma_f32_16x16x32_bf16 %0, %1, %2, %0" : "+v"(acc) : "v"(a), "v"(b)); }
DI void amfma_c(f32x4& d, bf16x8 a, bf16x8 b, f32x4 cc) { asm volatile("v_mfma_f32_16x16x32_bf16 %0, %1, %2, %3" : "=&v"(d) : "v"(a), "v"(b), "v"(cc)); }
DI void amfma_z(f32x4& d, bf16x8 a, bf16x8 b) { asm volatile("v_mfma_f32_16x16x32_bf16 %0, %1, %2, 0" : "=&v"(d) : "v"(a), "v"(b)); }
template <bool Z> DI void amf(f32x4& d, bf16x8 a, bf16x8 b) { if constexpr (Z) amfma_z(d, a, b); else amfma(d, a, b); }
DI float aexp(float x) { asm volatile("v_exp_f32 %0, %0" : "+v"(x)); return x; }
DI unsigned aor3(unsigned a, unsigned b, unsigned c) { unsigned r; asm volatile("v_or3_b32 %0, %1, %2, %3" : "=v"(r) : "v"(a), "v"(b), "v"(c)); return r; }
DI unsigned acvt(float lo, float hi) { unsigned r; asm volatile("v_cvt_pk_bf16_f32 %0, %1, %2" : "=v"(r) : "v"(lo), "v"(hi)); return r; }
DI void use(bf16x8 f) { asm volatile("" :: "v"(f)); }
DI void aaddu(unsigned& a, int d) { asm volatile("v_add_u32 %0, %1, %0" : "+v"(a) : "s"(d)); }
DI unsigned laneid() { unsigned l; asm volatile("v_mbcnt_lo_u32_b32 %0, -1, 0\n\tv_mbcnt_hi_u32_b32 %0, -1, %0" : "=v"(l)); return l; }
template <class T> DI void pin(T& v) { asm volatile("" : "+v"(v)); }

#define STAMP(i) do{ unsigned long long a_, b_; asm volatile("s_memrealtime %0\n\ts_memtime %1\n\ts_waitcnt lgkmcnt(0)" : "=s"(a_), "=s"(b_) :: "memory"); st_[i] = (a_ << 32) | (b_ & 0xffffffffull); }while(0)

struct Ctx {
  rsrc_t rx, rw, rk, rv;
  unsigned lds0;
  int j, hs, blk0, rot, krot, w, id;
  const u16* wo; u16* out; char* ws;
};
template <int NB> struct St {
  f32x4 o[NB][8], negm[NB], S0[NB], S1[NB], l[NB];
  bf16x8 qf[NB][4], ones;
  u32x4 pf[NB];
  unsigned orv;
};
template <int NB, int E> DI void exp1(f32x4 (&S)[NB]) { if constexpr (E >= 0 && E < 4 * NB) S[E % NB][E / NB] = aexp(S[E % NB][E / NB]); }
template <int NB, int LO, int HI> DI void exps(f32x4 (&S)[NB]) { if constexpr (LO < HI) { exp1<NB, LO>(S); exps<NB, LO + 1, HI>(S); } }
DI float max8(f32x4 a, f32x4 b) { return fmaxf(fmaxf(fmaxf(a[0], a[1]), fmaxf(a[2], a[3])), fmaxf(fmaxf(b[0], b[1]), fmaxf(b[2], b[3]))); }

template <int NB, bool HAS_NEXT> DI void refix(St<NB>& s, rsrc_t rk, int ksoff) {
  const unsigned ln = laneid();
  const int kvo = (ln & 15) * 256 + ((((ln >> 4) & 1) << 1) | (ln >> 5)) * 16;
  asm volatile("s_nop 7");
#pragma unroll
  for (int nb = 0; nb < NB; ++nb) {
    f32x4 a = s.negm[nb], b = s.negm[nb];
#pragma unroll
    for (int st = 0; st < 4; ++st) {
      a = mfma(asb(__builtin_amdgcn_raw_buffer_load_b128(rk, kvo + st * 64, ksoff, 0)), s.qf[nb][st], a);
      SB();
      b = mfma(asb(__builtin_amdgcn_raw_buffer_load_b128(rk, kvo + st * 64, ksoff + 4096, 0)), s.qf[nb][st], b);
      SB();
    }
    const float d = fmaxf(xmax32(xmax16(max8(a, b))) + BIAS, 0.f);
    const float f = __builtin_amdgcn_exp2f(-d);
    s.negm[nb] -= d; if (HAS_NEXT) s.S0[nb] -= d;
    s.l[nb] *= f;
#pragma unroll
    for (int db = 0; db < 8; ++db) s.o[nb][db] *= f;
#pragma unroll
    for (int r = 0; r < 4; ++r) { a[r] = __builtin_amdgcn_exp2f(a[r] - d); b[r] = __builtin_amdgcn_exp2f(b[r] - d); }
    u32x4 w = {cvtpk(a[0], a[1]), cvtpk(a[2], a[3]), cvtpk(b[0], b[1]), cvtpk(b[2], b[3])};
    s.pf[nb] = w;
    SB();
  }
  asm volatile("s_nop 1");
}
template <int NB, bool HAS_NEXT> DI void check(St<NB>& s, rsrc_t rk, int ksoff) {
  if (__builtin_expect(__any((s.orv & 0x40004000u) != 0u), 0)) refix<NB, HAS_NEXT>(s, rk, ksoff);
}
template <int... I, class F> DI void static_for(std::integer_sequence<int, I...>, F f) { (f(IC<I>{}), ...); }

template <int NB> DI void body(const Ctx& c) {
  unsigned long long st_[16] = {0, 0, 0, 0, 0, 0, 0, 0, 0, 0, 0, 0, 0, 0, 0, 0};
  const int lane = threadIdx.x & 63, l15 = lane & 15, g = lane >> 4;
  const unsigned lds0 = c.lds0;
  const f32x4 z4 = {0.f, 0.f, 0.f, 0.f};
  auto N0 = [] {};
  const unsigned voffK = (lane >> 4) * 256 + (((lane & 15) ^ (4 * c.j + (lane >> 4))) * 16);
  const unsigned voffV = ((lane >> 1) & 7) * 256 + (lane >> 4) * 32 + (lane & 1) * 16;
  auto kvl = [&](int t, int q) { return lds0 + (q >> 1) * KV_HS + (t & 3) * 8192 + (c.j + 4 * (q & 1)) * 1024; };
  auto kso = [&](int t, int q) { return (q >> 1) * 131072 + ((t + c.rot) & 15) * 8192 + (c.j + 4 * (q & 1)) * 1024; };
  auto vso = [&](int t, int q) { const int i = c.j + 4 * (q & 1); return (q >> 1) * 131072 + ((t + c.rot) & 15) * 8192 + (i >> 1) * 2048 + (i & 1) * 128; };
  auto dma_k1 = [&](int t, int q) { dma16<true>(kvl(t, q), voffK, c.rk, t < 16 ? kso(t, q) : OOB); };
  auto dma_v1 = [&](int t, int q) { dma16<true>(V_OFF + kvl(t, q), voffV, c.rv, t < 16 ? vso(t, q) : OOB); };
  STAMP(0);
  St<NB> s;
  const unsigned long long epoch = __hip_atomic_load((gu64*)(c.ws + WS_SYNC), RLX_AGENT);
  u32x4 kvr[16];
  {
    unsigned voffS;
    if constexpr (NB == 3) { const int f = (2 * (c.j & 1) + (lane >> 5)) | ((c.j >> 1) << 2); voffS = (lane >> 5) * 8192 + (((lane & 31) ^ (f << 1)) * 16); }
    else voffS = (lane >> 3) * 1280 + (((lane & 7) ^ (4 * (c.j & 1) + g)) * 16);
    auto dma_w = [&](int st, int buf, int n) {
      int ks = st + 2 * c.krot; ks = ks >= 20 ? ks - 20 : ks;
      const int i = (c.j & 1) + 4 * (c.j >> 1) + 2 * (n & 1) + 8 * (n >> 1);
      dma16<WNT>(lds0 + woff(buf) + i * 1024, voffS, c.rw, st < 20 ? (ks * 32 + 2 * i) * 8192 : OOB);
    };
    auto dma_x = [&](int ch, int n) {
      int kc = ch + c.krot; kc = kc >= 10 ? kc - 10 : kc;
      const int i = c.j + 4 * n;
      dma16<XNT>(lds0 + xoff(ch & 1) + i * 1024, voffS, c.rx, i * (8 * 1280) + kc * 128);
    };
    auto extra = [&](int m, int q) {
      if (m == 0 || m == 1) dma_k1(m, q);
      else if (m == 2) dma_v1(0, q);
      else if (m == 3 || m == 5) kvr[(m - 3) * 4 + q] = __builtin_amdgcn_raw_buffer_load_b128(c.rk, voffK, kso((m + 1) / 2, q), 2);
      else if (m == 4 || m == 6) kvr[(m - 3) * 4 + q] = __builtin_amdgcn_raw_buffer_load_b128(c.rv, voffV, vso((m - 2) / 2, q), 2);
    };
    if constexpr (NB == 3) {
#pragma unroll
      for (int st = 0; st < 3; ++st)
#pragma unroll
        for (int n = 0; n < 4; ++n) dma_w(st, st, n);
    } else {
#pragma unroll
      for (int n = 0; n < 5; ++n) dma_x(0, n);
      __builtin_amdgcn_s_setprio(1);
    }
    unsigned wa[2][8], xa[4];
    const int qq = l15 >> 2, p = lane & 3, f = qq | ((g & 1) << 2);
#pragma unroll
    for (int db = 0; db < 8; ++db) { wa[0][db] = lds0 + woff(0) + (8 * g + qq) * 512 + c.hs * 256 + ((db ^ f) * 32) + p * 8; wa[1][db] = wa[0][db] + (woff(2) - woff(0)); pin(wa[1][db]); }
#pragma unroll
    for (int u = 0; u < 4; ++u) { xa[u] = lds0 + xoff(u >> 1) + (c.blk0 * 16 + l15) * 128 + (((4 * (u & 1) + g) ^ ((l15 >> 1) & 7)) * 16); pin(xa[u]); }
    bf16x8 w4, w5, w6, w7, xp[NB];
    auto qstep = [&](auto U, auto PH, auto D) {
      constexpr int u = decltype(U)::value, ph = decltype(PH)::value;
      auto rw = [&](int db) { const unsigned a = wa[u >> 1][db] + (woff(u) - woff(u & 2)); return cat(ldtr(a), ldtr(a + 2048)); };
      auto rx = [&](int nb) { return ld128(xa[u] + nb * 2048); };
      auto mm = [&](auto Z, int db, bf16x8& wf, bf16x8 (&xf)[NB], auto g0, auto g1, auto g2) {
        constexpr int z = decltype(Z)::value;
        if constexpr (z == 2) { g0(); g1(); g2(); SB(); }
        else {
          amf<z == 1>(s.o[0][db], wf, xf[0]); SB(); g0(); SB();
          amf<z == 1>(s.o[1][db], wf, xf[1]); SB(); g1(); SB();
          if constexpr (NB == 3) { amf<z == 1>(s.o[2][db], wf, xf[2]); SB(); }
          g2(); SB();
        }
      };
      constexpr IC<ph == 0 ? 2 : ph == 1 ? 1 : 0> H{};
      constexpr IC<ph == 0 ? 1 : 0> C{};
      bf16x8 xf[NB], w0, w1, w2, w3;
      if constexpr (NB == 3) {
        mm(H, 4, w4, xp, [&] { w0 = rw(0); }, [&] { xf[0] = rx(0); xf[1] = rx(1); }, [&] { xf[2] = rx(2); });
        mm(H, 5, w5, xp, [&] { w1 = rw(1); }, [&] { D(0); }, N0);
        mm(H, 6, w6, xp, [&] { w2 = rw(2); }, [&] { w3 = rw(3); }, N0);
        mm(H, 7, w7, xp, [&] { w4 = rw(4); }, [&] { D(1); }, [&] { use(xf[NB - 1]); });
        mm(C, 0, w0, xf, [&] { w5 = rw(5); }, [&] { use(w1); w6 = rw(6); }, N0);
        mm(C, 1, w1, xf, [&] { w7 = rw(7); }, [&] { use(w2); D(2); }, N0);
        mm(C, 2, w2, xf, [&] { use(w3); }, [&] { D(3); }, N0);
        mm(C, 3, w3, xf, [&] { use(w7); }, N0, N0);
      } else {
        mm(H, 4, w4, xp, [&] { w0 = rw(0); }, [&] { xf[0] = rx(0); xf[1] = rx(1); }, N0);
        mm(H, 5, w5, xp, [&] { w1 = rw(1); }, [&] { D(0); }, N0);
        mm(H, 6, w6, xp, [&] { w2 = rw(2); }, [&] { w3 = rw(3); D(1); }, N0);
        mm(H, 7, w7, xp, [&] { w4 = rw(4); }, [&] { D(2); use(xf[NB - 1]); }, N0);
        mm(C, 0, w0, xf, [&] { w5 = rw(5); }, [&] { use(w1); w6 = rw(6); D(3); }, N0);
        mm(C, 1, w1, xf, [&] { w7 = rw(7); }, [&] { use(w2); D(4); }, N0);
        mm(C, 2, w2, xf, [&] { use(w3); }, N0, N0);
        mm(C, 3, w3, xf, [&] { use(w7); }, N0, N0);
      }
#pragma unroll
      for (int nb = 0; nb < NB; ++nb) xp[nb] = xf[nb];
    };
    STAMP(1);
    SB();
    if constexpr (NB == 3) {
      static_for(std::make_integer_sequence<int, 4>{}, [&](auto U) {
        constexpr int u = decltype(U)::value;
        wait_vm_bar<8>(); qstep(U, IC<(u < 2 ? u : 2)>{}, [&](int n) { dma_w(u + 3, (u + 3) & 3, n); });
      });
#pragma unroll 1
      for (int s4 = 4; s4 < 20; s4 += 4)
        static_for(std::make_integer_sequence<int, 4>{}, [&](auto U) {
          constexpr int u = decltype(U)::value;
          wait_vm_bar<8>(); qstep(U, IC<2>{}, [&](int n) { dma_w(s4 + u + 3, (u + 3) & 3, n); });
        });
    } else {
      static_for(std::make_integer_sequence<int, 20>{}, [&](auto ST) {
        constexpr int st = decltype(ST)::value, mp = (st >> 1) - 1 - KVN0;
        if constexpr (st & 1) wait_vm_bar<63>(); else wait_vm_bar<(mp >= 0 && mp < 7) ? 4 : 0>();
        qstep(IC<(st & 3)>{}, IC<(st < 2 ? st : 2)>{}, [&](int n) {
          if constexpr (st & 1) { if (n < 4) extra((st >> 1) - KVN0, n); } else if constexpr ((st >> 1) + 1 < 10) dma_x((st >> 1) + 1, n); });
      });
    }
#pragma unroll
    for (int nb = 0; nb < NB; ++nb) { amfma(s.o[nb][4], w4, xp[nb]); amfma(s.o[nb][5], w5, xp[nb]); amfma(s.o[nb][6], w6, xp[nb]); amfma(s.o[nb][7], w7, xp[nb]); }
    asm volatile("s_nop 7");
  }
  STAMP(2);
  wait_vm_bar<0>();
  const unsigned long long tag = (((unsigned long long)__builtin_amdgcn_readfirstlane((unsigned)(epoch >> 32)) << 32) | __builtin_amdgcn_readfirstlane((unsigned)epoch)) + 1ull;
  const int pg = ((g & 1) << 1) | (g >> 1);
  unsigned ka[4];
#pragma unroll
  for (int st = 0; st < 4; ++st) ka[st] = lds0 + c.hs * KV_HS + l15 * 256 + (((4 * st + pg) ^ l15) * 16);
  unsigned va = lds0 + V_OFF + c.hs * KV_HS + (g >> 1) * 2048 + (4 * (g & 1) + (l15 >> 2)) * 32 + (lane & 3) * 8;
  auto rk = [&](int kt, int st) { return ld128(ka[st] + kt * 4096); };
  auto rv = [&](int db) { const unsigned a = va + (db >> 2) * 1024 + (db & 3) * 256; return cat(ldtr(a), ldtr(a + 4096)); };
  {
    bf16x8 kf0[4], kf1[4];
#pragma unroll
    for (int st = 0; st < 4; ++st) { kf0[st] = rk(0, st); kf1[st] = rk(1, st); }
#pragma unroll
    for (int nb = 0; nb < NB; ++nb)
#pragma unroll
      for (int e = 0; e < 4; ++e) {
        const f32x4 a0 = s.o[nb][2 * e] * QSCALE, a1 = s.o[nb][2 * e + 1] * QSCALE;
        auto r0 = __builtin_amdgcn_permlane16_swap(cvtpk(a0[0], a0[1]), cvtpk(a1[0], a1[1]), false, false);
        auto r1 = __builtin_amdgcn_permlane16_swap(cvtpk(a0[2], a0[3]), cvtpk(a1[2], a1[3]), false, false);
        u32x4 w = {r0[0], r1[0], r0[1], r1[1]};
        s.qf[nb][e] = asb(w);
        if constexpr (NB == 2) {
#pragma unroll
          for (int h = 0; h < 2; ++h) { const int i = (nb * 4 + e) * 2 + h, m = i >> 2; st128(((m & 1) ? V_OFF + kvl(1 + (m >> 1), i & 3) : kvl(2 + (m >> 1), i & 3)) + lane * 16, kvr[i]); }
        }
      }
#pragma unroll
    for (int st = 0; st < 4; ++st)
#pragma unroll
      for (int nb = 0; nb < NB; ++nb) { s.S0[nb] = mfma(kf0[st], s.qf[nb][st], st == 0 ? z4 : s.S0[nb]); s.S1[nb] = mfma(kf1[st], s.qf[nb][st], st == 0 ? z4 : s.S1[nb]); }
#pragma unroll
    for (int nb = 0; nb < NB; ++nb) {
#pragma unroll
      for (int db = (NB == 3 ? 6 : 5); db < 8; ++db) { s.o[nb][db] = z4; pin(s.o[nb][db]); }
      const float m = xmax32(xmax16(max8(s.S0[nb], s.S1[nb]))) + BIAS;
      s.negm[nb] = z4 - m; s.S0[nb] -= m; s.S1[nb] -= m;
      pin(s.negm[nb]);
    }
    s.ones = asb(u32x4{0x3f803f80u, 0x3f803f80u, 0x3f803f80u, 0x3f803f80u});
    pin(s.ones);
  }
  exps<NB, 0, 4 * NB>(s.S0); exps<NB, 0, 2 * NB>(s.S1);
  bf16x8 v5h, v6, v7;
#pragma unroll
  for (int st = 0; st < 4; ++st) ka[st] += 8192;
  SB();
  auto us = [&](auto m, auto g0, auto g1, auto g2) {
    m(0); SB(); g0(); SB(); m(1); SB(); g1(); SB();
    if constexpr (NB == 3) { m(2); SB(); g2(); SB(); }
  };
  auto QK0 = [&](int st, bf16x8 fr) { return [&s, st, fr](int nb) { if (st == 0) amfma_c(s.S0[nb], fr, s.qf[nb][0], s.negm[nb]); else amfma(s.S0[nb], fr, s.qf[nb][st]); }; };
  auto QK1 = [&](int st, bf16x8 fr) { return [&s, st, fr](int nb) { if (st == 0) amfma_c(s.S1[nb], fr, s.qf[nb][0], s.negm[nb]); else amfma(s.S1[nb], fr, s.qf[nb][st]); }; };
  auto PVH = [&](int db, bf16x8& fr) { return [&s, db, &fr](int nb) { amfma(s.o[nb][db], fr, asb(s.pf[nb])); }; };
  auto N = [] {};
#define E0(e) [&] { exp1<NB, (e)>(s.S0); }
#define E1(e) [&] { exp1<NB, (e)>(s.S1); }
#define CLO(nb) [&] { if constexpr ((nb) < NB) { s.pf[nb][0] = acvt(s.S0[nb][0], s.S0[nb][1]); s.pf[nb][1] = acvt(s.S0[nb][2], s.S0[nb][3]); } }
#define CHI(nb) [&] { if constexpr ((nb) < NB) { s.pf[nb][2] = acvt(s.S1[nb][0], s.S1[nb][1]); s.pf[nb][3] = acvt(s.S1[nb][2], s.S1[nb][3]); } }
#define U(f, ...) [&] { use(f); SB(); (__VA_ARGS__)(); }
  auto astep = [&](auto FI, int t, auto kvh) {
    constexpr bool FIRST = decltype(FI)::value;
    const int dk = (t & 3) == 3 ? -24576 : 8192, dv = (t & 3) == 0 ? -24576 : 8192;
    if constexpr (NB == 2) wait_vm_bar<16>(); else wait_vm_bar<63>();
    auto PH = [&](int db, bf16x8& fr) { return [&, db](int nb) { if constexpr (!FIRST) amfma(s.o[nb][db], fr, asb(s.pf[nb])); }; };
    auto PV = [&](int db, bf16x8 fr) { return [&s, db, fr](int nb) { amf<FIRST>(s.o[nb][db], fr, asb(s.pf[nb])); }; };
    auto SUM = [&](int nb) { amf<FIRST>(s.l[nb], s.ones, asb(s.pf[nb])); };
    bf16x8 a0, a1, a2, a3, b0, b1, b2, b3, v0, v1, v2, v3, v4;
    unsigned o1, o2;
    if constexpr (NB == 3) {
      bf16x8 v5;
      us(PH(6, v6), [&] { a0 = rk(0, 0); a1 = rk(0, 1); }, [&] { a2 = rk(0, 2); a3 = rk(0, 3); }, E1(6));
      us(PH(7, v7), CLO(0), CLO(1), U(a0, CLO(2)));
      us(QK0(0, a0), [&] { b0 = rk(1, 0); b1 = rk(1, 1); }, E1(7), U(a1, E1(8)));
      us(QK0(1, a1), E1(9), E1(10), U(a2, E1(11)));
      us(QK0(2, a2), [&] { o1 = aor3(s.pf[0][0], s.pf[0][1], s.pf[1][0]); o2 = aor3(s.pf[1][1], s.pf[2][0], s.pf[2][1]); }, CHI(0), U(a3, CHI(1)));
      us(QK0(3, a3), CHI(2), [&] { o1 = aor3(o1, s.pf[0][2], s.pf[0][3]); o2 = aor3(o2, s.pf[1][2], s.pf[1][3]); },
                     U(b0, [&] { o1 = aor3(o1, s.pf[2][2], s.pf[2][3]); s.orv = o1 | o2; }));
      if constexpr (!FIRST) check<NB, true>(s, c.rk, c.hs * 131072 + ((t - 1 + c.rot) & 15) * 8192);
      SB();
      us(QK1(0, b0), [&] { b2 = rk(1, 2); b3 = rk(1, 3); }, [&] { v0 = rv(0); }, U(b1, [&] { aaddu(ka[0], dk); aaddu(ka[1], dk); }));
      us(QK1(1, b1), [&] { v1 = rv(1); }, E0(0), U(b2, E0(1)));
      us(QK1(2, b2), [&] { v2 = rv(2); }, E0(2), U(b3, E0(3)));
      us(QK1(3, b3), [&] { v3 = rv(3); }, E0(4), E0(5));
      us(SUM, E0(6), E0(7), U(v0, E0(8)));
      us(PV(0, v0), [&] { v4 = rv(4); }, E0(9), U(v1, E0(10)));
      us(PV(1, v1), [&] { v5 = rv(5); }, E0(11), U(v2, E1(0)));
      us(PV(2, v2), [&] { v6 = rv(6); }, E1(1), U(v3, E1(2)));
      us(PV(3, v3), [&] { v7 = rv(7); }, E1(3), U(v4, E1(4)));
      us(PV(4, v4), E1(5), [&] { aaddu(ka[2], dk); aaddu(ka[3], dk); }, U(v5, [&] { aaddu(va, dv); }));
      us(PV(5, v5), N, [&] { use(v7); }, N);
    } else {
      us(PH(5, v5h), [&] { a0 = rk(0, 0); a1 = rk(0, 1); }, [&] { a2 = rk(0, 2); a3 = rk(0, 3); }, N);
      us(PH(6, v6), E1(4), [&] { kvh(0); }, N);
      us(PH(7, v7), CLO(0), U(a0, CLO(1)), N);
      us(QK0(0, a0), [&] { b0 = rk(1, 0); b1 = rk(1, 1); }, U(a1, E1(5)), N);
      us(QK0(1, a1), E1(6), U(a2, E1(7)), N);
      us(QK0(2, a2), [&] { o1 = aor3(s.pf[0][0], s.pf[0][1], s.pf[1][0]); kvh(1); }, U(a3, CHI(0)), N);
      us(QK0(3, a3), CHI(1), U(b0, [&] { o1 = aor3(o1, s.pf[1][1], s.pf[0][2]); o2 = aor3(s.pf[0][3], s.pf[1][2], s.pf[1][3]); s.orv = o1 | o2; }), N);
      if constexpr (!FIRST) check<NB, true>(s, c.rk, c.hs * 131072 + ((t - 1 + c.rot) & 15) * 8192);
      SB();
      us(QK1(0, b0), [&] { b2 = rk(1, 2); b3 = rk(1, 3); }, U(b1, [&] { v0 = rv(0); kvh(2); }), N);
      us(QK1(1, b1), [&] { v1 = rv(1); }, U(b2, [&] { aaddu(ka[0], dk); aaddu(ka[1], dk); kvh(3); }), N);
      us(QK1(2, b2), [&] { v2 = rv(2); }, U(b3, E0(0)), N);
      us(QK1(3, b3), [&] { v3 = rv(3); }, [&] { exp1<NB, 1>(s.S0); kvh(4); }, N);
      us(SUM, E0(2), U(v0, E0(3)), N);
      us(PV(0, v0), [&] { v4 = rv(4); }, U(v1, [&] { exp1<NB, 4>(s.S0); kvh(5); }), N);
      us(PV(1, v1), [&] { v5h = rv(5); }, U(v2, E0(5)), N);
      us(PV(2, v2), [&] { v6 = rv(6); aaddu(ka[2], dk); aaddu(ka[3], dk); }, U(v3, [&] { exp1<NB, 6>(s.S0); kvh(6); }), N);
      us(PV(3, v3), [&] { v7 = rv(7); exp1<NB, 7>(s.S0); }, U(v4, [&] { exp1<NB, 0>(s.S1); kvh(7); }), N);
      us(PV(4, v4), [&] { exp1<NB, 1>(s.S1); exp1<NB, 2>(s.S1); }, [&] { exp1<NB, 3>(s.S1); aaddu(va, dv); use(v7); }, N);
    }
  };
  auto kv = [&](int t, int n) { if constexpr (NB == 2) { if (n < 4) dma_k1(t + 3, n); else dma_v1(t + 2, n - 4); } };
  STAMP(3);
  astep(std::true_type{}, 1, [&](int n) { kv(1, n); });
#pragma unroll 1
  for (int t = 2; t < (NB == 3 ? 16 : 14); ++t) astep(std::false_type{}, t, [&](int n) { kv(t, n); });
  rsrc_t rof, rwo, rslab; int pr, hg, rotb; gu64* sync; u16* ob;
  {
    int id = c.id; asm volatile("" : "+s"(id));
    const int br = (id >> 2) & 1, b = id >> 5;
    hg = id & 3; pr = (id >> 3) & 3; rotb = b & 3;
    rof = mkr(c.ws + (size_t)((br * 8 + b) * 4 + hg) * 327680, 327680);
    rwo = mkr(c.wo + (size_t)hg * 1024 * 640 + pr * 160, 1024 * 1280 - pr * 320);
    rslab = mkr(c.ws + WS_SLAB + (size_t)((br * 8 + b) * 4 + pr) * (12 * QB), 12 * QB);
    sync = (gu64*)(c.ws + WS_SYNC);
    ob = c.out + (size_t)(b * 160 + (hg & 1) * 80) * 1280 + br * 640 + pr * 160 + (hg >> 1) * 80;
  }
  unsigned lz = 0;
  const int dks = c.j >> 1, dh = c.j & 1;
  auto stg = [](int u) { return (u >> 1) * 81920 + ((u ^ (u >> 1)) & 1) * 40960; };
  auto kpair = [&](int i) { return ((pr + (i >> 2)) & 3) * 4 + ((i + rotb) & 3); };
  auto wvo = [&](int n) { const int e = 64 * n + (int)lz, row = e / 20, ch = e - row * 20; return row * 1280 + ((ch ^ (((row >> 2) & 1) << 1)) * 16); };
  auto wso = [&](int i) { return (kpair(i) * 64 + dks * 32 + dh * 16) * 1280; };
  auto wld = [&](int i, int n) { return lds0 + stg(i & 3) + dks * 20480 + 10240 + dh * 5120 + n * 1024; };
  u32x4 wr[15];
  if constexpr (NB == 2) {
    lz = laneid();
    unsigned vw[5];
#pragma unroll
    for (int n = 0; n < 5; ++n) vw[n] = wvo(n);
    auto wl = [&](int p) { if (p < 15) wr[p] = __builtin_amdgcn_raw_buffer_load_b128(rwo, vw[p % 5], wso(p / 5), WONT ? 2 : 0); };
    SB();
    astep(std::false_type{}, 14, [&](int n) { wl(n); });
    astep(std::false_type{}, 15, [&](int n) { wl(8 + n); });
  }
  STAMP(4);
  if constexpr (NB == 3) wait_vm_bar<63>(); else wait_vm_bar<15>();
  {
    bf16x8 vf[8];
#pragma unroll
    for (int db = 0; db < 4; ++db) vf[db] = rv(db);
    SB();
    if constexpr (NB == 2) us(PVH(5, v5h), N, N, N);
    us(PVH(6, v6), N, N, N);
    exps<NB, 2 * NB, 4 * NB>(s.S1);
    us(PVH(7, v7), CLO(0), CLO(1), CLO(2));
    CHI(0)(); CHI(1)(); CHI(2)();
    s.orv = 0;
#pragma unroll
    for (int nb = 0; nb < NB; ++nb) { s.orv = aor3(s.orv, s.pf[nb][0], s.pf[nb][1]); s.orv = aor3(s.orv, s.pf[nb][2], s.pf[nb][3]); }
    SB();
    check<NB, false>(s, c.rk, c.hs * 131072 + ((15 + c.rot) & 15) * 8192);
    SB();
    if constexpr (NB == 3) lz = laneid();
#pragma unroll
    for (int db = 4; db < 8; ++db) vf[db] = rv(db);
#pragma unroll
    for (int nb = 0; nb < NB; ++nb) s.l[nb] = mfma(s.ones, asb(s.pf[nb]), s.l[nb]);
#pragma unroll
    for (int db = 0; db < 8; ++db)
#pragma unroll
      for (int nb = 0; nb < NB; ++nb) {
        s.o[nb][db] = mfma(vf[db], asb(s.pf[nb]), s.o[nb][db]);
        if constexpr (NB == 2) { const int p = db * 2 + nb; if (p < 15) { SB(); st128(wld(p / 5, p % 5) + lz * 16, wr[p]); SB(); } }
      }
  }
  STAMP(5);
  asm volatile("s_waitcnt lgkmcnt(0)\n\ts_barrier" ::: "memory");
#pragma unroll
  for (int nb = 0; nb < NB; ++nb) {
    const float inv = __builtin_amdgcn_rcpf(s.l[nb][0]);
#pragma unroll
    for (int e = 0; e < 4; ++e) {
      const f32x4 a0 = s.o[nb][2 * e] * inv, a1 = s.o[nb][2 * e + 1] * inv;
      const u32x4 w = {cvtpk(a0[0], a0[1]), cvtpk(a0[2], a0[3]), cvtpk(a1[0], a1[1]), cvtpk(a1[2], a1[3])};
      const int u = (c.hs * 2 + (e >> 1) - rotb) & 3;
      st128(lds0 + stg(u) + (e & 1) * 20480 + (c.blk0 + nb) * 1024 + lz * 16, w);
      __builtin_amdgcn_raw_buffer_store_b128(w, rof, lz * 16 + (pr * 8 + c.hs * 4 + e) * 10240 + (c.blk0 + nb) * 1024, 0, 16);
    }
  }
  STAMP(6);
  {
    constexpr int NM = NB, M0 = NM == 3 ? 0 : 3;
    const int q = c.j, qm = q & 1, qn = q >> 1, zg = lz >> 4;
    unsigned vo3[5];
#pragma unroll
    for (int n = 0; n < 5; ++n) vo3[n] = NB == 3 ? wvo(n) : lz * 16;
    auto dmal = [&](int i, int u, int n) {
      if constexpr (NB == 3) dma16<WONT>(wld(u, n), vo3[n], rwo, i < 16 ? wso(i) : OOB);
      else dma16<false>(lds0 + stg(u) + dks * 20480 + dh * 5120 + n * 1024, vo3[0], rof, i < 16 ? (kpair(i) * 2 + dks) * 10240 + (dh * 5 + n) * 1024 : OOB);
    };
    if constexpr (NB == 3) {
#pragma unroll
      for (int n = 0; n < 5; ++n) dmal(3, 3, n);
    }
    unsigned oa[2], wa[2][2];
    {
      oa[0] = lds0 + (qm * 5 + M0) * 1024 + lz * 16; oa[1] = oa[0] + 81920;
      const unsigned wb = lds0 + 10240 + (4 * zg + ((lz & 15) >> 2)) * 320 + (lz & 3) * 8 + qn * 160;
      const int sg = (qn ? -32 : 32) * (zg & 1);
      wa[0][0] = wb + sg; wa[0][1] = wb - sg; wa[1][0] = wa[0][0] + 81920; wa[1][1] = wa[0][1] + 81920;
      pin(oa[1]); pin(wa[1][0]); pin(wa[1][1]);
    }
    f32x4 acc[NM][5];
    bf16x8 h2, h3, h4, op[NM];
    auto interval = [&](auto UU, auto D, auto tail) {
      constexpr bool FIRST = decltype(UU)::value < 0;
      constexpr int u = FIRST ? 0 : decltype(UU)::value, so = ((u ^ (u >> 1)) & 1) * 40960;
      auto ro = [&](int ks, int m) { return ld128(oa[u >> 1] + so + ks * 20480 + m * 1024); };
      auto rw = [&](int ks, int n) { const unsigned a = wa[u >> 1][n & 1] + so + ks * 20480 + n * 32; return cat(ldtr(a), ldtr(a + 5120)); };
      auto mm = [&](auto Z, int n, bf16x8& wf, bf16x8 (&of)[NM], auto g0, auto g1, auto g2) {
        constexpr int z = decltype(Z)::value;
        if constexpr (z == 2) { g0(); g1(); g2(); SB(); }
        else {
          amf<z == 1>(acc[0][n], wf, of[0]); SB(); g0(); SB(); amf<z == 1>(acc[1][n], wf, of[1]); SB(); g1(); SB();
          if constexpr (NM == 3) { amf<z == 1>(acc[2][n], wf, of[2]); SB(); }
          g2(); SB();
        }
      };
      constexpr IC<FIRST ? 2 : 0> H{};
      constexpr IC<FIRST ? 1 : 0> C{};
      constexpr IC<0> P{};
      bf16x8 o0[NM], o1[NM], a0, a1, a2, a3, a4, b0, b1;
      mm(H, 2, h2, op, [&] { a0 = rw(0, 0); }, [&] { o0[0] = ro(0, 0); o0[1] = ro(0, 1); }, [&] { if constexpr (NM == 3) o0[2] = ro(0, 2); });
      mm(H, 3, h3, op, [&] { a1 = rw(0, 1); }, [&] { a2 = rw(0, 2); }, [&] { D(0); });
      mm(H, 4, h4, op, [&] { a3 = rw(0, 3); }, [&] { a4 = rw(0, 4); }, [&] { use(o0[NM - 1]); });
      mm(C, 0, a0, o0, [&] { o1[0] = ro(1, 0); o1[1] = ro(1, 1); }, [&] { use(a1); if constexpr (NM == 3) o1[2] = ro(1, 2); }, [&] { D(1); });
      mm(C, 1, a1, o0, [&] { b0 = rw(1, 0); }, [&] { use(a2); b1 = rw(1, 1); }, N0);
      mm(C, 2, a2, o0, [&] { h2 = rw(1, 2); }, [&] { use(a3); }, [&] { D(2); });
      mm(C, 3, a3, o0, [&] { h3 = rw(1, 3); }, [&] { use(a4); }, N0);
      mm(C, 4, a4, o0, [&] { h4 = rw(1, 4); }, [&] { use(b0); }, [&] { D(3); });
      mm(P, 0, b0, o1, N0, [&] { use(b1); }, [&] { D(4); });
      mm(P, 1, b1, o1, [&] { use(h4); }, N0, N0);
      tail();
#pragma unroll
      for (int m = 0; m < NM; ++m) op[m] = o1[m];
    };
    auto ND = [](int) {};
    SB();
    wait_vm_bar<63>();
    interval(IC<-1>{}, ND, N0);
    wait_vm_bar<63>();
    STAMP(7);
    interval(IC<1>{}, [&](int n) { if constexpr (NB == 3) dmal(4, 0, n); }, N0);
    if constexpr (NB == 3) wait_vm_bar<10>(); else wait_vm_bar<0>();
    if (c.w == 3 && lz == 0) __hip_atomic_store(sync + (1 + c.id) * FS, tag, RLX_AGENT);
    interval(IC<2>{}, [&](int n) { if constexpr (NB == 3) dmal(5, 1, n); }, [&] {
      if (c.w == 2) {
        const int pid = c.id ^ (((lz & 3) ? (lz & 3) : 1) << 3);
        for (unsigned spins = 0; spins < (1u << 14); ++spins) {
          if (__all(__hip_atomic_load(sync + (1 + pid) * FS, RLX_AGENT) == tag)) break;
          __builtin_amdgcn_s_sleep(1);
        }
        __builtin_amdgcn_fence(__ATOMIC_ACQUIRE, "agent");
        asm volatile("s_waitcnt vmcnt(0)" ::: "memory");
      }
    });
    if constexpr (NB == 3) wait_vm_bar<10>(); else wait_vm_bar<63>();
    STAMP(8);
    interval(IC<3>{}, [&](int n) { if constexpr (NB == 3) dmal(6, 2, n); else { dmal(4, 0, n); dmal(5, 1, n); } }, N0);
    STAMP(9);
#pragma unroll 1
    for (int i4 = 4; i4 < 16; i4 += 4)
      static_for(std::make_integer_sequence<int, 4>{}, [&](auto UU) {
        constexpr int u = decltype(UU)::value;
        if constexpr (NB == 3) wait_vm_bar<10>(); else { if (u == 0 && i4 == 4) wait_vm_bar<0>(); else wait_vm_bar<5>(); }
        interval(UU, [&](int n) { if constexpr (NB == 3) dmal(i4 + u + 3, (u + 3) & 3, n); else dmal(i4 + u + 2, (u + 2) & 3, n); }, N0);
      });
#pragma unroll
    for (int m = 0; m < NM; ++m) { amfma(acc[m][2], h2, op[m]); amfma(acc[m][3], h3, op[m]); amfma(acc[m][4], h4, op[m]); }
    asm volatile("s_nop 7");
    STAMP(10);
    if (q != hg) {
      const int so = (q * 3 + ((hg - q - 1) & 3)) * QB;
#pragma unroll
      for (int e = 0; e < NM * 5; e += 2) {
        const f32x4 a = acc[e / 5][e % 5], b = e + 1 < NM * 5 ? acc[(e + 1) / 5][(e + 1) % 5] : z4;
        __builtin_amdgcn_raw_buffer_store_b128(u32x4{cvtpk(a[0], a[1]), cvtpk(a[2], a[3]), cvtpk(b[0], b[1]), cvtpk(b[2], b[3])}, rslab, lz * 16 + ((M0 ? 8 : 0) + e / 2) * 1024 + so, 0, 16);
      }
    }
    asm volatile("s_waitcnt vmcnt(0) lgkmcnt(0)\n\ts_barrier" ::: "memory");
    if (c.w == 0 && lz == 0) __hip_atomic_store(sync + (257 + c.id) * FS, tag, RLX_AGENT);
    STAMP(11);
    if (q == hg) {
#pragma unroll
      for (int e = 0; e < NM * 5; ++e) st128(lds0 + lz * 16 + (M0 * 5 + e) * 1024, __builtin_bit_cast(u32x4, acc[e / 5][e % 5]));
    }
    if (c.w == ((hg + 1) & 3)) {
      const int pid = c.id ^ ((lz & 3) ? (lz & 3) : 1);
      for (unsigned spins = 0; spins < (1u << 14); ++spins) {
        if (__all(__hip_atomic_load(sync + (257 + pid) * FS, RLX_AGENT) == tag)) break;
        __builtin_amdgcn_s_sleep(1);
      }
    }
    if (c.id == 0 && c.w == 7) {
      for (unsigned spins = 0; spins < (1u << 14); ++spins) {
        bool ok = true;
#pragma unroll
        for (int k = 0; k < 4; ++k) ok &= __hip_atomic_load(sync + (1 + lz + 64 * k) * FS, RLX_AGENT) == tag;
        if (__all(ok)) break;
        __builtin_amdgcn_s_sleep(1);
      }
      if (lz == 0) __hip_atomic_store(sync, tag, RLX_AGENT);
    }
    asm volatile("s_waitcnt vmcnt(0) lgkmcnt(0)\n\ts_barrier" ::: "memory");
    STAMP(12);
    u16* op16 = ob + (size_t)(lz & 15) * 1280 + 4 * zg;
    u32x4 r[2][3];
#pragma unroll
    for (int k = 0; k < 2; ++k)
#pragma unroll
      for (int sl = 0; sl < 3; ++sl) r[k][sl] = __builtin_amdgcn_raw_buffer_load_b128(rslab, c.w + 8 * k < 13 ? lz * 16 + (c.w + 8 * k) * 1024 : OOB, (hg * 3 + sl) * QB, 16);
#pragma unroll
    for (int k = 0; k < 2; ++k) {
      const int un = c.w + 8 * k;
#pragma unroll
      for (int hh = 0; hh < 2; ++hh) {
        const int e = un < 8 ? 2 * un + hh : 15 + 2 * (un - 8) + hh;
        if (un < 13 && !(un == 7 && hh == 1)) {
          f32x4 v = *(const LDS3 f32x4*)(size_t)(lds0 + lz * 16 + e * 1024);
#pragma unroll
          for (int sl = 0; sl < 3; ++sl) { const unsigned lo = r[k][sl][2 * hh], hi = r[k][sl][2 * hh + 1];
            v += f32x4{__uint_as_float(lo << 16), __uint_as_float(lo & 0xffff0000u), __uint_as_float(hi << 16), __uint_as_float(hi & 0xffff0000u)}; }
          const int m = e / 5, n = e - m * 5;
          *(u32x2*)(op16 + m * (16 * 1280) + n * 16) = u32x2{cvtpk(v[0], v[1]), cvtpk(v[2], v[3])};
        }
      }
    }
  }
  asm volatile("s_waitcnt vmcnt(0)" ::: "memory"); STAMP(13);
  if (lz == 0 && c.w == 0 && (c.id < 16 || (c.id & 31) == 5)) { unsigned b0 = (unsigned)(st_[0] >> 32);
    printf("wg%d xcc%d t0=%u: %u %u %u %u %u %u %u %u %u %u %u %u %u\n", c.id, __builtin_amdgcn_s_getreg((3 << 11) | 20) & 15, b0 & 0xffffff, (unsigned)(st_[1] >> 32) - b0, (unsigned)(st_[2] >> 32) - b0, (unsigned)(st_[3] >> 32) - b0, (unsigned)(st_[4] >> 32) - b0,
      (unsigned)(st_[5] >> 32) - b0, (unsigned)(st_[6] >> 32) - b0, (unsigned)(st_[7] >> 32) - b0, (unsigned)(st_[8] >> 32) - b0, (unsigned)(st_[9] >> 32) - b0, (unsigned)(st_[10] >> 32) - b0, (unsigned)(st_[11] >> 32) - b0, (unsigned)(st_[12] >> 32) - b0, (unsigned)(st_[13] >> 32) - b0); }
}

__global__ __launch_bounds__(512, 2) void fused(const u16* xA, const u16* kA, const u16* vA, const u16* wqA, const u16* woA,
                                                 const u16* xB, const u16* kB, const u16* vB, const u16* wqB, const u16* woB, u16* out, char* ws) {
  extern __shared__ __attribute__((aligned(16))) char smem[];
  const int id = blockIdx.x, xg = id & 7, cc = id >> 3, br = xg >> 2, hg = xg & 3, b = cc >> 2, pr = cc & 3, h0 = hg * 8 + pr * 2;
  const int w = __builtin_amdgcn_readfirstlane(threadIdx.x >> 6);
  asm volatile("s_nop 4");
  Ctx c;
  const u16* x = br ? xB : xA; const u16* k = br ? kB : kA; const u16* v = br ? vB : vA; const u16* wq = br ? wqB : wqA;
  c.rx = mkr(x + (size_t)b * SQ * DM, SQ * DM * 2);
  c.rw = mkr(wq + h0 * DH, DM * NH * DH * 2 - h0 * DH * 2);
  c.rk = mkr(k + (size_t)(b * NH + h0) * SK * DH, 2 * SK * DH * 2);
  c.rv = mkr(v + (size_t)(b * NH + h0) * SK * DH, 2 * SK * DH * 2);
  c.lds0 = (unsigned)(size_t)(LDS3 char*)smem;
  c.hs = w >> 2; c.j = w & 3; c.rot = (id * 5) & 15; c.krot = ((10 * pr + 5 * b) >> 2) % 10;
  c.w = w; c.id = id; c.wo = br ? woB : woA; c.out = out; c.ws = ws;
  const bool big = (c.j < 2) == (c.hs == 0);
  c.blk0 = c.hs == 0 ? (c.j < 2 ? 3 * c.j : 2 + 2 * c.j) : (c.j < 2 ? 2 * c.j : 3 * c.j - 2);
  if (big) body<3>(c); else body<2>(c);
}

extern "C" void kernel_launch(void* const* d_in, const int* in_sizes, int n_in, void* d_out, int out_size, void* d_ws, size_t ws_size, hipStream_t stream) {
  (void)hipFuncSetAttribute((const void*)fused, hipFuncAttributeMaxDynamicSharedMemorySize, LDS_BYTES);
  fused<<<256, 512, LDS_BYTES, stream>>>((const u16*)d_in[0], (const u16*)d_in[1], (const u16*)d_in[2], (const u16*)d_in[3], (const u16*)d_in[4],
                                         (const u16*)d_in[5], (const u16*)d_in[6], (const u16*)d_in[7], (const u16*)d_in[8], (const u16*)d_in[9], (u16*)d_out, (char*)d_ws);
}
```

```cpp
#include <hip/hip_runtime.h>
#include <hip/hip_bf16.h>
#include <type_traits>

typedef __bf16 bf16x8 __attribute__((ext_vector_type(8)));
typedef __bf16 bf16x4 __attribute__((ext_vector_type(4)));
typedef __bf16 bf16x2 __attribute__((ext_vector_type(2)));
typedef float f32x4 __attribute__((ext_vector_type(4)));
typedef float f32x2 __attribute__((ext_vector_type(2)));
typedef unsigned u32x4 __attribute__((ext_vector_type(4)));
typedef unsigned u32x2 __attribute__((ext_vector_type(2)));
typedef unsigned short u16;
typedef __attribute__((address_space(1))) unsigned long long gu64;
typedef __amdgpu_buffer_rsrc_t rsrc_t;
#define LDS3 __attribute__((address_space(3)))
#define DI __device__ __forceinline__
#define SB() __builtin_amdgcn_sched_barrier(0)
#define RLX_AGENT __ATOMIC_RELAXED, __HIP_MEMORY_SCOPE_AGENT
template <int N> using IC = std::integral_constant<int, N>;

constexpr int SQ = 160, DM = 640, NH = 32, SK = 512, DH = 128;
constexpr int LDS_BYTES = 163840;
constexpr int KV_HS = 32768, V_OFF = 65536;
__host__ __device__ constexpr int xoff(int b) { return b * 65536 + 73728; }
__host__ __device__ constexpr int woff(int b) { return b == 0 ? 16384 : b == 1 ? 49152 : b == 2 ? 106496 : 122880; }
constexpr float QSCALE = 0.08838834764831845f * 1.4426950408889634f;
constexpr float BIAS = 10.f;
constexpr unsigned OOB = 0x7ff00000u;
constexpr size_t WS_SLAB = 32u << 20, WS_SYNC = 64u << 20;
constexpr int FS = 32;
constexpr int QB = 13 * 1024;
#define KVN0 1
#define XNT false
#define WNT true
#define WONT false

DI unsigned cvtpk(float lo, float hi) { f32x2 v = {lo, hi}; bf16x2 b = __builtin_convertvector(v, bf16x2); return __builtin_bit_cast(unsigned, b); }
DI f32x4 mfma(bf16x8 a, bf16x8 b, f32x4 c) { return __builtin_amdgcn_mfma_f32_16x16x32_bf16(a, b, c, 0, 0, 0); }
DI bf16x8 ld128(unsigned addr) { return *(const LDS3 bf16x8*)(size_t)addr; }
DI void st128(unsigned addr, u32x4 v) { *(LDS3 u32x4*)(size_t)addr = v; }
DI bf16x4 ldtr(unsigned addr) { return __builtin_amdgcn_ds_read_tr16_b64_v4bf16((LDS3 bf16x4*)(size_t)addr); }
DI bf16x8 cat(bf16x4 a, bf16x4 b) { return __builtin_shufflevector(a, b, 0, 1, 2, 3, 4, 5, 6, 7); }
DI bf16x8 asb(u32x4 w) { return __builtin_bit_cast(bf16x8, w); }
DI rsrc_t mkr(const void* p, int bytes) { return __builtin_amdgcn_make_buffer_rsrc((void*)p, (short)0, bytes, 0x00020000); }
template <bool NT> DI void dma16(unsigned ldsaddr, unsigned voff, rsrc_t r, unsigned soff) {
  if constexpr (NT) asm volatile("s_mov_b32 m0, %0\n\ts_nop 0\n\tbuffer_load_dwordx4 %1, %2, %3 offen nt lds" :: "s"(ldsaddr), "v"(voff), "s"(r), "s"(soff) : "memory");
  else              asm volatile("s_mov_b32 m0, %0\n\ts_nop 0\n\tbuffer_load_dwordx4 %1, %2, %3 offen lds" :: "s"(ldsaddr), "v"(voff), "s"(r), "s"(soff) : "memory");
}
template <int N> DI void wait_vm_bar() { asm volatile("s_waitcnt vmcnt(%0) lgkmcnt(0)\n\ts_barrier" :: "i"(N) : "memory"); }
DI float xmax16(float v) { auto r = __builtin_amdgcn_permlane16_swap(__float_as_uint(v), __float_as_uint(v), false, false); return fmaxf(__uint_as_float(r[0]), __uint_as_float(r[1])); }
DI float xmax32(float v) { auto r = __builtin_amdgcn_permlane32_swap(__float_as_uint(v), __float_as_uint(v), false, false); return fmaxf(__uint_as_float(r[0]), __uint_as_float(r[1])); }
DI void amfma(f32x4& acc, bf16x8 a, bf16x8 b) { asm volatile("v_mfma_f32_16x16x32_bf16 %0, %1, %2, %0" : "+v"(acc) : "v"(a), "v"(b)); }
DI void amfma_c(f32x4& d, bf16x8 a, bf16x8 b, f32x4 cc) { asm volatile("v_mfma_f32_16x16x32_bf16 %0, %1, %2, %3" : "=&v"(d) : "v"(a), "v"(b), "v"(cc)); }
DI void amfma_z(f32x4& d, bf16x8 a, bf16x8 b) { asm volatile("v_mfma_f32_16x16x32_bf16 %0, %1, %2, 0" : "=&v"(d) : "v"(a), "v"(b)); }
template <bool Z> DI void amf(f32x4& d, bf16x8 a, bf16x8 b) { if constexpr (Z) amfma_z(d, a, b); else amfma(d, a, b); }
DI float aexp(float x) { asm volatile("v_exp_f32 %0, %0" : "+v"(x)); return x; }
DI unsigned aor3(unsigned a, unsigned b, unsigned c) { unsigned r; asm volatile("v_or3_b32 %0, %1, %2, %3" : "=v"(r) : "v"(a), "v"(b), "v"(c)); return r; }
DI unsigned acvt(float lo, float hi) { unsigned r; asm volatile("v_cvt_pk_bf16_f32 %0, %1, %2" : "=v"(r) : "v"(lo), "v"(hi)); return r; }
DI void use(bf16x8 f) { asm volatile("" :: "v"(f)); }
DI void aaddu(unsigned& a, int d) { asm volatile("v_add_u32 %0, %1, %0" : "+v"(a) : "s"(d)); }
DI unsigned laneid() { unsigned l; asm volatile("v_mbcnt_lo_u32_b32 %0, -1, 0\n\tv_mbcnt_hi_u32_b32 %0, -1, %0" : "=v"(l)); return l; }
template <class T> DI void pin(T& v) { asm volatile("" : "+v"(v)); }

#define STAMP(i)

struct Ctx {
  rsrc_t rx, rw, rk, rv;
  unsigned lds0;
  int j, hs, blk0, rot, krot, w, id;
  const u16* wo; u16* out; char* ws;
};
template <int NB> struct St {
  f32x4 o[NB][8], negm[NB], S0[NB], S1[NB], l[NB];
  bf16x8 qf[NB][4], ones;
  u32x4 pf[NB];
  unsigned orv;
};
template <int NB, int E> DI void exp1(f32x4 (&S)[NB]) { if constexpr (E >= 0 && E < 4 * NB) S[E % NB][E / NB] = aexp(S[E % NB][E / NB]); }
template <int NB, int LO, int HI> DI void exps(f32x4 (&S)[NB]) { if constexpr (LO < HI) { exp1<NB, LO>(S); exps<NB, LO + 1, HI>(S); } }
DI float max8(f32x4 a, f32x4 b) { return fmaxf(fmaxf(fmaxf(a[0], a[1]), fmaxf(a[2], a[3])), fmaxf(fmaxf(b[0], b[1]), fmaxf(b[2], b[3]))); }

template <int NB, bool HAS_NEXT> DI void refix(St<NB>& s, rsrc_t rk, int ksoff) {
  const unsigned ln = laneid();
  const int kvo = (ln & 15) * 256 + ((((ln >> 4) & 1) << 1) | (ln >> 5)) * 16;
  asm volatile("s_nop 7");
#pragma unroll
  for (int nb = 0; nb < NB; ++nb) {
    f32x4 a = s.negm[nb], b = s.negm[nb];
#pragma unroll
    for (int st = 0; st < 4; ++st) {
      a = mfma(asb(__builtin_amdgcn_raw_buffer_load_b128(rk, kvo + st * 64, ksoff, 0)), s.qf[nb][st], a);
      SB();
      b = mfma(asb(__builtin_amdgcn_raw_buffer_load_b128(rk, kvo + st * 64, ksoff + 4096, 0)), s.qf[nb][st], b);
      SB();
    }
    const float d = fmaxf(xmax32(xmax16(max8(a, b))) + BIAS, 0.f);
    const float f = __builtin_amdgcn_exp2f(-d);
    s.negm[nb] -= d; if (HAS_NEXT) s.S0[nb] -= d;
    s.l[nb] *= f;
#pragma unroll
    for (int db = 0; db < 8; ++db) s.o[nb][db] *= f;
#pragma unroll
    for (int r = 0; r < 4; ++r) { a[r] = __builtin_amdgcn_exp2f(a[r] - d); b[r] = __builtin_amdgcn_exp2f(b[r] - d); }
    u32x4 w = {cvtpk(a[0], a[1]), cvtpk(a[2], a[3]), cvtpk(b[0], b[1]), cvtpk(b[2], b[3])};
    s.pf[nb] = w;
    SB();
  }
  asm volatile("s_nop 1");
}
template <int NB, bool HAS_NEXT> DI void check(St<NB>& s, rsrc_t rk, int ksoff) {
  if (__builtin_expect(__any((s.orv & 0x40004000u) != 0u), 0)) refix<NB, HAS_NEXT>(s, rk, ksoff);
}
template <int... I, class F> DI void static_for(std::integer_sequence<int, I...>, F f) { (f(IC<I>{}), ...); }

template <int NB> DI void body(const Ctx& c) {
  const int lane = threadIdx.x & 63, l15 = lane & 15, g = lane >> 4;
  const unsigned lds0 = c.lds0;
  const f32x4 z4 = {0.f, 0.f, 0.f, 0.f};
  auto N0 = [] {};
  const unsigned voffK = (lane >> 4) * 256 + (((lane & 15) ^ (4 * c.j + (lane >> 4))) * 16);
  const unsigned voffV = ((lane >> 1) & 7) * 256 + (lane >> 4) * 32 + (lane & 1) * 16;
  auto kvl = [&](int t, int q) { return lds0 + (q >> 1) * KV_HS + (t & 3) * 8192 + (c.j + 4 * (q & 1)) * 1024; };
  auto kso = [&](int t, int q) { return (q >> 1) * 131072 + ((t + c.rot) & 15) * 8192 + (c.j + 4 * (q & 1)) * 1024; };
  auto vso = [&](int t, int q) { const int i = c.j + 4 * (q & 1); return (q >> 1) * 131072 + ((t + c.rot) & 15) * 8192 + (i >> 1) * 2048 + (i & 1) * 128; };
  auto dma_k1 = [&](int t, int q) { dma16<true>(kvl(t, q), voffK, c.rk, t < 16 ? kso(t, q) : OOB); };
  auto dma_v1 = [&](int t, int q) { dma16<true>(V_OFF + kvl(t, q), voffV, c.rv, t < 16 ? vso(t, q) : OOB); };
  STAMP(0);
  St<NB> s;
  const unsigned long long epoch = __hip_atomic_load((gu64*)(c.ws + WS_SYNC), RLX_AGENT);
  u32x4 kvr[16];
  {
    unsigned voffS;
    if constexpr (NB == 3) { const int f = (2 * (c.j & 1) + (lane >> 5)) | ((c.j >> 1) << 2); voffS = (lane >> 5) * 8192 + (((lane & 31) ^ (f << 1)) * 16); }
    else voffS = (lane >> 3) * 1280 + (((lane & 7) ^ (4 * (c.j & 1) + g)) * 16);
    auto dma_w = [&](int st, int buf, int n) {
      int ks = st + 2 * c.krot; ks = ks >= 20 ? ks - 20 : ks;
      const int i = (c.j & 1) + 4 * (c.j >> 1) + 2 * (n & 1) + 8 * (n >> 1);
      dma16<WNT>(lds0 + woff(buf) + i * 1024, voffS, c.rw, st < 20 ? (ks * 32 + 2 * i) * 8192 : OOB);
    };
    auto dma_x = [&](int ch, int n) {
      int kc = ch + c.krot; kc = kc >= 10 ? kc - 10 : kc;
      const int i = c.j + 4 * n;
      dma16<XNT>(lds0 + xoff(ch & 1) + i * 1024, voffS, c.rx, i * (8 * 1280) + kc * 128);
    };
    auto extra = [&](int m, int q) {
      if (m == 0 || m == 1) dma_k1(m, q);
      else if (m == 2) dma_v1(0, q);
      else if (m == 3 || m == 5) kvr[(m - 3) * 4 + q] = __builtin_amdgcn_raw_buffer_load_b128(c.rk, voffK, kso((m + 1) / 2, q), 2);
      else if (m == 4 || m == 6) kvr[(m - 3) * 4 + q] = __builtin_amdgcn_raw_buffer_load_b128(c.rv, voffV, vso((m - 2) / 2, q), 2);
    };
    if constexpr (NB == 3) {
#pragma unroll
      for (int st = 0; st < 3; ++st)
#pragma unroll
        for (int n = 0; n < 4; ++n) dma_w(st, st, n);
    } else {
#pragma unroll
      for (int n = 0; n < 5; ++n) dma_x(0, n);
      __builtin_amdgcn_s_setprio(1);
    }
    unsigned wa[2][8], xa[4];
    const int qq = l15 >> 2, p = lane & 3, f = qq | ((g & 1) << 2);
#pragma unroll
    for (int db = 0; db < 8; ++db) { wa[0][db] = lds0 + woff(0) + (8 * g + qq) * 512 + c.hs * 256 + ((db ^ f) * 32) + p * 8; wa[1][db] = wa[0][db] + (woff(2) - woff(0)); pin(wa[1][db]); }
#pragma unroll
    for (int u = 0; u < 4; ++u) { xa[u] = lds0 + xoff(u >> 1) + (c.blk0 * 16 + l15) * 128 + (((4 * (u & 1) + g) ^ ((l15 >> 1) & 7)) * 16); pin(xa[u]); }
    bf16x8 w4, w5, w6, w7, xp[NB];
    auto qstep = [&](auto U, auto PH, auto D) {
      constexpr int u = decltype(U)::value, ph = decltype(PH)::value;
      auto rw = [&](int db) { const unsigned a = wa[u >> 1][db] + (woff(u) - woff(u & 2)); return cat(ldtr(a), ldtr(a + 2048)); };
      auto rx = [&](int nb) { return ld128(xa[u] + nb * 2048); };
      auto mm = [&](auto Z, int db, bf16x8& wf, bf16x8 (&xf)[NB], auto g0, auto g1, auto g2) {
        constexpr int z = decltype(Z)::value;
        if constexpr (z == 2) { g0(); g1(); g2(); SB(); }
        else {
          amf<z == 1>(s.o[0][db], wf, xf[0]); SB(); g0(); SB();
          amf<z == 1>(s.o[1][db], wf, xf[1]); SB(); g1(); SB();
          if constexpr (NB == 3) { amf<z == 1>(s.o[2][db], wf, xf[2]); SB(); }
          g2(); SB();
        }
      };
      constexpr IC<ph == 0 ? 2 : ph == 1 ? 1 : 0> H{};
      constexpr IC<ph == 0 ? 1 : 0> C{};
      bf16x8 xf[NB], w0, w1, w2, w3;
      if constexpr (NB == 3) {
        mm(H, 4, w4, xp, [&] { w0 = rw(0); }, [&] { xf[0] = rx(0); xf[1] = rx(1); }, [&] { xf[2] = rx(2); });
        mm(H, 5, w5, xp, [&] { w1 = rw(1); }, [&] { D(0); }, N0);
        mm(H, 6, w6, xp, [&] { w2 = rw(2); }, [&] { w3 = rw(3); }, N0);
        mm(H, 7, w7, xp, [&] { w4 = rw(4); }, [&] { D(1); }, [&] { use(xf[NB - 1]); });
        mm(C, 0, w0, xf, [&] { w5 = rw(5); }, [&] { use(w1); w6 = rw(6); }, N0);
        mm(C, 1, w1, xf, [&] { w7 = rw(7); }, [&] { use(w2); D(2); }, N0);
        mm(C, 2, w2, xf, [&] { use(w3); }, [&] { D(3); }, N0);
        mm(C, 3, w3, xf, [&] { use(w7); }, N0, N0);
      } else {
        mm(H, 4, w4, xp, [&] { w0 = rw(0); }, [&] { xf[0] = rx(0); xf[1] = rx(1); }, N0);
        mm(H, 5, w5, xp, [&] { w1 = rw(1); }, [&] { D(0); }, N0);
        mm(H, 6, w6, xp, [&] { w2 = rw(2); }, [&] { w3 = rw(3); D(1); }, N0);
        mm(H, 7, w7, xp, [&] { w4 = rw(4); }, [&] { D(2); use(xf[NB - 1]); }, N0);
        mm(C, 0, w0, xf, [&] { w5 = rw(5); }, [&] { use(w1); w6 = rw(6); D(3); }, N0);
        mm(C, 1, w1, xf, [&] { w7 = rw(7); }, [&] { use(w2); D(4); }, N0);
        mm(C, 2, w2, xf, [&] { use(w3); }, N0, N0);
        mm(C, 3, w3, xf, [&] { use(w7); }, N0, N0);
      }
#pragma unroll
      for (int nb = 0; nb < NB; ++nb) xp[nb] = xf[nb];
    };
    STAMP(1);
    SB();
    if constexpr (NB == 3) {
      static_for(std::make_integer_sequence<int, 4>{}, [&](auto U) {
        constexpr int u = decltype(U)::value;
        wait_vm_bar<8>(); qstep(U, IC<(u < 2 ? u : 2)>{}, [&](int n) { dma_w(u + 3, (u + 3) & 3, n); });
      });
#pragma unroll 1
      for (int s4 = 4; s4 < 20; s4 += 4)
        static_for(std::make_integer_sequence<int, 4>{}, [&](auto U) {
          constexpr int u = decltype(U)::value;
          wait_vm_bar<8>(); qstep(U, IC<2>{}, [&](int n) { dma_w(s4 + u + 3, (u + 3) & 3, n); });
        });
    } else {
      static_for(std::make_integer_sequence<int, 20>{}, [&](auto ST) {
        constexpr int st = decltype(ST)::value, mp = (st >> 1) - 1 - KVN0;
        if constexpr (st & 1) wait_vm_bar<63>(); else wait_vm_bar<(mp >= 0 && mp < 7) ? 4 : 0>();
        qstep(IC<(st & 3)>{}, IC<(st < 2 ? st : 2)>{}, [&](int n) {
          if constexpr (st & 1) { if (n < 4) extra((st >> 1) - KVN0, n); } else if constexpr ((st >> 1) + 1 < 10) dma_x((st >> 1) + 1, n); });
      });
    }
#pragma unroll
    for (int nb = 0; nb < NB; ++nb) { amfma(s.o[nb][4], w4, xp[nb]); amfma(s.o[nb][5], w5, xp[nb]); amfma(s.o[nb][6], w6, xp[nb]); amfma(s.o[nb][7], w7, xp[nb]); }
    asm volatile("s_nop 7");
  }
  STAMP(2);
  wait_vm_bar<0>();
  const unsigned long long tag = (((unsigned long long)__builtin_amdgcn_readfirstlane((unsigned)(epoch >> 32)) << 32) | __builtin_amdgcn_readfirstlane((unsigned)epoch)) + 1ull;
  const int pg = ((g & 1) << 1) | (g >> 1);
  unsigned ka[4];
#pragma unroll
  for (int st = 0; st < 4; ++st) ka[st] = lds0 + c.hs * KV_HS + l15 * 256 + (((4 * st + pg) ^ l15) * 16);
  unsigned va = lds0 + V_OFF + c.hs * KV_HS + (g >> 1) * 2048 + (4 * (g & 1) + (l15 >> 2)) * 32 + (lane & 3) * 8;
  auto rk = [&](int kt, int st) { return ld128(ka[st] + kt * 4096); };
  auto rv = [&](int db) { const unsigned a = va + (db >> 2) * 1024 + (db & 3) * 256; return cat(ldtr(a), ldtr(a + 4096)); };
  {
    bf16x8 kf0[4], kf1[4];
#pragma unroll
    for (int st = 0; st < 4; ++st) { kf0[st] = rk(0, st); kf1[st] = rk(1, st); }
#pragma unroll
    for (int nb = 0; nb < NB; ++nb)
#pragma unroll
      for (int e = 0; e < 4; ++e) {
        const f32x4 a0 = s.o[nb][2 * e] * QSCALE, a1 = s.o[nb][2 * e + 1] * QSCALE;
        auto r0 = __builtin_amdgcn_permlane16_swap(cvtpk(a0[0], a0[1]), cvtpk(a1[0], a1[1]), false, false);
        auto r1 = __builtin_amdgcn_permlane16_swap(cvtpk(a0[2], a0[3]), cvtpk(a1[2], a1[3]), false, false);
        u32x4 w = {r0[0], r1[0], r0[1], r1[1]};
        s.qf[nb][e] = asb(w);
        if constexpr (NB == 2) {
#pragma unroll
          for (int h = 0; h < 2; ++h) { const int i = (nb * 4 + e) * 2 + h, m = i >> 2; st128(((m & 1) ? V_OFF + kvl(1 + (m >> 1), i & 3) : kvl(2 + (m >> 1), i & 3)) + lane * 16, kvr[i]); }
        }
      }
#pragma unroll
    for (int st = 0; st < 4; ++st)
#pragma unroll
      for (int nb = 0; nb < NB; ++nb) { s.S0[nb] = mfma(kf0[st], s.qf[nb][st], st == 0 ? z4 : s.S0[nb]); s.S1[nb] = mfma(kf1[st], s.qf[nb][st], st == 0 ? z4 : s.S1[nb]); }
#pragma unroll
    for (int nb = 0; nb < NB; ++nb) {
#pragma unroll
      for (int db = (NB == 3 ? 6 : 5); db < 8; ++db) { s.o[nb][db] = z4; pin(s.o[nb][db]); }
      const float m = xmax32(xmax16(max8(s.S0[nb], s.S1[nb]))) + BIAS;
      s.negm[nb] = z4 - m; s.S0[nb] -= m; s.S1[nb] -= m;
      pin(s.negm[nb]);
    }
    s.ones = asb(u32x4{0x3f803f80u, 0x3f803f80u, 0x3f803f80u, 0x3f803f80u});
    pin(s.ones);
  }
  exps<NB, 0, 4 * NB>(s.S0); exps<NB, 0, 2 * NB>(s.S1);
  bf16x8 v5h, v6, v7;
#pragma unroll
  for (int st = 0; st < 4; ++st) ka[st] += 8192;
  SB();
  auto us = [&](auto m, auto g0, auto g1, auto g2) {
    m(0); SB(); g0(); SB(); m(1); SB(); g1(); SB();
    if constexpr (NB == 3) { m(2); SB(); g2(); SB(); }
  };
  auto QK0 = [&](int st, bf16x8 fr) { return [&s, st, fr](int nb) { if (st == 0) amfma_c(s.S0[nb], fr, s.qf[nb][0], s.negm[nb]); else amfma(s.S0[nb], fr, s.qf[nb][st]); }; };
  auto QK1 = [&](int st, bf16x8 fr) { return [&s, st, fr](int nb) { if (st == 0) amfma_c(s.S1[nb], fr, s.qf[nb][0], s.negm[nb]); else amfma(s.S1[nb], fr, s.qf[nb][st]); }; };
  auto PVH = [&](int db, bf16x8& fr) { return [&s, db, &fr](int nb) { amfma(s.o[nb][db], fr, asb(s.pf[nb])); }; };
  auto N = [] {};
#define E0(e) [&] { exp1<NB, (e)>(s.S0); }
#define E1(e) [&] { exp1<NB, (e)>(s.S1); }
#define CLO(nb) [&] { if constexpr ((nb) < NB) { s.pf[nb][0] = acvt(s.S0[nb][0], s.S0[nb][1]); s.pf[nb][1] = acvt(s.S0[nb][2], s.S0[nb][3]); } }
#define CHI(nb) [&] { if constexpr ((nb) < NB) { s.pf[nb][2] = acvt(s.S1[nb][0], s.S1[nb][1]); s.pf[nb][3] = acvt(s.S1[nb][2], s.S1[nb][3]); } }
#define U(f, ...) [&] { use(f); SB(); (__VA_ARGS__)(); }
  auto astep = [&](auto FI, int t, auto kvh) {
    constexpr bool FIRST = decltype(FI)::value;
    const int dk = (t & 3) == 3 ? -24576 : 8192, dv = (t & 3) == 0 ? -24576 : 8192;
    if constexpr (NB == 2) wait_vm_bar<16>(); else wait_vm_bar<63>();
    auto PH = [&](int db, bf16x8& fr) { return [&, db](int nb) { if constexpr (!FIRST) amfma(s.o[nb][db], fr, asb(s.pf[nb])); }; };
    auto PV = [&](int db, bf16x8 fr) { return [&s, db, fr](int nb) { amf<FIRST>(s.o[nb][db], fr, asb(s.pf[nb])); }; };
    auto SUM = [&](int nb) { amf<FIRST>(s.l[nb], s.ones, asb(s.pf[nb])); };
    bf16x8 a0, a1, a2, a3, b0, b1, b2, b3, v0, v1, v2, v3, v4;
    unsigned o1, o2;
    if constexpr (NB == 3) {
      bf16x8 v5;
      us(PH(6, v6), [&] { a0 = rk(0, 0); a1 = rk(0, 1); }, [&] { a2 = rk(0, 2); a3 = rk(0, 3); }, E1(6));
      us(PH(7, v7), CLO(0), CLO(1), U(a0, CLO(2)));
      us(QK0(0, a0), [&] { b0 = rk(1, 0); b1 = rk(1, 1); }, E1(7), U(a1, E1(8)));
      us(QK0(1, a1), E1(9), E1(10), U(a2, E1(11)));
      us(QK0(2, a2), [&] { o1 = aor3(s.pf[0][0], s.pf[0][1], s.pf[1][0]); o2 = aor3(s.pf[1][1], s.pf[2][0], s.pf[2][1]); }, CHI(0), U(a3, CHI(1)));
      us(QK0(3, a3), CHI(2), [&] { o1 = aor3(o1, s.pf[0][2], s.pf[0][3]); o2 = aor3(o2, s.pf[1][2], s.pf[1][3]); },
                     U(b0, [&] { o1 = aor3(o1, s.pf[2][2], s.pf[2][3]); s.orv = o1 | o2; }));
      if constexpr (!FIRST) check<NB, true>(s, c.rk, c.hs * 131072 + ((t - 1 + c.rot) & 15) * 8192);
      SB();
      us(QK1(0, b0), [&] { b2 = rk(1, 2); b3 = rk(1, 3); }, [&] { v0 = rv(0); }, U(b1, [&] { aaddu(ka[0], dk); aaddu(ka[1], dk); }));
      us(QK1(1, b1), [&] { v1 = rv(1); }, E0(0), U(b2, E0(1)));
      us(QK1(2, b2), [&] { v2 = rv(2); }, E0(2), U(b3, E0(3)));
      us(QK1(3, b3), [&] { v3 = rv(3); }, E0(4), E0(5));
      us(SUM, E0(6), E0(7), U(v0, E0(8)));
      us(PV(0, v0), [&] { v4 = rv(4); }, E0(9), U(v1, E0(10)));
      us(PV(1, v1), [&] { v5 = rv(5); }, E0(11), U(v2, E1(0)));
      us(PV(2, v2), [&] { v6 = rv(6); }, E1(1), U(v3, E1(2)));
      us(PV(3, v3), [&] { v7 = rv(7); }, E1(3), U(v4, E1(4)));
      us(PV(4, v4), E1(5), [&] { aaddu(ka[2], dk); aaddu(ka[3], dk); }, U(v5, [&] { aaddu(va, dv); }));
      us(PV(5, v5), N, [&] { use(v7); }, N);
    } else {
      us(PH(5, v5h), [&] { a0 = rk(0, 0); a1 = rk(0, 1); }, [&] { a2 = rk(0, 2); a3 = rk(0, 3); }, N);
      us(PH(6, v6), E1(4), [&] { kvh(0); }, N);
      us(PH(7, v7), CLO(0), U(a0, CLO(1)), N);
      us(QK0(0, a0), [&] { b0 = rk(1, 0); b1 = rk(1, 1); }, U(a1, E1(5)), N);
      us(QK0(1, a1), E1(6), U(a2, E1(7)), N);
      us(QK0(2, a2), [&] { o1 = aor3(s.pf[0][0], s.pf[0][1], s.pf[1][0]); kvh(1); }, U(a3, CHI(0)), N);
      us(QK0(3, a3), CHI(1), U(b0, [&] { o1 = aor3(o1, s.pf[1][1], s.pf[0][2]); o2 = aor3(s.pf[0][3], s.pf[1][2], s.pf[1][3]); s.orv = o1 | o2; }), N);
      if constexpr (!FIRST) check<NB, true>(s, c.rk, c.hs * 131072 + ((t - 1 + c.rot) & 15) * 8192);
      SB();
      us(QK1(0, b0), [&] { b2 = rk(1, 2); b3 = rk(1, 3); }, U(b1, [&] { v0 = rv(0); kvh(2); }), N);
      us(QK1(1, b1), [&] { v1 = rv(1); }, U(b2, [&] { aaddu(ka[0], dk); aaddu(ka[1], dk); kvh(3); }), N);
      us(QK1(2, b2), [&] { v2 = rv(2); }, U(b3, E0(0)), N);
      us(QK1(3, b3), [&] { v3 = rv(3); }, [&] { exp1<NB, 1>(s.S0); kvh(4); }, N);
      us(SUM, E0(2), U(v0, E0(3)), N);
      us(PV(0, v0), [&] { v4 = rv(4); }, U(v1, [&] { exp1<NB, 4>(s.S0); kvh(5); }), N);
      us(PV(1, v1), [&] { v5h = rv(5); }, U(v2, E0(5)), N);
      us(PV(2, v2), [&] { v6 = rv(6); aaddu(ka[2], dk); aaddu(ka[3], dk); }, U(v3, [&] { exp1<NB, 6>(s.S0); kvh(6); }), N);
      us(PV(3, v3), [&] { v7 = rv(7); exp1<NB, 7>(s.S0); }, U(v4, [&] { exp1<NB, 0>(s.S1); kvh(7); }), N);
      us(PV(4, v4), [&] { exp1<NB, 1>(s.S1); exp1<NB, 2>(s.S1); }, [&] { exp1<NB, 3>(s.S1); aaddu(va, dv); use(v7); }, N);
    }
  };
  auto kv = [&](int t, int n) { if constexpr (NB == 2) { if (n < 4) dma_k1(t + 3, n); else dma_v1(t + 2, n - 4); } };
  STAMP(3);
  astep(std::true_type{}, 1, [&](int n) { kv(1, n); });
#pragma unroll 1
  for (int t = 2; t < (NB == 3 ? 16 : 14); ++t) astep(std::false_type{}, t, [&](int n) { kv(t, n); });
  rsrc_t rof, rwo, rslab; int pr, hg, rotb; gu64* sync; u16* ob;
  {
    int id = c.id; asm volatile("" : "+s"(id));
    const int br = (id >> 2) & 1, b = id >> 5;
    hg = id & 3; pr = (id >> 3) & 3; rotb = b & 3;
    rof = mkr(c.ws + (size_t)((br * 8 + b) * 4 + hg) * 327680, 327680);
    rwo = mkr(c.wo + (size_t)hg * 1024 * 640 + pr * 160, 1024 * 1280 - pr * 320);
    rslab = mkr(c.ws + WS_SLAB + (size_t)((br * 8 + b) * 4 + pr) * (12 * QB), 12 * QB);
    sync = (gu64*)(c.ws + WS_SYNC);
    ob = c.out + (size_t)(b * 160 + (hg & 1) * 80) * 1280 + br * 640 + pr * 160 + (hg >> 1) * 80;
  }
  unsigned lz = 0;
  const int dks = c.j >> 1, dh = c.j & 1;
  auto stg = [](int u) { return (u >> 1) * 81920 + ((u ^ (u >> 1)) & 1) * 40960; };
  auto kpair = [&](int i) { return ((pr + (i >> 2)) & 3) * 4 + ((i + rotb) & 3); };
  auto wvo = [&](int n) { const int e = 64 * n + (int)lz, row = e / 20, ch = e - row * 20; return row * 1280 + ((ch ^ (((row >> 2) & 1) << 1)) * 16); };
  auto wso = [&](int i) { return (kpair(i) * 64 + dks * 32 + dh * 16) * 1280; };
  auto wld = [&](int i, int n) { return lds0 + stg(i & 3) + dks * 20480 + 10240 + dh * 5120 + n * 1024; };
  u32x4 wr[15];
  if constexpr (NB == 2) {
    lz = laneid();
    unsigned vw[5];
#pragma unroll
    for (int n = 0; n < 5; ++n) vw[n] = wvo(n);
    auto wl = [&](int p) { if (p < 15) wr[p] = __builtin_amdgcn_raw_buffer_load_b128(rwo, vw[p % 5], wso(p / 5), WONT ? 2 : 0); };
    SB();
    astep(std::false_type{}, 14, [&](int n) { wl(n); });
    astep(std::false_type{}, 15, [&](int n) { wl(8 + n); });
  }
  STAMP(4);
  if constexpr (NB == 3) wait_vm_bar<63>(); else wait_vm_bar<15>();
  {
    bf16x8 vf[8];
#pragma unroll
    for (int db = 0; db < 4; ++db) vf[db] = rv(db);
    SB();
    if constexpr (NB == 2) us(PVH(5, v5h), N, N, N);
    us(PVH(6, v6), N, N, N);
    exps<NB, 2 * NB, 4 * NB>(s.S1);
    us(PVH(7, v7), CLO(0), CLO(1), CLO(2));
    CHI(0)(); CHI(1)(); CHI(2)();
    s.orv = 0;
#pragma unroll
    for (int nb = 0; nb < NB; ++nb) { s.orv = aor3(s.orv, s.pf[nb][0], s.pf[nb][1]); s.orv = aor3(s.orv, s.pf[nb][2], s.pf[nb][3]); }
    SB();
    check<NB, false>(s, c.rk, c.hs * 131072 + ((15 + c.rot) & 15) * 8192);
    SB();
    if constexpr (NB == 3) lz = laneid();
#pragma unroll
    for (int db = 4; db < 8; ++db) vf[db] = rv(db);
#pragma unroll
    for (int nb = 0; nb < NB; ++nb) s.l[nb] = mfma(s.ones, asb(s.pf[nb]), s.l[nb]);
#pragma unroll
    for (int db = 0; db < 8; ++db)
#pragma unroll
      for (int nb = 0; nb < NB; ++nb) {
        s.o[nb][db] = mfma(vf[db], asb(s.pf[nb]), s.o[nb][db]);
        if constexpr (NB == 2) { const int p = db * 2 + nb; if (p < 15) { SB(); st128(wld(p / 5, p % 5) + lz * 16, wr[p]); SB(); } }
      }
  }
  STAMP(5);
  asm volatile("s_waitcnt lgkmcnt(0)\n\ts_barrier" ::: "memory");
#pragma unroll
  for (int nb = 0; nb < NB; ++nb) {
    const float inv = __builtin_amdgcn_rcpf(s.l[nb][0]);
#pragma unroll
    for (int e = 0; e < 4; ++e) {
      const f32x4 a0 = s.o[nb][2 * e] * inv, a1 = s.o[nb][2 * e + 1] * inv;
      const u32x4 w = {cvtpk(a0[0], a0[1]), cvtpk(a0[2], a0[3]), cvtpk(a1[0], a1[1]), cvtpk(a1[2], a1[3])};
      const int u = (c.hs * 2 + (e >> 1) - rotb) & 3;
      st128(lds0 + stg(u) + (e & 1) * 20480 + (c.blk0 + nb) * 1024 + lz * 16, w);
      __builtin_amdgcn_raw_buffer_store_b128(w, rof, lz * 16 + (pr * 8 + c.hs * 4 + e) * 10240 + (c.blk0 + nb) * 1024, 0, 16);
    }
  }
  STAMP(6);
  {
    constexpr int NM = NB, M0 = NM == 3 ? 0 : 3;
    const int q = c.j, qm = q & 1, qn = q >> 1, zg = lz >> 4;
    unsigned vo3[5];
#pragma unroll
    for (int n = 0; n < 5; ++n) vo3[n] = NB == 3 ? wvo(n) : lz * 16;
    auto dmal = [&](int i, int u, int n) {
      if constexpr (NB == 3) dma16<WONT>(wld(u, n), vo3[n], rwo, i < 16 ? wso(i) : OOB);
      else dma16<false>(lds0 + stg(u) + dks * 20480 + dh * 5120 + n * 1024, vo3[0], rof, i < 16 ? (kpair(i) * 2 + dks) * 10240 + (dh * 5 + n) * 1024 : OOB);
    };
    if constexpr (NB == 3) {
#pragma unroll
      for (int n = 0; n < 5; ++n) dmal(3, 3, n);
    }
    unsigned oa[2], wa[2][2];
    {
      oa[0] = lds0 + (qm * 5 + M0) * 1024 + lz * 16; oa[1] = oa[0] + 81920;
      const unsigned wb = lds0 + 10240 + (4 * zg + ((lz & 15) >> 2)) * 320 + (lz & 3) * 8 + qn * 160;
      const int sg = (qn ? -32 : 32) * (zg & 1);
      wa[0][0] = wb + sg; wa[0][1] = wb - sg; wa[1][0] = wa[0][0] + 81920; wa[1][1] = wa[0][1] + 81920;
      pin(oa[1]); pin(wa[1][0]); pin(wa[1][1]);
    }
    f32x4 acc[NM][5];
    bf16x8 h2, h3, h4, op[NM];
    auto interval = [&](auto UU, auto D, auto tail) {
      constexpr bool FIRST = decltype(UU)::value < 0;
      constexpr int u = FIRST ? 0 : decltype(UU)::value, so = ((u ^ (u >> 1)) & 1) * 40960;
      auto ro = [&](int ks, int m) { return ld128(oa[u >> 1] + so + ks * 20480 + m * 1024); };
      auto rw = [&](int ks, int n) { const unsigned a = wa[u >> 1][n & 1] + so + ks * 20480 + n * 32; return cat(ldtr(a), ldtr(a + 5120)); };
      auto mm = [&](auto Z, int n, bf16x8& wf, bf16x8 (&of)[NM], auto g0, auto g1, auto g2) {
        constexpr int z = decltype(Z)::value;
        if constexpr (z == 2) { g0(); g1(); g2(); SB(); }
        else {
          amf<z == 1>(acc[0][n], wf, of[0]); SB(); g0(); SB(); amf<z == 1>(acc[1][n], wf, of[1]); SB(); g1(); SB();
          if constexpr (NM == 3) { amf<z == 1>(acc[2][n], wf, of[2]); SB(); }
          g2(); SB();
        }
      };
      constexpr IC<FIRST ? 2 : 0> H{};
      constexpr IC<FIRST ? 1 : 0> C{};
      constexpr IC<0> P{};
      bf16x8 o0[NM], o1[NM], a0, a1, a2, a3, a4, b0, b1;
      mm(H, 2, h2, op, [&] { a0 = rw(0, 0); }, [&] { o0[0] = ro(0, 0); o0[1] = ro(0, 1); }, [&] { if constexpr (NM == 3) o0[2] = ro(0, 2); });
      mm(H, 3, h3, op, [&] { a1 = rw(0, 1); }, [&] { a2 = rw(0, 2); }, [&] { D(0); });
      mm(H, 4, h4, op, [&] { a3 = rw(0, 3); }, [&] { a4 = rw(0, 4); }, [&] { use(o0[NM - 1]); });
      mm(C, 0, a0, o0, [&] { o1[0] = ro(1, 0); o1[1] = ro(1, 1); }, [&] { use(a1); if constexpr (NM == 3) o1[2] = ro(1, 2); }, [&] { D(1); });
      mm(C, 1, a1, o0, [&] { b0 = rw(1, 0); }, [&] { use(a2); b1 = rw(1, 1); }, N0);
      mm(C, 2, a2, o0, [&] { h2 = rw(1, 2); }, [&] { use(a3); }, [&] { D(2); });
      mm(C, 3, a3, o0, [&] { h3 = rw(1, 3); }, [&] { use(a4); }, N0);
      mm(C, 4, a4, o0, [&] { h4 = rw(1, 4); }, [&] { use(b0); }, [&] { D(3); });
      mm(P, 0, b0, o1, N0, [&] { use(b1); }, [&] { D(4); });
      mm(P, 1, b1, o1, [&] { use(h4); }, N0, N0);
      tail();
#pragma unroll
      for (int m = 0; m < NM; ++m) op[m] = o1[m];
    };
    auto ND = [](int) {};
    SB();
    wait_vm_bar<63>();
    interval(IC<-1>{}, ND, N0);
    wait_vm_bar<63>();
    STAMP(7);
    interval(IC<1>{}, [&](int n) { if constexpr (NB == 3) dmal(4, 0, n); }, N0);
    if constexpr (NB == 3) wait_vm_bar<10>(); else wait_vm_bar<0>();
    if (c.w == 3 && lz == 0) __hip_atomic_store(sync + (1 + c.id) * FS, tag, RLX_AGENT);
    interval(IC<2>{}, [&](int n) { if constexpr (NB == 3) dmal(5, 1, n); }, [&] {
      if (c.w == 2) {
        const int pid = c.id ^ (((lz & 3) ? (lz & 3) : 1) << 3);
        for (unsigned spins = 0; spins < (1u << 14); ++spins) {
          if (__all(__hip_atomic_load(sync + (1 + pid) * FS, RLX_AGENT) == tag)) break;
          __builtin_amdgcn_s_sleep(1);
        }
        __builtin_amdgcn_fence(__ATOMIC_ACQUIRE, "agent");
        asm volatile("s_waitcnt vmcnt(0)" ::: "memory");
      }
    });
    if constexpr (NB == 3) wait_vm_bar<10>(); else wait_vm_bar<63>();
    STAMP(8);
    interval(IC<3>{}, [&](int n) { if constexpr (NB == 3) dmal(6, 2, n); else { dmal(4, 0, n); dmal(5, 1, n); } }, N0);
    STAMP(9);
#pragma unroll 1
    for (int i4 = 4; i4 < 16; i4 += 4)
      static_for(std::make_integer_sequence<int, 4>{}, [&](auto UU) {
        constexpr int u = decltype(UU)::value;
        if constexpr (NB == 3) wait_vm_bar<10>(); else { if (u == 0 && i4 == 4) wait_vm_bar<0>(); else wait_vm_bar<5>(); }
        interval(UU, [&](int n) { if constexpr (NB == 3) dmal(i4 + u + 3, (u + 3) & 3, n); else dmal(i4 + u + 2, (u + 2) & 3, n); }, N0);
      });
#pragma unroll
    for (int m = 0; m < NM; ++m) { amfma(acc[m][2], h2, op[m]); amfma(acc[m][3], h3, op[m]); amfma(acc[m][4], h4, op[m]); }
    asm volatile("s_nop 7");
    STAMP(10);
    if (q != hg) {
      const int so = (q * 3 + ((hg - q - 1) & 3)) * QB;
#pragma unroll
      for (int e = 0; e < NM * 5; e += 2) {
        const f32x4 a = acc[e / 5][e % 5], b = e + 1 < NM * 5 ? acc[(e + 1) / 5][(e + 1) % 5] : z4;
        __builtin_amdgcn_raw_buffer_store_b128(u32x4{cvtpk(a[0], a[1]), cvtpk(a[2], a[3]), cvtpk(b[0], b[1]), cvtpk(b[2], b[3])}, rslab, lz * 16 + ((M0 ? 8 : 0) + e / 2) * 1024 + so, 0, 16);
      }
    }
    asm volatile("s_waitcnt vmcnt(0) lgkmcnt(0)\n\ts_barrier" ::: "memory");
    if (c.w == 0 && lz == 0) __hip_atomic_store(sync + (257 + c.id) * FS, tag, RLX_AGENT);
    STAMP(11);
    if (q == hg) {
#pragma unroll
      for (int e = 0; e < NM * 5; ++e) st128(lds0 + lz * 16 + (M0 * 5 + e) * 1024, __builtin_bit_cast(u32x4, acc[e / 5][e % 5]));
    }
    if (c.w == ((hg + 1) & 3)) {
      const int pid = c.id ^ ((lz & 3) ? (lz & 3) : 1);
      for (unsigned spins = 0; spins < (1u << 14); ++spins) {
        if (__all(__hip_atomic_load(sync + (257 + pid) * FS, RLX_AGENT) == tag)) break;
        __builtin_amdgcn_s_sleep(1);
      }
    }
    if (c.id == 0 && c.w == 7) {
      for (unsigned spins = 0; spins < (1u << 14); ++spins) {
        bool ok = true;
#pragma unroll
        for (int k = 0; k < 4; ++k) ok &= __hip_atomic_load(sync + (1 + lz + 64 * k) * FS, RLX_AGENT) == tag;
        if (__all(ok)) break;
        __builtin_amdgcn_s_sleep(1);
      }
      if (lz == 0) __hip_atomic_store(sync, tag, RLX_AGENT);
    }
    asm volatile("s_waitcnt vmcnt(0) lgkmcnt(0)\n\ts_barrier" ::: "memory");
    STAMP(12);
    u16* op16 = ob + (size_t)(lz & 15) * 1280 + 4 * zg;
    u32x4 r[2][3];
#pragma unroll
    for (int k = 0; k < 2; ++k)
#pragma unroll
      for (int sl = 0; sl < 3; ++sl) r[k][sl] = __builtin_amdgcn_raw_buffer_load_b128(rslab, c.w + 8 * k < 13 ? lz * 16 + (c.w + 8 * k) * 1024 : OOB, (hg * 3 + sl) * QB, 16);
#pragma unroll
    for (int k = 0; k < 2; ++k) {
      const int un = c.w + 8 * k;
#pragma unroll
      for (int hh = 0; hh < 2; ++hh) {
        const int e = un < 8 ? 2 * un + hh : 15 + 2 * (un - 8) + hh;
        if (un < 13 && !(un == 7 && hh == 1)) {
          f32x4 v = *(const LDS3 f32x4*)(size_t)(lds0 + lz * 16 + e * 1024);
#pragma unroll
          for (int sl = 0; sl < 3; ++sl) { const unsigned lo = r[k][sl][2 * hh], hi = r[k][sl][2 * hh + 1];
            v += f32x4{__uint_as_float(lo << 16), __uint_as_float(lo & 0xffff0000u), __uint_as_float(hi << 16), __uint_as_float(hi & 0xffff0000u)}; }
          const int m = e / 5, n = e - m * 5;
          *(u32x2*)(op16 + m * (16 * 1280) + n * 16) = u32x2{cvtpk(v[0], v[1]), cvtpk(v[2], v[3])};
        }
      }
    }
  }
}

__global__ __launch_bounds__(512, 2) void fused(const u16* xA, const u16* kA, const u16* vA, const u16* wqA, const u16* woA,
                                                 const u16* xB, const u16* kB, const u16* vB, const u16* wqB, const u16* woB, u16* out, char* ws) {
  extern __shared__ __attribute__((aligned(16))) char smem[];
  const int id = blockIdx.x, xg = id & 7, cc = id >> 3, br = xg >> 2, hg = xg & 3, b = cc >> 2, pr = cc & 3, h0 = hg * 8 + pr * 2;
  const int w = __builtin_amdgcn_readfirstlane(threadIdx.x >> 6);
  asm volatile("s_nop 4");
  Ctx c;
  const u16* x = br ? xB : xA; const u16* k = br ? kB : kA; const u16* v = br ? vB : vA; const u16* wq = br ? wqB : wqA;
  c.rx = mkr(x + (size_t)b * SQ * DM, SQ * DM * 2);
  c.rw = mkr(wq + h0 * DH, DM * NH * DH * 2 - h0 * DH * 2);
  c.rk = mkr(k + (size_t)(b * NH + h0) * SK * DH, 2 * SK * DH * 2);
  c.rv = mkr(v + (size_t)(b * NH + h0) * SK * DH, 2 * SK * DH * 2);
  c.lds0 = (unsigned)(size_t)(LDS3 char*)smem;
  c.hs = w >> 2; c.j = w & 3; c.rot = (id * 5) & 15; c.krot = ((10 * pr + 5 * b) >> 2) % 10;
  c.w = w; c.id = id; c.wo = br ? woB : woA; c.out = out; c.ws = ws;
  const bool big = (c.j < 2) == (c.hs == 0);
  c.blk0 = c.hs == 0 ? (c.j < 2 ? 3 * c.j : 2 + 2 * c.j) : (c.j < 2 ? 2 * c.j : 3 * c.j - 2);
  {
    unsigned r = (threadIdx.x * 2654435761u) & 0x3fff3fffu | 0x3c003c00u;
    u32x4 a = {r, r * 3 & 0xbfffbfffu, r * 5 & 0xbfffbfffu, r * 7 & 0xbfffbfffu};
    f32x4 acc[4] = {};
    pin(a); pin(acc[0]); pin(acc[1]); pin(acc[2]); pin(acc[3]); asm volatile("s_nop 4");
    for (int p = 0; p < 150; ++p) { amfma(acc[0], asb(a), asb(a)); amfma(acc[1], asb(a), asb(a)); amfma(acc[2], asb(a), asb(a)); amfma(acc[3], asb(a), asb(a)); }
    asm volatile("s_nop 7\n\ts_nop 7" :: "v"(acc[0]), "v"(acc[1]), "v"(acc[2]), "v"(acc[3]));
  }
  if (big) body<3>(c); else body<2>(c);
}

extern "C" void kernel_launch(void* const* d_in, const int* in_sizes, int n_in, void* d_out, int out_size, void* d_ws, size_t ws_size, hipStream_t stream) {
  (void)hipFuncSetAttribute((const void*)fused, hipFuncAttributeMaxDynamicSharedMemorySize, LDS_BYTES);
  fused<<<256, 512, LDS_BYTES, stream>>>((const u16*)d_in[0], (const u16*)d_in[1], (const u16*)d_in[2], (const u16*)d_in[3], (const u16*)d_in[4],
                                         (const u16*)d_in[5], (const u16*)d_in[6], (const u16*)d_in[7], (const u16*)d_in[8], (const u16*)d_in[9], (u16*)d_out, (char*)d_ws);
}
```

```cpp
#include <hip/hip_runtime.h>
#include <hip/hip_bf16.h>
#include <type_traits>

typedef __bf16 bf16x8 __attribute__((ext_vector_type(8)));
typedef __bf16 bf16x4 __attribute__((ext_vector_type(4)));
typedef __bf16 bf16x2 __attribute__((ext_vector_type(2)));
typedef float f32x4 __attribute__((ext_vector_type(4)));
typedef float f32x2 __attribute__((ext_vector_type(2)));
typedef unsigned u32x4 __attribute__((ext_vector_type(4)));
typedef unsigned u32x2 __attribute__((ext_vector_type(2)));
typedef unsigned short u16;
typedef __attribute__((address_space(1))) unsigned long long gu64;
typedef __amdgpu_buffer_rsrc_t rsrc_t;
#define LDS3 __attribute__((address_space(3)))
#define DI __device__ __forceinline__
#define SB() __builtin_amdgcn_sched_barrier(0)
#define RLX_AGENT __ATOMIC_RELAXED, __HIP_MEMORY_SCOPE_AGENT
template <int N> using IC = std::integral_constant<int, N>;

constexpr int SQ = 160, DM = 640, NH = 32, SK = 512, DH = 128;
constexpr int LDS_BYTES = 163840;
constexpr int KV_HS = 32768, V_OFF = 65536;
__host__ __device__ constexpr int xoff(int b) { return b * 65536 + 73728; }
__host__ __device__ constexpr int woff(int b) { return b == 0 ? 16384 : b == 1 ? 49152 : b == 2 ? 106496 : 122880; }
constexpr float QSCALE = 0.08838834764831845f * 1.4426950408889634f;
constexpr float BIAS = 10.f;
constexpr unsigned OOB = 0x7ff00000u;
constexpr size_t WS_SLAB = 32u << 20, WS_SYNC = 64u << 20;
constexpr int FS = 32;
constexpr int QB = 13 * 1024;
#define KVN0 1
#define XNT false
#define WNT true
#define WONT false

DI unsigned cvtpk(float lo, float hi) { f32x2 v = {lo, hi}; bf16x2 b = __builtin_convertvector(v, bf16x2); return __builtin_bit_cast(unsigned, b); }
DI f32x4 mfma(bf16x8 a, bf16x8 b, f32x4 c) { return __builtin_amdgcn_mfma_f32_16x16x32_bf16(a, b, c, 0, 0, 0); }
DI bf16x8 ld128(unsigned addr) { return *(const LDS3 bf16x8*)(size_t)addr; }
DI void st128(unsigned addr, u32x4 v) { *(LDS3 u32x4*)(size_t)addr = v; }
DI bf16x4 ldtr(unsigned addr) { return __builtin_amdgcn_ds_read_tr16_b64_v4bf16((LDS3 bf16x4*)(size_t)addr); }
DI bf16x8 cat(bf16x4 a, bf16x4 b) { return __builtin_shufflevector(a, b, 0, 1, 2, 3, 4, 5, 6, 7); }
DI bf16x8 asb(u32x4 w) { return __builtin_bit_cast(bf16x8, w); }
DI rsrc_t mkr(const void* p, int bytes) { return __builtin_amdgcn_make_buffer_rsrc((void*)p, (short)0, bytes, 0x00020000); }
template <bool NT> DI void dma16(unsigned ldsaddr, unsigned voff, rsrc_t r, unsigned soff) {
  if constexpr (NT) asm volatile("s_mov_b32 m0, %0\n\ts_nop 0\n\tbuffer_load_dwordx4 %1, %2, %3 offen nt lds" :: "s"(ldsaddr), "v"(voff), "s"(r), "s"(soff) : "memory");
  else              asm volatile("s_mov_b32 m0, %0\n\ts_nop 0\n\tbuffer_load_dwordx4 %1, %2, %3 offen lds" :: "s"(ldsaddr), "v"(voff), "s"(r), "s"(soff) : "memory");
}
template <int N> DI void wait_vm_bar() { asm volatile("s_waitcnt vmcnt(%0) lgkmcnt(0)\n\ts_barrier" :: "i"(N) : "memory"); }
DI float xmax16(float v) { auto r = __builtin_amdgcn_permlane16_swap(__float_as_uint(v), __float_as_uint(v), false, false); return fmaxf(__uint_as_float(r[0]), __uint_as_float(r[1])); }
DI float xmax32(float v) { auto r = __builtin_amdgcn_permlane32_swap(__float_as_uint(v), __float_as_uint(v), false, false); return fmaxf(__uint_as_float(r[0]), __uint_as_float(r[1])); }
DI void amfma(f32x4& acc, bf16x8 a, bf16x8 b) { asm volatile("v_mfma_f32_16x16x32_bf16 %0, %1, %2, %0" : "+v"(acc) : "v"(a), "v"(b)); }
DI void amfma_c(f32x4& d, bf16x8 a, bf16x8 b, f32x4 cc) { asm volatile("v_mfma_f32_16x16x32_bf16 %0, %1, %2, %3" : "=&v"(d) : "v"(a), "v"(b), "v"(cc)); }
DI void amfma_z(f32x4& d, bf16x8 a, bf16x8 b) { asm volatile("v_mfma_f32_16x16x32_bf16 %0, %1, %2, 0" : "=&v"(d) : "v"(a), "v"(b)); }
template <bool Z> DI void amf(f32x4& d, bf16x8 a, bf16x8 b) { if constexpr (Z) amfma_z(d, a, b); else amfma(d, a, b); }
DI float aexp(float x) { asm volatile("v_exp_f32 %0, %0" : "+v"(x)); return x; }
DI unsigned aor3(unsigned a, unsigned b, unsigned c) { unsigned r; asm volatile("v_or3_b32 %0, %1, %2, %3" : "=v"(r) : "v"(a), "v"(b), "v"(c)); return r; }
DI unsigned acvt(float lo, float hi) { unsigned r; asm volatile("v_cvt_pk_bf16_f32 %0, %1, %2" : "=v"(r) : "v"(lo), "v"(hi)); return r; }
DI void use(bf16x8 f) { asm volatile("" :: "v"(f)); }
DI void aaddu(unsigned& a, int d) { asm volatile("v_add_u32 %0, %1, %0" : "+v"(a) : "s"(d)); }
DI unsigned laneid() { unsigned l; asm volatile("v_mbcnt_lo_u32_b32 %0, -1, 0\n\tv_mbcnt_hi_u32_b32 %0, -1, %0" : "=v"(l)); return l; }
template <class T> DI void pin(T& v) { asm volatile("" : "+v"(v)); }

#define STAMP(i)

struct Ctx {
  rsrc_t rx, rw, rk, rv;
  unsigned lds0;
  int j, hs, blk0, rot, krot, w, id;
  const u16* wo; u16* out; char* ws;
};
template <int NB> struct St {
  f32x4 o[NB][8], negm[NB], S0[NB], S1[NB], l[NB];
  bf16x8 qf[NB][4], ones;
  u32x4 pf[NB];
  unsigned orv;
};
template <int NB, int E> DI void exp1(f32x4 (&S)[NB]) { if constexpr (E >= 0 && E < 4 * NB) S[E % NB][E / NB] = aexp(S[E % NB][E / NB]); }
template <int NB, int LO, int HI> DI void exps(f32x4 (&S)[NB]) { if constexpr (LO < HI) { exp1<NB, LO>(S); exps<NB, LO + 1, HI>(S); } }
DI float max8(f32x4 a, f32x4 b) { return fmaxf(fmaxf(fmaxf(a[0], a[1]), fmaxf(a[2], a[3])), fmaxf(fmaxf(b[0], b[1]), fmaxf(b[2], b[3]))); }

template <int NB, bool HAS_NEXT> DI void refix(St<NB>& s, rsrc_t rk, int ksoff) {
  const unsigned ln = laneid();
  const int kvo = (ln & 15) * 256 + ((((ln >> 4) & 1) << 1) | (ln >> 5)) * 16;
  asm volatile("s_nop 7");
#pragma unroll
  for (int nb = 0; nb < NB; ++nb) {
    f32x4 a = s.negm[nb], b = s.negm[nb];
#pragma unroll
    for (int st = 0; st < 4; ++st) {
      a = mfma(asb(__builtin_amdgcn_raw_buffer_load_b128(rk, kvo + st * 64, ksoff, 0)), s.qf[nb][st], a);
      SB();
      b = mfma(asb(__builtin_amdgcn_raw_buffer_load_b128(rk, kvo + st * 64, ksoff + 4096, 0)), s.qf[nb][st], b);
      SB();
    }
    const float d = fmaxf(xmax32(xmax16(max8(a, b))) + BIAS, 0.f);
    const float f = __builtin_amdgcn_exp2f(-d);
    s.negm[nb] -= d; if (HAS_NEXT) s.S0[nb] -= d;
    s.l[nb] *= f;
#pragma unroll
    for (int db = 0; db < 8; ++db) s.o[nb][db] *= f;
#pragma unroll
    for (int r = 0; r < 4; ++r) { a[r] = __builtin_amdgcn_exp2f(a[r] - d); b[r] = __builtin_amdgcn_exp2f(b[r] - d); }
    u32x4 w = {cvtpk(a[0], a[1]), cvtpk(a[2], a[3]), cvtpk(b[0], b[1]), cvtpk(b[2], b[3])};
    s.pf[nb] = w;
    SB();
  }
  asm volatile("s_nop 1");
}
template <int NB, bool HAS_NEXT> DI void check(St<NB>& s, rsrc_t rk, int ksoff) {
  if (__builtin_expect(__any((s.orv & 0x40004000u) != 0u), 0)) refix<NB, HAS_NEXT>(s, rk, ksoff);
}
template <int... I, class F> DI void static_for(std::integer_sequence<int, I...>, F f) { (f(IC<I>{}), ...); }

template <int NB> DI void body(const Ctx& c) {
  const int lane = threadIdx.x & 63, l15 = lane & 15, g = lane >> 4;
  const unsigned lds0 = c.lds0;
  const f32x4 z4 = {0.f, 0.f, 0.f, 0.f};
  auto N0 = [] {};
  const unsigned voffK = (lane >> 4) * 256 + (((lane & 15) ^ (4 * c.j + (lane >> 4))) * 16);
  const unsigned voffV = ((lane >> 1) & 7) * 256 + (lane >> 4) * 32 + (lane & 1) * 16;
  auto kvl = [&](int t, int q) { return lds0 + (q >> 1) * KV_HS + (t & 3) * 8192 + (c.j + 4 * (q & 1)) * 1024; };
  auto kso = [&](int t, int q) { return (q >> 1) * 131072 + ((t + c.rot) & 15) * 8192 + (c.j + 4 * (q & 1)) * 1024; };
  auto vso = [&](int t, int q) { const int i = c.j + 4 * (q & 1); return (q >> 1) * 131072 + ((t + c.rot) & 15) * 8192 + (i >> 1) * 2048 + (i & 1) * 128; };
  auto dma_k1 = [&](int t, int q) { dma16<true>(kvl(t, q), voffK, c.rk, t < 16 ? kso(t, q) : OOB); };
  auto dma_v1 = [&](int t, int q) { dma16<true>(V_OFF + kvl(t, q), voffV, c.rv, t < 16 ? vso(t, q) : OOB); };
  STAMP(0);
  St<NB> s;
  const unsigned long long epoch = __hip_atomic_load((gu64*)(c.ws + WS_SYNC), RLX_AGENT);
  u32x4 kvr[16];
  {
    unsigned voffS;
    if constexpr (NB == 3) { const int f = (2 * (c.j & 1) + (lane >> 5)) | ((c.j >> 1) << 2); voffS = (lane >> 5) * 8192 + (((lane & 31) ^ (f << 1)) * 16); }
    else voffS = (lane >> 3) * 1280 + (((lane & 7) ^ (4 * (c.j & 1) + g)) * 16);
    auto dma_w = [&](int st, int buf, int n) {
      int ks = st + 2 * c.krot; ks = ks >= 20 ? ks - 20 : ks;
      const int i = (c.j & 1) + 4 * (c.j >> 1) + 2 * (n & 1) + 8 * (n >> 1);
      dma16<WNT>(lds0 + woff(buf) + i * 1024, voffS, c.rw, st < 20 ? (ks * 32 + 2 * i) * 8192 : OOB);
    };
    auto dma_x = [&](int ch, int n) {
      int kc = ch + c.krot; kc = kc >= 10 ? kc - 10 : kc;
      const int i = c.j + 4 * n;
      dma16<XNT>(lds0 + xoff(ch & 1) + i * 1024, voffS, c.rx, i * (8 * 1280) + kc * 128);
    };
    auto extra = [&](int m, int q) {
      if (m == 0 || m == 1) dma_k1(m, q);
      else if (m == 2) dma_v1(0, q);
      else if (m == 3 || m == 5) kvr[(m - 3) * 4 + q] = __builtin_amdgcn_raw_buffer_load_b128(c.rk, voffK, kso((m + 1) / 2, q), 2);
      else if (m == 4 || m == 6) kvr[(m - 3) * 4 + q] = __builtin_amdgcn_raw_buffer_load_b128(c.rv, voffV, vso((m - 2) / 2, q), 2);
    };
    if constexpr (NB == 3) {
#pragma unroll
      for (int st = 0; st < 3; ++st)
#pragma unroll
        for (int n = 0; n < 4; ++n) dma_w(st, st, n);
    } else {
#pragma unroll
      for (int n = 0; n < 5; ++n) dma_x(0, n);
      __builtin_amdgcn_s_setprio(1);
    }
    unsigned wa[2][8], xa[4];
    const int qq = l15 >> 2, p = lane & 3, f = qq | ((g & 1) << 2);
#pragma unroll
    for (int db = 0; db < 8; ++db) { wa[0][db] = lds0 + woff(0) + (8 * g + qq) * 512 + c.hs * 256 + ((db ^ f) * 32) + p * 8; wa[1][db] = wa[0][db] + (woff(2) - woff(0)); pin(wa[1][db]); }
#pragma unroll
    for (int u = 0; u < 4; ++u) { xa[u] = lds0 + xoff(u >> 1) + (c.blk0 * 16 + l15) * 128 + (((4 * (u & 1) + g) ^ ((l15 >> 1) & 7)) * 16); pin(xa[u]); }
    bf16x8 w4, w5, w6, w7, xp[NB];
    auto qstep = [&](auto U, auto PH, auto D) {
      constexpr int u = decltype(U)::value, ph = decltype(PH)::value;
      auto rw = [&](int db) { const unsigned a = wa[u >> 1][db] + (woff(u) - woff(u & 2)); return cat(ldtr(a), ldtr(a + 2048)); };
      auto rx = [&](int nb) { return ld128(xa[u] + nb * 2048); };
      auto mm = [&](auto Z, int db, bf16x8& wf, bf16x8 (&xf)[NB], auto g0, auto g1, auto g2) {
        constexpr int z = decltype(Z)::value;
        if constexpr (z == 2) { g0(); g1(); g2(); SB(); }
        else {
          amf<z == 1>(s.o[0][db], wf, xf[0]); SB(); g0(); SB();
          amf<z == 1>(s.o[1][db], wf, xf[1]); SB(); g1(); SB();
          if constexpr (NB == 3) { amf<z == 1>(s.o[2][db], wf, xf[2]); SB(); }
          g2(); SB();
        }
      };
      constexpr IC<ph == 0 ? 2 : ph == 1 ? 1 : 0> H{};
      constexpr IC<ph == 0 ? 1 : 0> C{};
      bf16x8 xf[NB], w0, w1, w2, w3;
      if constexpr (NB == 3) {
        mm(H, 4, w4, xp, [&] { w0 = rw(0); }, [&] { xf[0] = rx(0); xf[1] = rx(1); }, [&] { xf[2] = rx(2); });
        mm(H, 5, w5, xp, [&] { w1 = rw(1); }, [&] { D(0); }, N0);
        mm(H, 6, w6, xp, [&] { w2 = rw(2); }, [&] { w3 = rw(3); }, N0);
        mm(H, 7, w7, xp, [&] { w4 = rw(4); }, [&] { D(1); }, [&] { use(xf[NB - 1]); });
        mm(C, 0, w0, xf, [&] { w5 = rw(5); }, [&] { use(w1); w6 = rw(6); }, N0);
        mm(C, 1, w1, xf, [&] { w7 = rw(7); }, [&] { use(w2); D(2); }, N0);
        mm(C, 2, w2, xf, [&] { use(w3); }, [&] { D(3); }, N0);
        mm(C, 3, w3, xf, [&] { use(w7); }, N0, N0);
      } else {
        mm(H, 4, w4, xp, [&] { w0 = rw(0); }, [&] { xf[0] = rx(0); xf[1] = rx(1); }, N0);
        mm(H, 5, w5, xp, [&] { w1 = rw(1); }, [&] { D(0); }, N0);
        mm(H, 6, w6, xp, [&] { w2 = rw(2); }, [&] { w3 = rw(3); D(1); }, N0);
        mm(H, 7, w7, xp, [&] { w4 = rw(4); }, [&] { D(2); use(xf[NB - 1]); }, N0);
        mm(C, 0, w0, xf, [&] { w5 = rw(5); }, [&] { use(w1); w6 = rw(6); D(3); }, N0);
        mm(C, 1, w1, xf, [&] { w7 = rw(7); }, [&] { use(w2); D(4); }, N0);
        mm(C, 2, w2, xf, [&] { use(w3); }, N0, N0);
        mm(C, 3, w3, xf, [&] { use(w7); }, N0, N0);
      }
#pragma unroll
      for (int nb = 0; nb < NB; ++nb) xp[nb] = xf[nb];
    };
    STAMP(1);
    SB();
    if constexpr (NB == 3) {
      static_for(std::make_integer_sequence<int, 4>{}, [&](auto U) {
        constexpr int u = decltype(U)::value;
        wait_vm_bar<8>(); qstep(U, IC<(u < 2 ? u : 2)>{}, [&](int n) { dma_w(u + 3, (u + 3) & 3, n); });
      });
#pragma unroll 1
      for (int s4 = 4; s4 < 20; s4 += 4)
        static_for(std::make_integer_sequence<int, 4>{}, [&](auto U) {
          constexpr int u = decltype(U)::value;
          wait_vm_bar<8>(); qstep(U, IC<2>{}, [&](int n) { dma_w(s4 + u + 3, (u + 3) & 3, n); });
        });
    } else {
      static_for(std::make_integer_sequence<int, 20>{}, [&](auto ST) {
        constexpr int st = decltype(ST)::value, mp = (st >> 1) - 1 - KVN0;
        if constexpr (st & 1) wait_vm_bar<63>(); else wait_vm_bar<(mp >= 0 && mp < 7) ? 4 : 0>();
        qstep(IC<(st & 3)>{}, IC<(st < 2 ? st : 2)>{}, [&](int n) {
          if constexpr (st & 1) { if (n < 4) extra((st >> 1) - KVN0, n); } else if constexpr ((st >> 1) + 1 < 10) dma_x((st >> 1) + 1, n); });
      });
    }
#pragma unroll
    for (int nb = 0; nb < NB; ++nb) { amfma(s.o[nb][4], w4, xp[nb]); amfma(s.o[nb][5], w5, xp[nb]); amfma(s.o[nb][6], w6, xp[nb]); amfma(s.o[nb][7], w7, xp[nb]); }
    asm volatile("s_nop 7");
  }
  STAMP(2);
  wait_vm_bar<0>();
  const unsigned long long tag = (((unsigned long long)__builtin_amdgcn_readfirstlane((unsigned)(epoch >> 32)) << 32) | __builtin_amdgcn_readfirstlane((unsigned)epoch)) + 1ull;
  const int pg = ((g & 1) << 1) | (g >> 1);
  unsigned ka[4];
#pragma unroll
  for (int st = 0; st < 4; ++st) ka[st] = lds0 + c.hs * KV_HS + l15 * 256 + (((4 * st + pg) ^ l15) * 16);
  unsigned va = lds0 + V_OFF + c.hs * KV_HS + (g >> 1) * 2048 + (4 * (g & 1) + (l15 >> 2)) * 32 + (lane & 3) * 8;
  auto rk = [&](int kt, int st) { return ld128(ka[st] + kt * 4096); };
  auto rv = [&](int db) { const unsigned a = va + (db >> 2) * 1024 + (db & 3) * 256; return cat(ldtr(a), ldtr(a + 4096)); };
  {
    bf16x8 kf0[4], kf1[4];
#pragma unroll
    for (int st = 0; st < 4; ++st) { kf0[st] = rk(0, st); kf1[st] = rk(1, st); }
#pragma unroll
    for (int nb = 0; nb < NB; ++nb)
#pragma unroll
      for (int e = 0; e < 4; ++e) {
        const f32x4 a0 = s.o[nb][2 * e] * QSCALE, a1 = s.o[nb][2 * e + 1] * QSCALE;
        auto r0 = __builtin_amdgcn_permlane16_swap(cvtpk(a0[0], a0[1]), cvtpk(a1[0], a1[1]), false, false);
        auto r1 = __builtin_amdgcn_permlane16_swap(cvtpk(a0[2], a0[3]), cvtpk(a1[2], a1[3]), false, false);
        u32x4 w = {r0[0], r1[0], r0[1], r1[1]};
        s.qf[nb][e] = asb(w);
        if constexpr (NB == 2) {
#pragma unroll
          for (int h = 0; h < 2; ++h) { const int i = (nb * 4 + e) * 2 + h, m = i >> 2; st128(((m & 1) ? V_OFF + kvl(1 + (m >> 1), i & 3) : kvl(2 + (m >> 1), i & 3)) + lane * 16, kvr[i]); }
        }
      }
#pragma unroll
    for (int st = 0; st < 4; ++st)
#pragma unroll
      for (int nb = 0; nb < NB; ++nb) { s.S0[nb] = mfma(kf0[st], s.qf[nb][st], st == 0 ? z4 : s.S0[nb]); s.S1[nb] = mfma(kf1[st], s.qf[nb][st], st == 0 ? z4 : s.S1[nb]); }
#pragma unroll
    for (int nb = 0; nb < NB; ++nb) {
#pragma unroll
      for (int db = (NB == 3 ? 6 : 5); db < 8; ++db) { s.o[nb][db] = z4; pin(s.o[nb][db]); }
      const float m = xmax32(xmax16(max8(s.S0[nb], s.S1[nb]))) + BIAS;
      s.negm[nb] = z4 - m; s.S0[nb] -= m; s.S1[nb] -= m;
      pin(s.negm[nb]);
    }
    s.ones = asb(u32x4{0x3f803f80u, 0x3f803f80u, 0x3f803f80u, 0x3f803f80u});
    pin(s.ones);
  }
  exps<NB, 0, 4 * NB>(s.S0); exps<NB, 0, 2 * NB>(s.S1);
  bf16x8 v5h, v6, v7;
#pragma unroll
  for (int st = 0; st < 4; ++st) ka[st] += 8192;
  SB();
  auto us = [&](auto m, auto g0, auto g1, auto g2) {
    m(0); SB(); g0(); SB(); m(1); SB(); g1(); SB();
    if constexpr (NB == 3) { m(2); SB(); g2(); SB(); }
  };
  auto QK0 = [&](int st, bf16x8 fr) { return [&s, st, fr](int nb) { if (st == 0) amfma_c(s.S0[nb], fr, s.qf[nb][0], s.negm[nb]); else amfma(s.S0[nb], fr, s.qf[nb][st]); }; };
  auto QK1 = [&](int st, bf16x8 fr) { return [&s, st, fr](int nb) { if (st == 0) amfma_c(s.S1[nb], fr, s.qf[nb][0], s.negm[nb]); else amfma(s.S1[nb], fr, s.qf[nb][st]); }; };
  auto PVH = [&](int db, bf16x8& fr) { return [&s, db, &fr](int nb) { amfma(s.o[nb][db], fr, asb(s.pf[nb])); }; };
  auto N = [] {};
#define E0(e) [&] { exp1<NB, (e)>(s.S0); }
#define E1(e) [&] { exp1<NB, (e)>(s.S1); }
#define CLO(nb) [&] { if constexpr ((nb) < NB) { s.pf[nb][0] = acvt(s.S0[nb][0], s.S0[nb][1]); s.pf[nb][1] = acvt(s.S0[nb][2], s.S0[nb][3]); } }
#define CHI(nb) [&] { if constexpr ((nb) < NB) { s.pf[nb][2] = acvt(s.S1[nb][0], s.S1[nb][1]); s.pf[nb][3] = acvt(s.S1[nb][2], s.S1[nb][3]); } }
#define U(f, ...) [&] { use(f); SB(); (__VA_ARGS__)(); }
  auto astep = [&](auto FI, int t, auto kvh) {
    constexpr bool FIRST = decltype(FI)::value;
    const int dk = (t & 3) == 3 ? -24576 : 8192, dv = (t & 3) == 0 ? -24576 : 8192;
    if constexpr (NB == 2) wait_vm_bar<16>(); else wait_vm_bar<63>();
    auto PH = [&](int db, bf16x8& fr) { return [&, db](int nb) { if constexpr (!FIRST) amfma(s.o[nb][db], fr, asb(s.pf[nb])); }; };
    auto PV = [&](int db, bf16x8 fr) { return [&s, db, fr](int nb) { amf<FIRST>(s.o[nb][db], fr, asb(s.pf[nb])); }; };
    auto SUM = [&](int nb) { amf<FIRST>(s.l[nb], s.ones, asb(s.pf[nb])); };
    bf16x8 a0, a1, a2, a3, b0, b1, b2, b3, v0, v1, v2, v3, v4;
    unsigned o1, o2;
    if constexpr (NB == 3) {
      bf16x8 v5;
      us(PH(6, v6), [&] { a0 = rk(0, 0); a1 = rk(0, 1); }, [&] { a2 = rk(0, 2); a3 = rk(0, 3); }, E1(6));
      us(PH(7, v7), CLO(0), CLO(1), U(a0, CLO(2)));
      us(QK0(0, a0), [&] { b0 = rk(1, 0); b1 = rk(1, 1); }, E1(7), U(a1, E1(8)));
      us(QK0(1, a1), E1(9), E1(10), U(a2, E1(11)));
      us(QK0(2, a2), [&] { o1 = aor3(s.pf[0][0], s.pf[0][1], s.pf[1][0]); o2 = aor3(s.pf[1][1], s.pf[2][0], s.pf[2][1]); }, CHI(0), U(a3, CHI(1)));
      us(QK0(3, a3), CHI(2), [&] { o1 = aor3(o1, s.pf[0][2], s.pf[0][3]); o2 = aor3(o2, s.pf[1][2], s.pf[1][3]); },
                     U(b0, [&] { o1 = aor3(o1, s.pf[2][2], s.pf[2][3]); s.orv = o1 | o2; }));
      if constexpr (!FIRST) check<NB, true>(s, c.rk, c.hs * 131072 + ((t - 1 + c.rot) & 15) * 8192);
      SB();
      us(QK1(0, b0), [&] { b2 = rk(1, 2); b3 = rk(1, 3); }, [&] { v0 = rv(0); }, U(b1, [&] { aaddu(ka[0], dk); aaddu(ka[1], dk); }));
      us(QK1(1, b1), [&] { v1 = rv(1); }, E0(0), U(b2, E0(1)));
      us(QK1(2, b2), [&] { v2 = rv(2); }, E0(2), U(b3, E0(3)));
      us(QK1(3, b3), [&] { v3 = rv(3); }, E0(4), E0(5));
      us(SUM, E0(6), E0(7), U(v0, E0(8)));
      us(PV(0, v0), [&] { v4 = rv(4); }, E0(9), U(v1, E0(10)));
      us(PV(1, v1), [&] { v5 = rv(5); }, E0(11), U(v2, E1(0)));
      us(PV(2, v2), [&] { v6 = rv(6); }, E1(1), U(v3, E1(2)));
      us(PV(3, v3), [&] { v7 = rv(7); }, E1(3), U(v4, E1(4)));
      us(PV(4, v4), E1(5), [&] { aaddu(ka[2], dk); aaddu(ka[3], dk); }, U(v5, [&] { aaddu(va, dv); }));
      us(PV(5, v5), N, [&] { use(v7); }, N);
    } else {
      us(PH(5, v5h), [&] { a0 = rk(0, 0); a1 = rk(0, 1); }, [&] { a2 = rk(0, 2); a3 = rk(0, 3); }, N);
      us(PH(6, v6), E1(4), [&] { kvh(0); }, N);
      us(PH(7, v7), CLO(0), U(a0, CLO(1)), N);
      us(QK0(0, a0), [&] { b0 = rk(1, 0); b1 = rk(1, 1); }, U(a1, E1(5)), N);
      us(QK0(1, a1), E1(6), U(a2, E1(7)), N);
      us(QK0(2, a2), [&] { o1 = aor3(s.pf[0][0], s.pf[0][1], s.pf[1][0]); kvh(1); }, U(a3, CHI(0)), N);
      us(QK0(3, a3), CHI(1), U(b0, [&] { o1 = aor3(o1, s.pf[1][1], s.pf[0][2]); o2 = aor3(s.pf[0][3], s.pf[1][2], s.pf[1][3]); s.orv = o1 | o2; }), N);
      if constexpr (!FIRST) check<NB, true>(s, c.rk, c.hs * 131072 + ((t - 1 + c.rot) & 15) * 8192);
      SB();
      us(QK1(0, b0), [&] { b2 = rk(1, 2); b3 = rk(1, 3); }, U(b1, [&] { v0 = rv(0); kvh(2); }), N);
      us(QK1(1, b1), [&] { v1 = rv(1); }, U(b2, [&] { aaddu(ka[0], dk); aaddu(ka[1], dk); kvh(3); }), N);
      us(QK1(2, b2), [&] { v2 = rv(2); }, U(b3, E0(0)), N);
      us(QK1(3, b3), [&] { v3 = rv(3); }, [&] { exp1<NB, 1>(s.S0); kvh(4); }, N);
      us(SUM, E0(2), U(v0, E0(3)), N);
      us(PV(0, v0), [&] { v4 = rv(4); }, U(v1, [&] { exp1<NB, 4>(s.S0); kvh(5); }), N);
      us(PV(1, v1), [&] { v5h = rv(5); }, U(v2, E0(5)), N);
      us(PV(2, v2), [&] { v6 = rv(6); aaddu(ka[2], dk); aaddu(ka[3], dk); }, U(v3, [&] { exp1<NB, 6>(s.S0); kvh(6); }), N);
      us(PV(3, v3), [&] { v7 = rv(7); exp1<NB, 7>(s.S0); }, U(v4, [&] { exp1<NB, 0>(s.S1); kvh(7); }), N);
      us(PV(4, v4), [&] { exp1<NB, 1>(s.S1); exp1<NB, 2>(s.S1); }, [&] { exp1<NB, 3>(s.S1); aaddu(va, dv); use(v7); }, N);
    }
  };
  auto kv = [&](int t, int n) { if constexpr (NB == 2) { if (n < 4) dma_k1(t + 3, n); else dma_v1(t + 2, n - 4); } };
  STAMP(3);
  astep(std::true_type{}, 1, [&](int n) { kv(1, n); });
#pragma unroll 1
  for (int t = 2; t < (NB == 3 ? 16 : 14); ++t) astep(std::false_type{}, t, [&](int n) { kv(t, n); });
  rsrc_t rof, rwo, rslab; int pr, hg, rotb; gu64* sync; u16* ob;
  {
    int id = c.id; asm volatile("" : "+s"(id));
    const int br = (id >> 2) & 1, b = id >> 5;
    hg = id & 3; pr = (id >> 3) & 3; rotb = b & 3;
    rof = mkr(c.ws + (size_t)((br * 8 + b) * 4 + hg) * 327680, 327680);
    rwo = mkr(c.wo + (size_t)hg * 1024 * 640 + pr * 160, 1024 * 1280 - pr * 320);
    rslab = mkr(c.ws + WS_SLAB + (size_t)((br * 8 + b) * 4 + pr) * (12 * QB), 12 * QB);
    sync = (gu64*)(c.ws + WS_SYNC);
    ob = c.out + (size_t)(b * 160 + (hg & 1) * 80) * 1280 + br * 640 + pr * 160 + (hg >> 1) * 80;
  }
  unsigned lz = 0;
  const int dks = c.j >> 1, dh = c.j & 1;
  auto stg = [](int u) { return (u >> 1) * 81920 + ((u ^ (u >> 1)) & 1) * 40960; };
  auto kpair = [&](int i) { return ((pr + (i >> 2)) & 3) * 4 + ((i + rotb) & 3); };
  auto wvo = [&](int n) { const int e = 64 * n + (int)lz, row = e / 20, ch = e - row * 20; return row * 1280 + ((ch ^ (((row >> 2) & 1) << 1)) * 16); };
  auto wso = [&](int i) { return (kpair(i) * 64 + dks * 32 + dh * 16) * 1280; };
  auto wld = [&](int i, int n) { return lds0 + stg(i & 3) + dks * 20480 + 10240 + dh * 5120 + n * 1024; };
  u32x4 wr[15];
  if constexpr (NB == 2) {
    lz = laneid();
    unsigned vw[5];
#pragma unroll
    for (int n = 0; n < 5; ++n) vw[n] = wvo(n);
    auto wl = [&](int p) { if (p < 15) wr[p] = __builtin_amdgcn_raw_buffer_load_b128(rwo, vw[p % 5], wso(p / 5), WONT ? 2 : 0); };
    SB();
    astep(std::false_type{}, 14, [&](int n) { wl(n); });
    astep(std::false_type{}, 15, [&](int n) { wl(8 + n); });
  }
  STAMP(4);
  if constexpr (NB == 3) wait_vm_bar<63>(); else wait_vm_bar<15>();
  {
    bf16x8 vf[8];
#pragma unroll
    for (int db = 0; db < 4; ++db) vf[db] = rv(db);
    SB();
    if constexpr (NB == 2) us(PVH(5, v5h), N, N, N);
    us(PVH(6, v6), N, N, N);
    exps<NB, 2 * NB, 4 * NB>(s.S1);
    us(PVH(7, v7), CLO(0), CLO(1), CLO(2));
    CHI(0)(); CHI(1)(); CHI(2)();
    s.orv = 0;
#pragma unroll
    for (int nb = 0; nb < NB; ++nb) { s.orv = aor3(s.orv, s.pf[nb][0], s.pf[nb][1]); s.orv = aor3(s.orv, s.pf[nb][2], s.pf[nb][3]); }
    SB();
    check<NB, false>(s, c.rk, c.hs * 131072 + ((15 + c.rot) & 15) * 8192);
    SB();
    if constexpr (NB == 3) lz = laneid();
#pragma unroll
    for (int db = 4; db < 8; ++db) vf[db] = rv(db);
#pragma unroll
    for (int nb = 0; nb < NB; ++nb) s.l[nb] = mfma(s.ones, asb(s.pf[nb]), s.l[nb]);
#pragma unroll
    for (int db = 0; db < 8; ++db)
#pragma unroll
      for (int nb = 0; nb < NB; ++nb) {
        s.o[nb][db] = mfma(vf[db], asb(s.pf[nb]), s.o[nb][db]);
        if constexpr (NB == 2) { const int p = db * 2 + nb; if (p < 15) { SB(); st128(wld(p / 5, p % 5) + lz * 16, wr[p]); SB(); } }
      }
  }
  STAMP(5);
  asm volatile("s_waitcnt lgkmcnt(0)\n\ts_barrier" ::: "memory");
#pragma unroll
  for (int nb = 0; nb < NB; ++nb) {
    const float inv = __builtin_amdgcn_rcpf(s.l[nb][0]);
#pragma unroll
    for (int e = 0; e < 4; ++e) {
      const f32x4 a0 = s.o[nb][2 * e] * inv, a1 = s.o[nb][2 * e + 1] * inv;
      const u32x4 w = {cvtpk(a0[0], a0[1]), cvtpk(a0[2], a0[3]), cvtpk(a1[0], a1[1]), cvtpk(a1[2], a1[3])};
      const int u = (c.hs * 2 + (e >> 1) - rotb) & 3;
      st128(lds0 + stg(u) + (e & 1) * 20480 + (c.blk0 + nb) * 1024 + lz * 16, w);
      __builtin_amdgcn_raw_buffer_store_b128(w, rof, lz * 16 + (pr * 8 + c.hs * 4 + e) * 10240 + (c.blk0 + nb) * 1024, 0, 16);
    }
  }
  STAMP(6);
  {
    constexpr int NM = NB, M0 = NM == 3 ? 0 : 3;
    const int q = c.j, qm = q & 1, qn = q >> 1, zg = lz >> 4;
    unsigned vo3[5];
#pragma unroll
    for (int n = 0; n < 5; ++n) vo3[n] = NB == 3 ? wvo(n) : lz * 16;
    auto dmal = [&](int i, int u, int n) {
      if constexpr (NB == 3) dma16<WONT>(wld(u, n), vo3[n], rwo, i < 16 ? wso(i) : OOB);
      else dma16<false>(lds0 + stg(u) + dks * 20480 + dh * 5120 + n * 1024, vo3[0], rof, i < 16 ? (kpair(i) * 2 + dks) * 10240 + (dh * 5 + n) * 1024 : OOB);
    };
    if constexpr (NB == 3) {
#pragma unroll
      for (int n = 0; n < 5; ++n) dmal(3, 3, n);
    }
    unsigned oa[2], wa[2][2];
    {
      oa[0] = lds0 + (qm * 5 + M0) * 1024 + lz * 16; oa[1] = oa[0] + 81920;
      const unsigned wb = lds0 + 10240 + (4 * zg + ((lz & 15) >> 2)) * 320 + (lz & 3) * 8 + qn * 160;
      const int sg = (qn ? -32 : 32) * (zg & 1);
      wa[0][0] = wb + sg; wa[0][1] = wb - sg; wa[1][0] = wa[0][0] + 81920; wa[1][1] = wa[0][1] + 81920;
      pin(oa[1]); pin(wa[1][0]); pin(wa[1][1]);
    }
    f32x4 acc[NM][5];
    bf16x8 h2, h3, h4, op[NM];
    auto interval = [&](auto UU, auto D, auto tail) {
      constexpr bool FIRST = decltype(UU)::value < 0;
      constexpr int u = FIRST ? 0 : decltype(UU)::value, so = ((u ^ (u >> 1)) & 1) * 40960;
      auto ro = [&](int ks, int m) { return ld128(oa[u >> 1] + so + ks * 20480 + m * 1024); };
      auto rw = [&](int ks, int n) { const unsigned a = wa[u >> 1][n & 1] + so + ks * 20480 + n * 32; return cat(ldtr(a), ldtr(a + 5120)); };
      auto mm = [&](auto Z, int n, bf16x8& wf, bf16x8 (&of)[NM], auto g0, auto g1, auto g2) {
        constexpr int z = decltype(Z)::value;
        if constexpr (z == 2) { g0(); g1(); g2(); SB(); }
        else {
          amf<z == 1>(acc[0][n], wf, of[0]); SB(); g0(); SB(); amf<z == 1>(acc[1][n], wf, of[1]); SB(); g1(); SB();
          if constexpr (NM == 3) { amf<z == 1>(acc[2][n], wf, of[2]); SB(); }
          g2(); SB();
        }
      };
      constexpr IC<FIRST ? 2 : 0> H{};
      constexpr IC<FIRST ? 1 : 0> C{};
      constexpr IC<0> P{};
      bf16x8 o0[NM], o1[NM], a0, a1, a2, a3, a4, b0, b1;
      mm(H, 2, h2, op, [&] { a0 = rw(0, 0); }, [&] { o0[0] = ro(0, 0); o0[1] = ro(0, 1); }, [&] { if constexpr (NM == 3) o0[2] = ro(0, 2); });
      mm(H, 3, h3, op, [&] { a1 = rw(0, 1); }, [&] { a2 = rw(0, 2); }, [&] { D(0); });
      mm(H, 4, h4, op, [&] { a3 = rw(0, 3); }, [&] { a4 = rw(0, 4); }, [&] { use(o0[NM - 1]); });
      mm(C, 0, a0, o0, [&] { o1[0] = ro(1, 0); o1[1] = ro(1, 1); }, [&] { use(a1); if constexpr (NM == 3) o1[2] = ro(1, 2); }, [&] { D(1); });
      mm(C, 1, a1, o0, [&] { b0 = rw(1, 0); }, [&] { use(a2); b1 = rw(1, 1); }, N0);
      mm(C, 2, a2, o0, [&] { h2 = rw(1, 2); }, [&] { use(a3); }, [&] { D(2); });
      mm(C, 3, a3, o0, [&] { h3 = rw(1, 3); }, [&] { use(a4); }, N0);
      mm(C, 4, a4, o0, [&] { h4 = rw(1, 4); }, [&] { use(b0); }, [&] { D(3); });
      mm(P, 0, b0, o1, N0, [&] { use(b1); }, [&] { D(4); });
      mm(P, 1, b1, o1, [&] { use(h4); }, N0, N0);
      tail();
#pragma unroll
      for (int m = 0; m < NM; ++m) op[m] = o1[m];
    };
    auto ND = [](int) {};
    SB();
    wait_vm_bar<63>();
    interval(IC<-1>{}, ND, N0);
    wait_vm_bar<63>();
    STAMP(7);
    interval(IC<1>{}, [&](int n) { if constexpr (NB == 3) dmal(4, 0, n); }, N0);
    if constexpr (NB == 3) wait_vm_bar<10>(); else wait_vm_bar<0>();
    if (c.w == 3 && lz == 0) __hip_atomic_store(sync + (1 + c.id) * FS, tag, RLX_AGENT);
    interval(IC<2>{}, [&](int n) { if constexpr (NB == 3) dmal(5, 1, n); }, [&] {
      if (c.w == 2) {
        const int pid = c.id ^ (((lz & 3) ? (lz & 3) : 1) << 3);
        for (unsigned spins = 0; spins < (1u << 14); ++spins) {
          if (__all(__hip_atomic_load(sync + (1 + pid) * FS, RLX_AGENT) == tag)) break;
          __builtin_amdgcn_s_sleep(1);
        }
        __builtin_amdgcn_fence(__ATOMIC_ACQUIRE, "agent");
        asm volatile("s_waitcnt vmcnt(0)" ::: "memory");
      }
    });
    if constexpr (NB == 3) wait_vm_bar<10>(); else wait_vm_bar<63>();
    STAMP(8);
    interval(IC<3>{}, [&](int n) { if constexpr (NB == 3) dmal(6, 2, n); else { dmal(4, 0, n); dmal(5, 1, n); } }, N0);
    STAMP(9);
#pragma unroll 1
    for (int i4 = 4; i4 < 16; i4 += 4)
      static_for(std::make_integer_sequence<int, 4>{}, [&](auto UU) {
        constexpr int u = decltype(UU)::value;
        if constexpr (NB == 3) wait_vm_bar<10>(); else { if (u == 0 && i4 == 4) wait_vm_bar<0>(); else wait_vm_bar<5>(); }
        interval(UU, [&](int n) { if constexpr (NB == 3) dmal(i4 + u + 3, (u + 3) & 3, n); else dmal(i4 + u + 2, (u + 2) & 3, n); }, N0);
      });
#pragma unroll
    for (int m = 0; m < NM; ++m) { amfma(acc[m][2], h2, op[m]); amfma(acc[m][3], h3, op[m]); amfma(acc[m][4], h4, op[m]); }
    asm volatile("s_nop 7");
    STAMP(10);
    if (q != hg) {
      const int so = (q * 3 + ((hg - q - 1) & 3)) * QB;
#pragma unroll
      for (int e = 0; e < NM * 5; e += 2) {
        const f32x4 a = acc[e / 5][e % 5], b = e + 1 < NM * 5 ? acc[(e + 1) / 5][(e + 1) % 5] : z4;
        __builtin_amdgcn_raw_buffer_store_b128(u32x4{cvtpk(a[0], a[1]), cvtpk(a[2], a[3]), cvtpk(b[0], b[1]), cvtpk(b[2], b[3])}, rslab, lz * 16 + ((M0 ? 8 : 0) + e / 2) * 1024 + so, 0, 16);
      }
    }
    asm volatile("s_waitcnt vmcnt(0) lgkmcnt(0)\n\ts_barrier" ::: "memory");
    if (c.w == 0 && lz == 0) __hip_atomic_store(sync + (257 + c.id) * FS, tag, RLX_AGENT);
    STAMP(11);
    if (q == hg) {
#pragma unroll
      for (int e = 0; e < NM * 5; ++e) st128(lds0 + lz * 16 + (M0 * 5 + e) * 1024, __builtin_bit_cast(u32x4, acc[e / 5][e % 5]));
    }
    if (c.w == ((hg + 1) & 3)) {
      const int pid = c.id ^ ((lz & 3) ? (lz & 3) : 1);
      for (unsigned spins = 0; spins < (1u << 14); ++spins) {
        if (__all(__hip_atomic_load(sync + (257 + pid) * FS, RLX_AGENT) == tag)) break;
        __builtin_amdgcn_s_sleep(1);
      }
    }
    if (c.id == 0 && c.w == 7) {
      for (unsigned spins = 0; spins < (1u << 14); ++spins) {
        bool ok = true;
#pragma unroll
        for (int k = 0; k < 4; ++k) ok &= __hip_atomic_load(sync + (1 + lz + 64 * k) * FS, RLX_AGENT) == tag;
        if (__all(ok)) break;
        __builtin_amdgcn_s_sleep(1);
      }
      if (lz == 0) __hip_atomic_store(sync, tag, RLX_AGENT);
    }
    asm volatile("s_waitcnt vmcnt(0) lgkmcnt(0)\n\ts_barrier" ::: "memory");
    STAMP(12);
    u16* op16 = ob + (size_t)(lz & 15) * 1280 + 4 * zg;
    u32x4 r[2][3];
#pragma unroll
    for (int k = 0; k < 2; ++k)
#pragma unroll
      for (int sl = 0; sl < 3; ++sl) r[k][sl] = __builtin_amdgcn_raw_buffer_load_b128(rslab, c.w + 8 * k < 13 ? lz * 16 + (c.w + 8 * k) * 1024 : OOB, (hg * 3 + sl) * QB, 16);
#pragma unroll
    for (int k = 0; k < 2; ++k) {
      const int un = c.w + 8 * k;
#pragma unroll
      for (int hh = 0; hh < 2; ++hh) {
        const int e = un < 8 ? 2 * un + hh : 15 + 2 * (un - 8) + hh;
        if (un < 13 && !(un == 7 && hh == 1)) {
          f32x4 v = *(const LDS3 f32x4*)(size_t)(lds0 + lz * 16 + e * 1024);
#pragma unroll
          for (int sl = 0; sl < 3; ++sl) { const unsigned lo = r[k][sl][2 * hh], hi = r[k][sl][2 * hh + 1];
            v += f32x4{__uint_as_float(lo << 16), __uint_as_float(lo & 0xffff0000u), __uint_as_float(hi << 16), __uint_as_float(hi & 0xffff0000u)}; }
          const int m = e / 5, n = e - m * 5;
          *(u32x2*)(op16 + m * (16 * 1280) + n * 16) = u32x2{cvtpk(v[0], v[1]), cvtpk(v[2], v[3])};
        }
      }
    }
  }
}

__global__ __launch_bounds__(512, 2) void fused(const u16* xA, const u16* kA, const u16* vA, const u16* wqA, const u16* woA,
                                                 const u16* xB, const u16* kB, const u16* vB, const u16* wqB, const u16* woB, u16* out, char* ws) {
  extern __shared__ __attribute__((aligned(16))) char smem[];
  const int id = blockIdx.x, xg = id & 7, cc = id >> 3, br = xg >> 2, hg = xg & 3, b = cc >> 2, pr = cc & 3, h0 = hg * 8 + pr * 2;
  const int w = __builtin_amdgcn_readfirstlane(threadIdx.x >> 6);
  asm volatile("s_nop 4");
  Ctx c;
  const u16* x = br ? xB : xA; const u16* k = br ? kB : kA; const u16* v = br ? vB : vA; const u16* wq = br ? wqB : wqA;
  c.rx = mkr(x + (size_t)b * SQ * DM, SQ * DM * 2);
  c.rw = mkr(wq + h0 * DH, DM * NH * DH * 2 - h0 * DH * 2);
  c.rk = mkr(k + (size_t)(b * NH + h0) * SK * DH, 2 * SK * DH * 2);
  c.rv = mkr(v + (size_t)(b * NH + h0) * SK * DH, 2 * SK * DH * 2);
  c.lds0 = (unsigned)(size_t)(LDS3 char*)smem;
  c.hs = w >> 2; c.j = w & 3; c.rot = (id * 5) & 15; c.krot = ((10 * pr + 5 * b) >> 2) % 10;
  c.w = w; c.id = id; c.wo = br ? woB : woA; c.out = out; c.ws = ws;
  const bool big = (c.j < 2) == (c.hs == 0);
  c.blk0 = c.hs == 0 ? (c.j < 2 ? 3 * c.j : 2 + 2 * c.j) : (c.j < 2 ? 2 * c.j : 3 * c.j - 2);
  const unsigned long long t0_ = __builtin_amdgcn_s_memrealtime();
  if (big) body<3>(c); else body<2>(c);
  while (__builtin_amdgcn_s_memrealtime() - t0_ < 6400ull) __builtin_amdgcn_s_sleep(2);
}

extern "C" void kernel_launch(void* const* d_in, const int* in_sizes, int n_in, void* d_out, int out_size, void* d_ws, size_t ws_size, hipStream_t stream) {
  (void)hipFuncSetAttribute((const void*)fused, hipFuncAttributeMaxDynamicSharedMemorySize, LDS_BYTES);
  fused<<<256, 512, LDS_BYTES, stream>>>((const u16*)d_in[0], (const u16*)d_in[1], (const u16*)d_in[2], (const u16*)d_in[3], (const u16*)d_in[4],
                                         (const u16*)d_in[5], (const u16*)d_in[6], (const u16*)d_in[7], (const u16*)d_in[8], (const u16*)d_in[9], (u16*)d_out, (char*)d_ws);
}
```

```cpp
#include <hip/hip_runtime.h>
#include <hip/hip_bf16.h>
#include <type_traits>

typedef __bf16 bf16x8 __attribute__((ext_vector_type(8)));
typedef __bf16 bf16x4 __attribute__((ext_vector_type(4)));
typedef __bf16 bf16x2 __attribute__((ext_vector_type(2)));
typedef float f32x4 __attribute__((ext_vector_type(4)));
typedef float f32x2 __attribute__((ext_vector_type(2)));
typedef unsigned u32x4 __attribute__((ext_vector_type(4)));
typedef unsigned u32x2 __attribute__((ext_vector_type(2)));
typedef unsigned short u16;
typedef __attribute__((address_space(1))) unsigned long long gu64;
typedef __amdgpu_buffer_rsrc_t rsrc_t;
#define LDS3 __attribute__((address_space(3)))
#define DI __device__ __forceinline__
#define SB() __builtin_amdgcn_sched_barrier(0)
#define RLX_AGENT __ATOMIC_RELAXED, __HIP_MEMORY_SCOPE_AGENT
template <int N> using IC = std::integral_constant<int, N>;

constexpr int SQ = 160, DM = 640, NH = 32, SK = 512, DH = 128;
constexpr int LDS_BYTES = 163840;
constexpr int KV_HS = 32768, V_OFF = 65536;
__host__ __device__ constexpr int xoff(int b) { return b * 65536 + 73728; }
__host__ __device__ constexpr int woff(int b) { return b == 0 ? 16384 : b == 1 ? 49152 : b == 2 ? 106496 : 122880; }
constexpr float QSCALE = 0.08838834764831845f * 1.4426950408889634f;
constexpr float BIAS = 10.f;
constexpr unsigned OOB = 0x7ff00000u;
constexpr size_t WS_SLAB = 32u << 20, WS_SYNC = 64u << 20;
constexpr int FS = 32;
constexpr int QB = 13 * 1024;
#define KVN0 1
#define THR 4
#define THRB THR
#define THRW 63
#define THRX 63
#define THR3 63
#define XNT false
#define WNT true
#define WONT false

DI unsigned cvtpk(float lo, float hi) { f32x2 v = {lo, hi}; bf16x2 b = __builtin_convertvector(v, bf16x2); return __builtin_bit_cast(unsigned, b); }
DI f32x4 mfma(bf16x8 a, bf16x8 b, f32x4 c) { return __builtin_amdgcn_mfma_f32_16x16x32_bf16(a, b, c, 0, 0, 0); }
DI bf16x8 ld128(unsigned addr) { return *(const LDS3 bf16x8*)(size_t)addr; }
DI void st128(unsigned addr, u32x4 v) { *(LDS3 u32x4*)(size_t)addr = v; }
DI bf16x4 ldtr(unsigned addr) { return __builtin_amdgcn_ds_read_tr16_b64_v4bf16((LDS3 bf16x4*)(size_t)addr); }
DI bf16x8 cat(bf16x4 a, bf16x4 b) { return __builtin_shufflevector(a, b, 0, 1, 2, 3, 4, 5, 6, 7); }
DI bf16x8 asb(u32x4 w) { return __builtin_bit_cast(bf16x8, w); }
DI rsrc_t mkr(const void* p, int bytes) { return __builtin_amdgcn_make_buffer_rsrc((void*)p, (short)0, bytes, 0x00020000); }
template <bool NT> DI void dma16(unsigned ldsaddr, unsigned voff, rsrc_t r, unsigned soff) {
  if constexpr (NT) asm volatile("s_mov_b32 m0, %0\n\ts_nop 0\n\tbuffer_load_dwordx4 %1, %2, %3 offen nt lds" :: "s"(ldsaddr), "v"(voff), "s"(r), "s"(soff) : "memory");
  else              asm volatile("s_mov_b32 m0, %0\n\ts_nop 0\n\tbuffer_load_dwordx4 %1, %2, %3 offen lds" :: "s"(ldsaddr), "v"(voff), "s"(r), "s"(soff) : "memory");
}
DI void setm0(unsigned v) { asm volatile("s_mov_b32 m0, %0" :: "s"(v) : "memory"); }
template <int POL, int OFF> DI void dmao(unsigned voff, rsrc_t r, unsigned soff) {
  if constexpr (POL == 1)      asm volatile("s_nop 0\n\tbuffer_load_dwordx4 %0, %1, %2 offen offset:%3 nt lds" :: "v"(voff), "s"(r), "s"(soff), "i"(OFF) : "memory");
  else if constexpr (POL == 2) asm volatile("s_nop 0\n\tbuffer_load_dwordx4 %0, %1, %2 offen offset:%3 sc1 lds" :: "v"(voff), "s"(r), "s"(soff), "i"(OFF) : "memory");
  else                         asm volatile("s_nop 0\n\tbuffer_load_dwordx4 %0, %1, %2 offen offset:%3 lds" :: "v"(voff), "s"(r), "s"(soff), "i"(OFF) : "memory");
}
template <int POL, int OFF> DI void dmao2(unsigned voff, rsrc_t r, unsigned soff) {
  if constexpr (POL == 1)      asm volatile("buffer_load_dwordx4 %0, %1, %2 offen offset:%3 nt lds" :: "v"(voff), "s"(r), "s"(soff), "i"(OFF) : "memory");
  else if constexpr (POL == 2) asm volatile("buffer_load_dwordx4 %0, %1, %2 offen offset:%3 sc1 lds" :: "v"(voff), "s"(r), "s"(soff), "i"(OFF) : "memory");
  else                         asm volatile("buffer_load_dwordx4 %0, %1, %2 offen offset:%3 lds" :: "v"(voff), "s"(r), "s"(soff), "i"(OFF) : "memory");
}
template <class T> DI void pins(T& v) { asm volatile("" : "+s"(v)); }
template <int N> DI void wait_vm_bar() { asm volatile("s_waitcnt vmcnt(%0) lgkmcnt(0)\n\ts_barrier" :: "i"(N) : "memory"); }
DI float xmax16(float v) { auto r = __builtin_amdgcn_permlane16_swap(__float_as_uint(v), __float_as_uint(v), false, false); return fmaxf(__uint_as_float(r[0]), __uint_as_float(r[1])); }
DI float xmax32(float v) { auto r = __builtin_amdgcn_permlane32_swap(__float_as_uint(v), __float_as_uint(v), false, false); return fmaxf(__uint_as_float(r[0]), __uint_as_float(r[1])); }
DI void amfma(f32x4& acc, bf16x8 a, bf16x8 b) { asm volatile("v_mfma_f32_16x16x32_bf16 %0, %1, %2, %0" : "+v"(acc) : "v"(a), "v"(b)); }
DI void amfma_c(f32x4& d, bf16x8 a, bf16x8 b, f32x4 cc) { asm volatile("v_mfma_f32_16x16x32_bf16 %0, %1, %2, %3" : "=&v"(d) : "v"(a), "v"(b), "v"(cc)); }
DI void amfma_z(f32x4& d, bf16x8 a, bf16x8 b) { asm volatile("v_mfma_f32_16x16x32_bf16 %0, %1, %2, 0" : "=&v"(d) : "v"(a), "v"(b)); }
template <bool Z> DI void amf(f32x4& d, bf16x8 a, bf16x8 b) { if constexpr (Z) amfma_z(d, a, b); else amfma(d, a, b); }
DI float aexp(float x) { asm volatile("v_exp_f32 %0, %0" : "+v"(x)); return x; }
DI float amul(float a, float b) { float r; asm("v_mul_f32 %0, %1, %2" : "=v"(r) : "s"(b), "v"(a)); return r; }
DI unsigned aor3(unsigned a, unsigned b, unsigned c) { unsigned r; asm volatile("v_or3_b32 %0, %1, %2, %3" : "=v"(r) : "v"(a), "v"(b), "v"(c)); return r; }
DI unsigned acvt(float lo, float hi) { unsigned r; asm volatile("v_cvt_pk_bf16_f32 %0, %1, %2" : "=v"(r) : "v"(lo), "v"(hi)); return r; }
DI void use(bf16x8 f) { asm volatile("" :: "v"(f)); }
DI void aaddu(unsigned& a, int d) { asm volatile("v_add_u32 %0, %1, %0" : "+v"(a) : "s"(d)); }
DI unsigned laneid() { unsigned l; asm volatile("v_mbcnt_lo_u32_b32 %0, -1, 0\n\tv_mbcnt_hi_u32_b32 %0, -1, %0" : "=v"(l)); return l; }
template <class T> DI void pin(T& v) { asm volatile("" : "+v"(v)); }

#define STAMP(i)

struct Ctx {
  rsrc_t rx, rw, rk, rv;
  unsigned lds0;
  int j, hs, blk0, rot, krot, w, id;
  const u16* wo; u16* out; char* ws;
};
template <int NB> struct St {
  f32x4 o[NB][8], negm[NB], S0[NB], S1[NB], l[NB];
  bf16x8 qf[NB][4], ones;
  u32x4 pf[NB];
  unsigned orv;
};
template <int NB, int E> DI void exp1(f32x4 (&S)[NB]) { if constexpr (E >= 0 && E < 4 * NB) S[E % NB][E / NB] = aexp(S[E % NB][E / NB]); }
template <int NB, int LO, int HI> DI void exps(f32x4 (&S)[NB]) { if constexpr (LO < HI) { exp1<NB, LO>(S); exps<NB, LO + 1, HI>(S); } }
DI float max8(f32x4 a, f32x4 b) { return fmaxf(fmaxf(fmaxf(a[0], a[1]), fmaxf(a[2], a[3])), fmaxf(fmaxf(b[0], b[1]), fmaxf(b[2], b[3]))); }

template <int NB, bool HAS_NEXT> DI void refix(St<NB>& s, rsrc_t rk, int ksoff) {
  const unsigned ln = laneid();
  const int kvo = (ln & 15) * 256 + ((((ln >> 4) & 1) << 1) | (ln >> 5)) * 16;
  asm volatile("s_nop 7");
#pragma unroll
  for (int nb = 0; nb < NB; ++nb) {
    f32x4 a = s.negm[nb], b = s.negm[nb];
#pragma unroll
    for (int st = 0; st < 4; ++st) {
      a = mfma(asb(__builtin_amdgcn_raw_buffer_load_b128(rk, kvo + st * 64, ksoff, 0)), s.qf[nb][st], a);
      SB();
      b = mfma(asb(__builtin_amdgcn_raw_buffer_load_b128(rk, kvo + st * 64, ksoff + 4096, 0)), s.qf[nb][st], b);
      SB();
    }
    const float d = fmaxf(xmax32(xmax16(max8(a, b))) + BIAS, 0.f);
    const float f = __builtin_amdgcn_exp2f(-d);
    s.negm[nb] -= d; if (HAS_NEXT) s.S0[nb] -= d;
    s.l[nb] *= f;
#pragma unroll
    for (int db = 0; db < 8; ++db) s.o[nb][db] *= f;
#pragma unroll
    for (int r = 0; r < 4; ++r) { a[r] = __builtin_amdgcn_exp2f(a[r] - d); b[r] = __builtin_amdgcn_exp2f(b[r] - d); }
    u32x4 w = {cvtpk(a[0], a[1]), cvtpk(a[2], a[3]), cvtpk(b[0], b[1]), cvtpk(b[2], b[3])};
    s.pf[nb] = w;
    SB();
  }
  asm volatile("s_nop 1");
}
template <int NB, bool HAS_NEXT> DI void check(St<NB>& s, rsrc_t rk, int ksoff) {
  if (__builtin_expect(__any((s.orv & 0x40004000u) != 0u), 0)) refix<NB, HAS_NEXT>(s, rk, ksoff);
}
template <int... I, class F> DI void static_for(std::integer_sequence<int, I...>, F f) { (f(IC<I>{}), ...); }

template <int NB> DI void body(const Ctx& c) {
  const int lane = threadIdx.x & 63, l15 = lane & 15, g = lane >> 4;
  const unsigned lds0 = c.lds0;
  const f32x4 z4 = {0.f, 0.f, 0.f, 0.f};
  auto N0 = [] {};
  unsigned voffK[2], voffV[2];
#pragma unroll
  for (int h = 0; h < 2; ++h) {
    voffK[h] = (lane >> 4) * 256 + (((lane & 15) ^ (8 * (c.j & 1) + 4 * h + (lane >> 4))) * 16);
    voffV[h] = ((lane >> 1) & 7) * 256 + (lane >> 4) * 32 + (lane & 1) * 16 + (h ? 128 : 1024);
  }
  auto kvl = [&](int t, int q) { return lds0 + (q >> 1) * KV_HS + (t & 3) * 8192 + c.j * 2048 + (q & 1) * 1024; };
  auto tso = [&](int t) { return (unsigned)(((t + c.rot) & 15) * 8192 + c.j * 2048); };
  auto dma_kv = [&](bool isv, unsigned lb, unsigned so, int q) {
    if (q & 1) { if (isv) dmao2<1, 1024>(voffV[1], c.rv, so + (q >> 1) * 131072); else dmao2<1, 1024>(voffK[1], c.rk, so + (q >> 1) * 131072); }
    else { setm0(lb + (q >> 1) * KV_HS); if (isv) dmao<1, 0>(voffV[0], c.rv, so + (q >> 1) * 131072); else dmao<1, 0>(voffK[0], c.rk, so + (q >> 1) * 131072); }
  };
  auto ld_k = [&](int t, int q) { return __builtin_amdgcn_raw_buffer_load_b128(c.rk, voffK[q & 1] + (q & 1) * 1024, tso(t) + (q >> 1) * 131072, 2); };
  auto ld_v = [&](int t, int q) { return __builtin_amdgcn_raw_buffer_load_b128(c.rv, voffV[q & 1] + (q & 1) * 1024, tso(t) + (q >> 1) * 131072, 2); };
  STAMP(0);
  St<NB> s;
  const unsigned long long epoch = __hip_atomic_load((gu64*)(c.ws + WS_SYNC), RLX_AGENT);
  u32x4 kvr[NB == 2 ? 20 : 4];
  {
    unsigned voffS;
    if constexpr (NB == 3) { const int f = (2 * (c.j & 1) + (lane >> 5)) | ((c.j >> 1) << 2); voffS = (lane >> 5) * 8192 + (((lane & 31) ^ (f << 1)) * 16); }
    else voffS = (lane >> 3) * 1280 + (((lane & 7) ^ (4 * (c.j & 1) + g)) * 16);
    auto dma_w = [&](int st, int buf, int n) {
      int ks = st + 2 * c.krot; ks = ks >= 20 ? ks - 20 : ks;
      const int i = (c.j & 1) + 4 * (c.j >> 1) + 2 * (n & 1) + 8 * (n >> 1);
      if (THRW < 63) asm volatile("s_waitcnt vmcnt(%0)" :: "i"(THRW) : "memory");
      dma16<WNT>(lds0 + woff(buf) + i * 1024, voffS, c.rw, (ks * 32 + 2 * i) * 8192);
    };
    auto dma_x = [&](int ch, int n) {
      int kc = ch + c.krot; kc = kc >= 10 ? kc - 10 : kc;
      const int i = c.j + 4 * n;
      if (THRX < 63) asm volatile("s_waitcnt vmcnt(%0)" :: "i"(THRX) : "memory");
      dma16<XNT>(lds0 + xoff(ch & 1) + i * 1024, voffS, c.rx, i * (8 * 1280) + kc * 128);
    };
    auto extra = [&](int m, int q) {
      if (THRX < 63 && m >= 0 && m < 8) asm volatile("s_waitcnt vmcnt(%0)" :: "i"(THRX) : "memory");
      if (m == 0 || m == 1) dma_kv(false, kvl(m, 0), tso(m), q);
      else if (m == 2) dma_kv(true, V_OFF + kvl(0, 0), tso(0), q);
      else if (m == 3 || m == 5 || m == 7) kvr[(m - 3) * 4 + q] = ld_k((m + 1) / 2, q);
      else if (m == 4 || m == 6) kvr[(m - 3) * 4 + q] = ld_v((m - 2) / 2, q);
    };
    if constexpr (NB == 3) {
#pragma unroll
      for (int st = 0; st < 3; ++st)
#pragma unroll
        for (int n = 0; n < 4; ++n) dma_w(st, st, n);
    } else {
#pragma unroll
      for (int n = 0; n < 5; ++n) dma_x(0, n);
      __builtin_amdgcn_s_setprio(1);
    }
    unsigned wa[2][8], xa[4];
    const int qq = l15 >> 2, p = lane & 3, f = qq | ((g & 1) << 2);
#pragma unroll
    for (int db = 0; db < 8; ++db) { wa[0][db] = lds0 + woff(0) + (8 * g + qq) * 512 + c.hs * 256 + ((db ^ f) * 32) + p * 8; wa[1][db] = wa[0][db] + (woff(2) - woff(0)); pin(wa[1][db]); }
#pragma unroll
    for (int u = 0; u < 4; ++u) { xa[u] = lds0 + xoff(u >> 1) + (c.blk0 * 16 + l15) * 128 + (((4 * (u & 1) + g) ^ ((l15 >> 1) & 7)) * 16); pin(xa[u]); }
    bf16x8 w4, w5, w6, w7, xp[NB];
    auto qstep = [&](auto U, auto PH, auto D) {
      constexpr int u = decltype(U)::value, ph = decltype(PH)::value;
      auto rw = [&](int db) { const unsigned a = wa[u >> 1][db] + (woff(u) - woff(u & 2)); return cat(ldtr(a), ldtr(a + 2048)); };
      auto rx = [&](int nb) { return ld128(xa[u] + nb * 2048); };
      auto mm = [&](auto Z, int db, bf16x8& wf, bf16x8 (&xf)[NB], auto g0, auto g1, auto g2) {
        constexpr int z = decltype(Z)::value;
        if constexpr (z == 2) { g0(); g1(); g2(); SB(); }
        else {
          amf<z == 1>(s.o[0][db], wf, xf[0]); SB(); g0(); SB();
          amf<z == 1>(s.o[1][db], wf, xf[1]); SB(); g1(); SB();
          if constexpr (NB == 3) { amf<z == 1>(s.o[2][db], wf, xf[2]); SB(); }
          g2(); SB();
        }
      };
      constexpr IC<ph == 0 ? 2 : ph == 1 ? 1 : 0> H{};
      constexpr IC<ph == 0 ? 1 : 0> C{};
      bf16x8 xf[NB], w0, w1, w2, w3;
      if constexpr (NB == 3) {
        mm(H, 4, w4, xp, [&] { w0 = rw(0); }, [&] { xf[0] = rx(0); xf[1] = rx(1); }, [&] { xf[2] = rx(2); });
        mm(H, 5, w5, xp, [&] { w1 = rw(1); }, [&] { D(0); }, N0);
        mm(H, 6, w6, xp, [&] { w2 = rw(2); }, [&] { w3 = rw(3); }, N0);
        mm(H, 7, w7, xp, [&] { w4 = rw(4); }, [&] { D(1); }, [&] { use(xf[NB - 1]); });
        mm(C, 0, w0, xf, [&] { w5 = rw(5); }, [&] { use(w1); w6 = rw(6); }, N0);
        mm(C, 1, w1, xf, [&] { w7 = rw(7); }, [&] { use(w2); D(2); }, N0);
        mm(C, 2, w2, xf, [&] { use(w3); }, [&] { D(3); }, N0);
        mm(C, 3, w3, xf, [&] { use(w7); }, N0, N0);
      } else {
        mm(H, 4, w4, xp, [&] { w0 = rw(0); }, [&] { xf[0] = rx(0); xf[1] = rx(1); }, N0);
        mm(H, 5, w5, xp, [&] { w1 = rw(1); }, [&] { D(0); }, N0);
        mm(H, 6, w6, xp, [&] { w2 = rw(2); }, [&] { w3 = rw(3); D(1); }, N0);
        mm(H, 7, w7, xp, [&] { w4 = rw(4); }, [&] { D(2); use(xf[NB - 1]); }, N0);
        mm(C, 0, w0, xf, [&] { w5 = rw(5); }, [&] { use(w1); w6 = rw(6); D(3); }, N0);
        mm(C, 1, w1, xf, [&] { w7 = rw(7); }, [&] { use(w2); D(4); }, N0);
        mm(C, 2, w2, xf, [&] { use(w3); }, N0, N0);
        mm(C, 3, w3, xf, [&] { use(w7); }, N0, N0);
      }
#pragma unroll
      for (int nb = 0; nb < NB; ++nb) xp[nb] = xf[nb];
    };
    STAMP(1);
    SB();
    if constexpr (NB == 3) {
      static_for(std::make_integer_sequence<int, 4>{}, [&](auto U) {
        constexpr int u = decltype(U)::value;
        wait_vm_bar<8>(); qstep(U, IC<(u < 2 ? u : 2)>{}, [&](int n) { dma_w(u + 3, (u + 3) & 3, n); });
      });
#pragma unroll 1
      for (int s4 = 4; s4 < 16; s4 += 4)
        static_for(std::make_integer_sequence<int, 4>{}, [&](auto U) {
          constexpr int u = decltype(U)::value;
          wait_vm_bar<8>(); qstep(U, IC<2>{}, [&](int n) { dma_w(s4 + u + 3, (u + 3) & 3, n); });
        });
      wait_vm_bar<8>(); qstep(IC<0>{}, IC<2>{}, [&](int n) { dma_w(19, 3, n); });
      wait_vm_bar<8>(); qstep(IC<1>{}, IC<2>{}, [&](int n) { if (n < 4) kvr[n] = ld_v(3, n); });
      wait_vm_bar<8>(); qstep(IC<2>{}, IC<2>{}, [&](int) {});
      wait_vm_bar<4>(); qstep(IC<3>{}, IC<2>{}, [&](int) {});
    } else {
      static_for(std::make_integer_sequence<int, 20>{}, [&](auto ST) {
        constexpr int st = decltype(ST)::value, mp = (st >> 1) - 1 - KVN0;
        if constexpr (st & 1) wait_vm_bar<63>(); else wait_vm_bar<(mp >= 0 && mp < 8) ? 4 : 0>();
        qstep(IC<(st & 3)>{}, IC<(st < 2 ? st : 2)>{}, [&](int n) {
          if constexpr (st & 1) { if (n < 4) extra((st >> 1) - KVN0, n); } else if constexpr ((st >> 1) + 1 < 10) dma_x((st >> 1) + 1, n); });
      });
    }
#pragma unroll
    for (int nb = 0; nb < NB; ++nb) { amfma(s.o[nb][4], w4, xp[nb]); amfma(s.o[nb][5], w5, xp[nb]); amfma(s.o[nb][6], w6, xp[nb]); amfma(s.o[nb][7], w7, xp[nb]); }
    asm volatile("s_nop 7");
    SB();
  }
  STAMP(2);
  auto qconv = [&] {
#pragma unroll
    for (int nb = 0; nb < NB; ++nb)
#pragma unroll
      for (int e = 0; e < 4; ++e) {
        f32x4 a0 = s.o[nb][2 * e], a1 = s.o[nb][2 * e + 1];
#pragma unroll
        for (int r = 0; r < 4; ++r) { a0[r] = amul(a0[r], QSCALE); a1[r] = amul(a1[r], QSCALE); }
        auto r0 = __builtin_amdgcn_permlane16_swap(cvtpk(a0[0], a0[1]), cvtpk(a1[0], a1[1]), false, false);
        auto r1 = __builtin_amdgcn_permlane16_swap(cvtpk(a0[2], a0[3]), cvtpk(a1[2], a1[3]), false, false);
        u32x4 w = {r0[0], r1[0], r0[1], r1[1]};
        s.qf[nb][e] = asb(w); pin(s.qf[nb][e]);
      }
  };
  if constexpr (NB == 2) qconv();
  wait_vm_bar<0>();
  const unsigned long long tag = (((unsigned long long)__builtin_amdgcn_readfirstlane((unsigned)(epoch >> 32)) << 32) | __builtin_amdgcn_readfirstlane((unsigned)epoch)) + 1ull;
  const int pg = ((g & 1) << 1) | (g >> 1);
  unsigned ka[4];
#pragma unroll
  for (int st = 0; st < 4; ++st) ka[st] = lds0 + c.hs * KV_HS + l15 * 256 + (((4 * st + pg) ^ l15) * 16);
  unsigned va = lds0 + V_OFF + c.hs * KV_HS + (g >> 1) * 2048 + (4 * (g & 1) + (l15 >> 2)) * 32 + (lane & 3) * 8;
  auto rk = [&](int kt, int st) { return ld128(ka[st] + kt * 4096); };
  auto rv = [&](int db) { const unsigned a = va + (db >> 2) * 1024 + (db & 3) * 256; return cat(ldtr(a), ldtr(a + 4096)); };
  {
    bf16x8 kf0[4], kf1[4];
#pragma unroll
    for (int st = 0; st < 4; ++st) { kf0[st] = rk(0, st); kf1[st] = rk(1, st); }
    const unsigned wb = lds0 + c.j * 2048 + lane * 16;
    if constexpr (NB == 3) {
#pragma unroll
      for (int q = 0; q < 4; ++q) st128(wb + V_OFF + 3 * 8192 + (q >> 1) * KV_HS + (q & 1) * 1024, kvr[q]);
      SB();
      qconv();
    }
#pragma unroll
    for (int st = 0; st < 4; ++st)
#pragma unroll
      for (int nb = 0; nb < NB; ++nb) {
        s.S0[nb] = mfma(kf0[st], s.qf[nb][st], st == 0 ? z4 : s.S0[nb]); s.S1[nb] = mfma(kf1[st], s.qf[nb][st], st == 0 ? z4 : s.S1[nb]);
        if constexpr (NB == 2) {
#pragma unroll
          for (int h = 0; h < 2; ++h) { const int i = (st * 2 + nb) * 2 + h, m = i >> 2, q = i & 3;
            st128(wb + ((m & 1) ? V_OFF + (1 + (m >> 1)) * 8192 : (2 + (m >> 1)) * 8192) + (q >> 1) * KV_HS + (q & 1) * 1024, kvr[i]); }
          SB();
        }
      }
#pragma unroll
    for (int nb = 0; nb < NB; ++nb) {
#pragma unroll
      for (int db = (NB == 3 ? 6 : 5); db < 8; ++db) { s.o[nb][db] = z4; pin(s.o[nb][db]); }
      const float m = xmax32(xmax16(max8(s.S0[nb], s.S1[nb]))) + BIAS;
      s.negm[nb] = z4 - m; s.S0[nb] -= m; s.S1[nb] -= m;
      pin(s.negm[nb]);
    }
    s.ones = asb(u32x4{0x3f803f80u, 0x3f803f80u, 0x3f803f80u, 0x3f803f80u});
    pin(s.ones);
  }
  exps<NB, 0, 4 * NB>(s.S0); exps<NB, 0, 2 * NB>(s.S1);
  bf16x8 v5h, v6, v7;
#pragma unroll
  for (int st = 0; st < 4; ++st) ka[st] += 8192;
  SB();
  auto us = [&](auto m, auto g0, auto g1, auto g2) {
    m(0); SB(); g0(); SB(); m(1); SB(); g1(); SB();
    if constexpr (NB == 3) { m(2); SB(); g2(); SB(); }
  };
  auto QK0 = [&](int st, bf16x8 fr) { return [&s, st, fr](int nb) { if (st == 0) amfma_c(s.S0[nb], fr, s.qf[nb][0], s.negm[nb]); else amfma(s.S0[nb], fr, s.qf[nb][st]); }; };
  auto QK1 = [&](int st, bf16x8 fr) { return [&s, st, fr](int nb) { if (st == 0) amfma_c(s.S1[nb], fr, s.qf[nb][0], s.negm[nb]); else amfma(s.S1[nb], fr, s.qf[nb][st]); }; };
  auto PVH = [&](int db, bf16x8& fr) { return [&s, db, &fr](int nb) { amfma(s.o[nb][db], fr, asb(s.pf[nb])); }; };
  auto N = [] {};
#define E0(e) [&] { exp1<NB, (e)>(s.S0); }
#define E1(e) [&] { exp1<NB, (e)>(s.S1); }
#define CLO(nb) [&] { if constexpr ((nb) < NB) { s.pf[nb][0] = acvt(s.S0[nb][0], s.S0[nb][1]); s.pf[nb][1] = acvt(s.S0[nb][2], s.S0[nb][3]); } }
#define CHI(nb) [&] { if constexpr ((nb) < NB) { s.pf[nb][2] = acvt(s.S1[nb][0], s.S1[nb][1]); s.pf[nb][3] = acvt(s.S1[nb][2], s.S1[nb][3]); } }
#define U(f, ...) [&] { use(f); SB(); (__VA_ARGS__)(); }
  auto astep = [&](auto FI, int t, auto kvh) {
    constexpr bool FIRST = decltype(FI)::value;
    const int dk = (t & 3) == 3 ? -24576 : 8192, dv = (t & 3) == 0 ? -24576 : 8192;
    if constexpr (NB == 2) wait_vm_bar<16>(); else wait_vm_bar<63>();
    auto PH = [&](int db, bf16x8& fr) { return [&, db](int nb) { if constexpr (!FIRST) amfma(s.o[nb][db], fr, asb(s.pf[nb])); }; };
    auto PV = [&](int db, bf16x8 fr) { return [&s, db, fr](int nb) { amf<FIRST>(s.o[nb][db], fr, asb(s.pf[nb])); }; };
    auto SUM = [&](int nb) { amf<FIRST>(s.l[nb], s.ones, asb(s.pf[nb])); };
    bf16x8 a0, a1, a2, a3, b0, b1, b2, b3, v0, v1, v2, v3, v4;
    unsigned o1, o2;
    if constexpr (NB == 3) {
      bf16x8 v5;
      us(PH(6, v6), [&] { a0 = rk(0, 0); a1 = rk(0, 1); }, [&] { a2 = rk(0, 2); a3 = rk(0, 3); }, E1(6));
      us(PH(7, v7), CLO(0), CLO(1), U(a0, CLO(2)));
      us(QK0(0, a0), [&] { b0 = rk(1, 0); b1 = rk(1, 1); }, E1(7), U(a1, E1(8)));
      us(QK0(1, a1), E1(9), E1(10), U(a2, E1(11)));
      us(QK0(2, a2), [&] { o1 = aor3(s.pf[0][0], s.pf[0][1], s.pf[1][0]); o2 = aor3(s.pf[1][1], s.pf[2][0], s.pf[2][1]); }, CHI(0), U(a3, CHI(1)));
      us(QK0(3, a3), CHI(2), [&] { o1 = aor3(o1, s.pf[0][2], s.pf[0][3]); o2 = aor3(o2, s.pf[1][2], s.pf[1][3]); },
                     U(b0, [&] { o1 = aor3(o1, s.pf[2][2], s.pf[2][3]); s.orv = o1 | o2; }));
      if constexpr (!FIRST) check<NB, true>(s, c.rk, c.hs * 131072 + ((t - 1 + c.rot) & 15) * 8192);
      SB();
      us(QK1(0, b0), [&] { b2 = rk(1, 2); b3 = rk(1, 3); }, [&] { v0 = rv(0); }, U(b1, [&] { aaddu(ka[0], dk); aaddu(ka[1], dk); }));
      us(QK1(1, b1), [&] { v1 = rv(1); }, E0(0), U(b2, E0(1)));
      us(QK1(2, b2), [&] { v2 = rv(2); }, E0(2), U(b3, E0(3)));
      us(QK1(3, b3), [&] { v3 = rv(3); }, E0(4), E0(5));
      us(SUM, E0(6), E0(7), U(v0, E0(8)));
      us(PV(0, v0), [&] { v4 = rv(4); }, E0(9), U(v1, E0(10)));
      us(PV(1, v1), [&] { v5 = rv(5); }, E0(11), U(v2, E1(0)));
      us(PV(2, v2), [&] { v6 = rv(6); }, E1(1), U(v3, E1(2)));
      us(PV(3, v3), [&] { v7 = rv(7); }, E1(3), U(v4, E1(4)));
      us(PV(4, v4), E1(5), [&] { aaddu(ka[2], dk); aaddu(ka[3], dk); }, U(v5, [&] { aaddu(va, dv); }));
      us(PV(5, v5), N, [&] { use(v7); }, N);
    } else {
      us(PH(5, v5h), [&] { a0 = rk(0, 0); a1 = rk(0, 1); }, [&] { a2 = rk(0, 2); a3 = rk(0, 3); }, N);
      us(PH(6, v6), E1(4), [&] { kvh(0); }, N);
      us(PH(7, v7), CLO(0), U(a0, CLO(1)), N);
      us(QK0(0, a0), [&] { b0 = rk(1, 0); b1 = rk(1, 1); }, U(a1, E1(5)), N);
      us(QK0(1, a1), E1(6), U(a2, E1(7)), N);
      us(QK0(2, a2), [&] { o1 = aor3(s.pf[0][0], s.pf[0][1], s.pf[1][0]); kvh(1); }, U(a3, CHI(0)), N);
      us(QK0(3, a3), CHI(1), U(b0, [&] { o1 = aor3(o1, s.pf[1][1], s.pf[0][2]); o2 = aor3(s.pf[0][3], s.pf[1][2], s.pf[1][3]); s.orv = o1 | o2; }), N);
      if constexpr (!FIRST) check<NB, true>(s, c.rk, c.hs * 131072 + ((t - 1 + c.rot) & 15) * 8192);
      SB();
      us(QK1(0, b0), [&] { b2 = rk(1, 2); b3 = rk(1, 3); }, U(b1, [&] { v0 = rv(0); kvh(2); }), N);
      us(QK1(1, b1), [&] { v1 = rv(1); }, U(b2, [&] { aaddu(ka[0], dk); aaddu(ka[1], dk); kvh(3); }), N);
      us(QK1(2, b2), [&] { v2 = rv(2); }, U(b3, E0(0)), N);
      us(QK1(3, b3), [&] { v3 = rv(3); }, [&] { exp1<NB, 1>(s.S0); kvh(4); }, N);
      us(SUM, E0(2), U(v0, E0(3)), N);
      us(PV(0, v0), [&] { v4 = rv(4); }, U(v1, [&] { exp1<NB, 4>(s.S0); kvh(5); }), N);
      us(PV(1, v1), [&] { v5h = rv(5); }, U(v2, E0(5)), N);
      us(PV(2, v2), [&] { v6 = rv(6); aaddu(ka[2], dk); aaddu(ka[3], dk); }, U(v3, [&] { exp1<NB, 6>(s.S0); kvh(6); }), N);
      us(PV(3, v3), [&] { v7 = rv(7); exp1<NB, 7>(s.S0); }, U(v4, [&] { exp1<NB, 0>(s.S1); kvh(7); }), N);
      us(PV(4, v4), [&] { exp1<NB, 1>(s.S1); exp1<NB, 2>(s.S1); }, [&] { exp1<NB, 3>(s.S1); aaddu(va, dv); use(v7); }, N);
    }
  };
  STAMP(3);
  astep(std::true_type{}, 1, [&](int n) { if constexpr (NB == 2) { if (n < 4) st128(lds0 + c.j * 2048 + lane * 16 + (n >> 1) * KV_HS + (n & 1) * 1024, kvr[16 + n]); } });
#pragma unroll 1
  for (int t = 2; t < (NB == 3 ? 16 : 14); ++t) {
    unsigned lk = 0, lv = 0, sk = 0, sv = 0;
    if constexpr (NB == 2) { lk = kvl(t + 3, 0); lv = V_OFF + kvl(t + 2, 0); sk = t < 13 ? tso(t + 3) : OOB; sv = tso(t + 2); pins(lk); pins(lv); pins(sk); pins(sv); }
    astep(std::false_type{}, t, [&](int n) { if constexpr (NB == 2) { if (n & 1) asm volatile("s_waitcnt vmcnt(%0)" :: "i"(THR) : "memory"); else asm volatile("s_waitcnt vmcnt(%0)" :: "i"(THRB) : "memory"); if (n < 4) dma_kv(false, lk, sk, n); else dma_kv(true, lv, sv, n - 4); } });
  }
  rsrc_t rof, rwo, rslab; int pr, hg, rotb; gu64* sync; u16* ob;
  {
    int id = c.id; asm volatile("" : "+s"(id));
    const int br = (id >> 2) & 1, b = id >> 5;
    hg = id & 3; pr = (id >> 3) & 3; rotb = b & 3;
    rof = mkr(c.ws + (size_t)((br * 8 + b) * 4 + hg) * 327680, 327680);
    rwo = mkr((const char*)(c.wo + (size_t)hg * 1024 * 640 + pr * 160) - 4096, 1024 * 1280 - pr * 320 + 4096);
    rslab = mkr(c.ws + WS_SLAB + (size_t)((br * 8 + b) * 4 + pr) * (12 * QB), 12 * QB);
    sync = (gu64*)(c.ws + WS_SYNC);
    ob = c.out + (size_t)(b * 160 + (hg & 1) * 80) * 1280 + br * 640 + pr * 160 + (hg >> 1) * 80;
  }
  unsigned lz = 0;
  const int dks = c.j >> 1, dh = c.j & 1;
  auto stg = [](int u) { return (u >> 1) * 81920 + ((u ^ (u >> 1)) & 1) * 40960; };
  auto kpair = [&](int i) { return ((pr + (i >> 2)) & 3) * 4 + ((i + rotb) & 3); };
  auto wvo = [&](int n) { const int e = 64 * n + (int)lz, row = e / 20, ch = e - row * 20; return row * 1280 + ((ch ^ (((row >> 2) & 1) << 1)) * 16) + 4096; };
  auto wso = [&](int i) { return (kpair(i) * 64 + dks * 32 + dh * 16) * 1280; };
  auto wld = [&](int i, int n) { return lds0 + stg(i & 3) + dks * 20480 + 10240 + dh * 5120 + n * 1024; };
  u32x4 wr[15];
  if constexpr (NB == 2) {
    lz = laneid();
    unsigned vw[5];
#pragma unroll
    for (int n = 0; n < 5; ++n) vw[n] = wvo(n);
    auto wl = [&](int p) { asm volatile("s_waitcnt vmcnt(%0)" :: "i"(THR) : "memory"); if (p < 15) wr[p] = __builtin_amdgcn_raw_buffer_load_b128(rwo, vw[p % 5], wso(p / 5), WONT ? 2 : 0); };
    SB();
    astep(std::false_type{}, 14, [&](int n) { wl(n); });
    astep(std::false_type{}, 15, [&](int n) { wl(8 + n); });
  }
  STAMP(4);
  if constexpr (NB == 3) wait_vm_bar<63>(); else wait_vm_bar<15>();
  {
    bf16x8 vf[8];
#pragma unroll
    for (int db = 0; db < 4; ++db) vf[db] = rv(db);
    SB();
    if constexpr (NB == 2) us(PVH(5, v5h), N, N, N);
    us(PVH(6, v6), N, N, N);
    exps<NB, 2 * NB, 4 * NB>(s.S1);
    us(PVH(7, v7), CLO(0), CLO(1), CLO(2));
    CHI(0)(); CHI(1)(); CHI(2)();
    s.orv = 0;
#pragma unroll
    for (int nb = 0; nb < NB; ++nb) { s.orv = aor3(s.orv, s.pf[nb][0], s.pf[nb][1]); s.orv = aor3(s.orv, s.pf[nb][2], s.pf[nb][3]); }
    SB();
    check<NB, false>(s, c.rk, c.hs * 131072 + ((15 + c.rot) & 15) * 8192);
    SB();
    if constexpr (NB == 3) lz = laneid();
#pragma unroll
    for (int db = 4; db < 8; ++db) vf[db] = rv(db);
#pragma unroll
    for (int nb = 0; nb < NB; ++nb) s.l[nb] = mfma(s.ones, asb(s.pf[nb]), s.l[nb]);
#pragma unroll
    for (int db = 0; db < 8; ++db)
#pragma unroll
      for (int nb = 0; nb < NB; ++nb) {
        s.o[nb][db] = mfma(vf[db], asb(s.pf[nb]), s.o[nb][db]);
        if constexpr (NB == 2) { const int p = db * 2 + nb; if (p < 15) { SB(); st128(wld(p / 5, p % 5) + lz * 16, wr[p]); SB(); } }
      }
  }
  STAMP(5);
  {
    u32x4 ow[NB][4];
    const unsigned gb = lz * 16 + (pr * 8 + c.hs * 4) * 10240 + c.blk0 * 1024;
#pragma unroll
    for (int e = 0; e < 4; ++e)
#pragma unroll
      for (int nb = 0; nb < NB; ++nb) {
        const float inv = __builtin_amdgcn_rcpf(s.l[nb][0]);
        const f32x4 a0 = s.o[nb][2 * e] * inv, a1 = s.o[nb][2 * e + 1] * inv;
        ow[nb][e] = u32x4{cvtpk(a0[0], a0[1]), cvtpk(a0[2], a0[3]), cvtpk(a1[0], a1[1]), cvtpk(a1[2], a1[3])};
        __builtin_amdgcn_raw_buffer_store_b128(ow[nb][e], rof, gb + e * 10240 + nb * 1024, 0, 16);
      }
    asm volatile("s_waitcnt lgkmcnt(0)\n\ts_barrier" ::: "memory");
#pragma unroll
    for (int eh = 0; eh < 2; ++eh) {
      const unsigned lb = lds0 + stg((c.hs * 2 + eh - rotb) & 3) + c.blk0 * 1024 + lz * 16;
#pragma unroll
      for (int el = 0; el < 2; ++el)
#pragma unroll
        for (int nb = 0; nb < NB; ++nb) st128(lb + el * 20480 + nb * 1024, ow[nb][eh * 2 + el]);
    }
  }
  STAMP(6);
  {
    constexpr int NM = NB, M0 = NM == 3 ? 0 : 3;
    const int q = c.j, qm = q & 1, qn = q >> 1, zg = lz >> 4;
    unsigned vo3[5];
#pragma unroll
    for (int n = 0; n < 5; ++n) vo3[n] = NB == 3 ? wvo(n) - (n & 3) * 1024 : lz * 16 + (n >> 2) * 4096;
    const unsigned lb3 = lds0 + dks * 20480 + dh * 5120 + (NB == 3 ? 10240 : 0);
    auto so3 = [&](int i) { unsigned v = i >= 16 ? OOB : NB == 3 ? wso(i) : (kpair(i) * 2 + dks) * 10240 + dh * 5120; pins(v); return v; };
    auto dmal = [&](unsigned so, int u, int n) {
      constexpr int POL = NB == 3 ? (WONT ? 1 : 0) : 2;
      const rsrc_t r = NB == 3 ? rwo : rof;
      const unsigned vo = vo3[NB == 3 ? n : (n >> 2) * 4];
      if (THR3 < 63) asm volatile("s_waitcnt vmcnt(%0)" :: "i"(THR3) : "memory");
      if (n == 0) { setm0(lb3 + stg(u)); dmao<POL, 0>(vo, r, so); }
      else if (n == 1) dmao2<POL, 1024>(vo, r, so);
      else if (n == 2) dmao2<POL, 2048>(vo, r, so);
      else if (n == 3) dmao2<POL, 3072>(vo, r, so);
      else { setm0(lb3 + stg(u) + 4096); dmao<POL, 0>(vo, r, so); }
    };
    if constexpr (NB == 3) {
      const unsigned so = so3(3);
#pragma unroll
      for (int n = 0; n < 5; ++n) dmal(so, 3, n);
    }
    unsigned oa[2], wa[2][2];
    {
      oa[0] = lds0 + (qm * 5 + M0) * 1024 + lz * 16; oa[1] = oa[0] + 81920;
      const unsigned wb = lds0 + 10240 + (4 * zg + ((lz & 15) >> 2)) * 320 + (lz & 3) * 8 + qn * 160;
      const int sg = (qn ? -32 : 32) * (zg & 1);
      wa[0][0] = wb + sg; wa[0][1] = wb - sg; wa[1][0] = wa[0][0] + 81920; wa[1][1] = wa[0][1] + 81920;
      pin(oa[1]); pin(wa[1][0]); pin(wa[1][1]);
    }
    f32x4 acc[NM][5];
    bf16x8 h2, h3, h4, op[NM];
    auto interval = [&](auto UU, auto D, auto tail) {
      constexpr bool FIRST = decltype(UU)::value < 0;
      constexpr int u = FIRST ? 0 : decltype(UU)::value, so = ((u ^ (u >> 1)) & 1) * 40960;
      auto ro = [&](int ks, int m) { return ld128(oa[u >> 1] + so + ks * 20480 + m * 1024); };
      auto rw = [&](int ks, int n) { const unsigned a = wa[u >> 1][n & 1] + so + ks * 20480 + n * 32; return cat(ldtr(a), ldtr(a + 5120)); };
      auto mm = [&](auto Z, int n, bf16x8& wf, bf16x8 (&of)[NM], auto g0, auto g1, auto g2) {
        constexpr int z = decltype(Z)::value;
        if constexpr (z == 2) { g0(); g1(); g2(); SB(); }
        else {
          amf<z == 1>(acc[0][n], wf, of[0]); SB(); g0(); SB(); amf<z == 1>(acc[1][n], wf, of[1]); SB(); g1(); SB();
          if constexpr (NM == 3) { amf<z == 1>(acc[2][n], wf, of[2]); SB(); }
          g2(); SB();
        }
      };
      constexpr IC<FIRST ? 2 : 0> H{};
      constexpr IC<FIRST ? 1 : 0> C{};
      constexpr IC<0> P{};
      bf16x8 o0[NM], o1[NM], a0, a1, a2, a3, a4, b0, b1;
      mm(H, 2, h2, op, [&] { a0 = rw(0, 0); }, [&] { o0[0] = ro(0, 0); o0[1] = ro(0, 1); }, [&] { if constexpr (NM == 3) o0[2] = ro(0, 2); });
      mm(H, 3, h3, op, [&] { a1 = rw(0, 1); }, [&] { a2 = rw(0, 2); }, [&] { D(0); });
      mm(H, 4, h4, op, [&] { a3 = rw(0, 3); }, [&] { a4 = rw(0, 4); }, [&] { use(o0[NM - 1]); });
      mm(C, 0, a0, o0, [&] { o1[0] = ro(1, 0); o1[1] = ro(1, 1); }, [&] { use(a1); if constexpr (NM == 3) o1[2] = ro(1, 2); }, [&] { D(1); });
      mm(C, 1, a1, o0, [&] { b0 = rw(1, 0); }, [&] { use(a2); b1 = rw(1, 1); }, N0);
      mm(C, 2, a2, o0, [&] { h2 = rw(1, 2); }, [&] { use(a3); }, [&] { D(2); });
      mm(C, 3, a3, o0, [&] { h3 = rw(1, 3); }, [&] { use(a4); }, N0);
      mm(C, 4, a4, o0, [&] { h4 = rw(1, 4); }, [&] { use(b0); }, [&] { D(3); });
      mm(P, 0, b0, o1, N0, [&] { use(b1); }, [&] { D(4); });
      mm(P, 1, b1, o1, [&] { use(h4); }, N0, N0);
      tail();
#pragma unroll
      for (int m = 0; m < NM; ++m) op[m] = o1[m];
    };
    auto ND = [](int) {};
    SB();
    wait_vm_bar<63>();
    interval(IC<-1>{}, ND, N0);
    wait_vm_bar<63>();
    STAMP(7);
    { const unsigned so = so3(4); interval(IC<1>{}, [&](int n) { if constexpr (NB == 3) dmal(so, 0, n); }, N0); }
    if constexpr (NB == 3) wait_vm_bar<10>(); else wait_vm_bar<0>();
    if (c.w == 3 && lz == 0) __hip_atomic_store(sync + (1 + c.id) * FS, tag, RLX_AGENT);
    const unsigned so5 = so3(5);
    interval(IC<2>{}, [&](int n) { if constexpr (NB == 3) dmal(so5, 1, n); }, [&] {
      if (c.w == 2) {
        const int pid = c.id ^ (((lz & 3) ? (lz & 3) : 1) << 3);
        for (unsigned spins = 0; spins < (1u << 14); ++spins) {
          if (__all(__hip_atomic_load(sync + (1 + pid) * FS, RLX_AGENT) == tag)) break;
          __builtin_amdgcn_s_sleep(1);
        }
      }
    });
    if constexpr (NB == 3) wait_vm_bar<10>(); else wait_vm_bar<63>();
    STAMP(8);
    { const unsigned sa = so3(NB == 3 ? 6 : 4);
      interval(IC<3>{}, [&](int n) { if constexpr (NB == 3) dmal(sa, 2, n);
        else { if (n == 0) { dmal(sa, 0, 0); dmal(sa, 0, 1); } else if (n == 1) { dmal(sa, 0, 2); dmal(sa, 0, 3); } else if (n == 2) { dmal(sa, 0, 4); dmal(so5, 1, 0); }
               else if (n == 3) { dmal(so5, 1, 1); dmal(so5, 1, 2); } else { dmal(so5, 1, 3); dmal(so5, 1, 4); } } }, N0); }
    STAMP(9);
#pragma unroll 1
    for (int i4 = 4; i4 < 16; i4 += 4)
      static_for(std::make_integer_sequence<int, 4>{}, [&](auto UU) {
        constexpr int u = decltype(UU)::value;
        const unsigned so = so3(i4 + u + (NB == 3 ? 3 : 2));
        if constexpr (NB == 3) wait_vm_bar<10>(); else wait_vm_bar<5>();
        interval(UU, [&](int n) { dmal(so, (u + (NB == 3 ? 3 : 2)) & 3, n); }, N0);
      });
#pragma unroll
    for (int m = 0; m < NM; ++m) { amfma(acc[m][2], h2, op[m]); amfma(acc[m][3], h3, op[m]); amfma(acc[m][4], h4, op[m]); }
    asm volatile("s_nop 7");
    SB();
    STAMP(10);
    if (q != hg) {
      const int so = (q * 3 + ((hg - q - 1) & 3)) * QB;
#pragma unroll
      for (int e = 0; e < NM * 5; e += 2) {
        const f32x4 a = acc[e / 5][e % 5], b = e + 1 < NM * 5 ? acc[(e + 1) / 5][(e + 1) % 5] : z4;
        __builtin_amdgcn_raw_buffer_store_b128(u32x4{cvtpk(a[0], a[1]), cvtpk(a[2], a[3]), cvtpk(b[0], b[1]), cvtpk(b[2], b[3])}, rslab, lz * 16 + ((M0 ? 8 : 0) + e / 2) * 1024 + so, 0, 16);
      }
    }
    asm volatile("s_waitcnt vmcnt(0) lgkmcnt(0)\n\ts_barrier" ::: "memory");
    if (c.w == 0 && lz == 0) __hip_atomic_store(sync + (257 + c.id) * FS, tag, RLX_AGENT);
    STAMP(11);
    if (q == hg) {
#pragma unroll
      for (int e = 0; e < NM * 5; ++e) st128(lds0 + lz * 16 + (M0 * 5 + e) * 1024, __builtin_bit_cast(u32x4, acc[e / 5][e % 5]));
    }
    if (c.w == ((hg + 1) & 3)) {
      const int pid = c.id ^ ((lz & 3) ? (lz & 3) : 1);
      for (unsigned spins = 0; spins < (1u << 14); ++spins) {
        if (__all(__hip_atomic_load(sync + (257 + pid) * FS, RLX_AGENT) == tag)) break;
        __builtin_amdgcn_s_sleep(1);
      }
    }
    if (c.id == 0 && c.w == 7) {
      for (unsigned spins = 0; spins < (1u << 14); ++spins) {
        bool ok = true;
#pragma unroll
        for (int k = 0; k < 4; ++k) ok &= __hip_atomic_load(sync + (1 + lz + 64 * k) * FS, RLX_AGENT) == tag;
        if (__all(ok)) break;
        __builtin_amdgcn_s_sleep(1);
      }
      if (lz == 0) __hip_atomic_store(sync, tag, RLX_AGENT);
    }
    asm volatile("s_waitcnt vmcnt(0) lgkmcnt(0)\n\ts_barrier" ::: "memory");
    STAMP(12);
    u16* op16 = ob + (size_t)(lz & 15) * 1280 + 4 * zg;
    u32x4 r[2][3];
#pragma unroll
    for (int k = 0; k < 2; ++k)
#pragma unroll
      for (int sl = 0; sl < 3; ++sl) r[k][sl] = __builtin_amdgcn_raw_buffer_load_b128(rslab, c.w + 8 * k < 13 ? lz * 16 + (c.w + 8 * k) * 1024 : OOB, (hg * 3 + sl) * QB, 16);
#pragma unroll
    for (int k = 0; k < 2; ++k) {
      const int un = c.w + 8 * k;
#pragma unroll
      for (int hh = 0; hh < 2; ++hh) {
        const int e = un < 8 ? 2 * un + hh : 15 + 2 * (un - 8) + hh;
        if (un < 13 && !(un == 7 && hh == 1)) {
          f32x4 v = *(const LDS3 f32x4*)(size_t)(lds0 + lz * 16 + e * 1024);
#pragma unroll
          for (int sl = 0; sl < 3; ++sl) { const unsigned lo = r[k][sl][2 * hh], hi = r[k][sl][2 * hh + 1];
            v += f32x4{__uint_as_float(lo << 16), __uint_as_float(lo & 0xffff0000u), __uint_as_float(hi << 16), __uint_as_float(hi & 0xffff0000u)}; }
          const int m = e / 5, n = e - m * 5;
          *(u32x2*)(op16 + m * (16 * 1280) + n * 16) = u32x2{cvtpk(v[0], v[1]), cvtpk(v[2], v[3])};
        }
      }
    }
  }
}

__global__ __launch_bounds__(512, 2) void fused(const u16* xA, const u16* kA, const u16* vA, const u16* wqA, const u16* woA,
                                                 const u16* xB, const u16* kB, const u16* vB, const u16* wqB, const u16* woB, u16* out, char* ws) {
  extern __shared__ __attribute__((aligned(16))) char smem[];
  const int id = blockIdx.x, xg = id & 7, cc = id >> 3, br = xg >> 2, hg = xg & 3, b = cc >> 2, pr = cc & 3, h0 = hg * 8 + pr * 2;
  const int w = __builtin_amdgcn_readfirstlane(threadIdx.x >> 6);
  asm volatile("s_nop 4");
  Ctx c;
  const u16* x = br ? xB : xA; const u16* k = br ? kB : kA; const u16* v = br ? vB : vA; const u16* wq = br ? wqB : wqA;
  c.rx = mkr(x + (size_t)b * SQ * DM, SQ * DM * 2);
  c.rw = mkr(wq + h0 * DH, DM * NH * DH * 2 - h0 * DH * 2);
  c.rk = mkr(k + (size_t)(b * NH + h0) * SK * DH, 2 * SK * DH * 2);
  c.rv = mkr((const char*)(v + (size_t)(b * NH + h0) * SK * DH) - 1024, 2 * SK * DH * 2 + 1024);
  c.lds0 = (unsigned)(size_t)(LDS3 char*)smem;
  c.hs = w >> 2; c.j = w & 3; c.rot = (id * 5) & 15; c.krot = ((10 * pr + 5 * b) >> 2) % 10;
  c.w = w; c.id = id; c.wo = br ? woB : woA; c.out = out; c.ws = ws;
  const bool big = (c.j < 2) == (c.hs == 0);
  c.blk0 = c.hs == 0 ? (c.j < 2 ? 3 * c.j : 2 + 2 * c.j) : (c.j < 2 ? 2 * c.j : 3 * c.j - 2);
  if (big) body<3>(c); else body<2>(c);
}

extern "C" void kernel_launch(void* const* d_in, const int* in_sizes, int n_in, void* d_out, int out_size, void* d_ws, size_t ws_size, hipStream_t stream) {
  (void)hipFuncSetAttribute((const void*)fused, hipFuncAttributeMaxDynamicSharedMemorySize, LDS_BYTES);
  fused<<<256, 512, LDS_BYTES, stream>>>((const u16*)d_in[0], (const u16*)d_in[1], (const u16*)d_in[2], (const u16*)d_in[3], (const u16*)d_in[4],
                                         (const u16*)d_in[5], (const u16*)d_in[6], (const u16*)d_in[7], (const u16*)d_in[8], (const u16*)d_in[9], (u16*)d_out, (char*)d_ws);
}
```

```cpp
#include <hip/hip_runtime.h>
#include <hip/hip_bf16.h>
#include <type_traits>

typedef __bf16 bf16x8 __attribute__((ext_vector_type(8)));
typedef __bf16 bf16x4 __attribute__((ext_vector_type(4)));
typedef __bf16 bf16x2 __attribute__((ext_vector_type(2)));
typedef float f32x4 __attribute__((ext_vector_type(4)));
typedef float f32x2 __attribute__((ext_vector_type(2)));
typedef unsigned u32x4 __attribute__((ext_vector_type(4)));
typedef unsigned u32x2 __attribute__((ext_vector_type(2)));
typedef unsigned short u16;
typedef __attribute__((address_space(1))) unsigned long long gu64;
typedef __amdgpu_buffer_rsrc_t rsrc_t;
#define LDS3 __attribute__((address_space(3)))
#define DI __device__ __forceinline__
#define SB() __builtin_amdgcn_sched_barrier(0)
#define RLX_AGENT __ATOMIC_RELAXED, __HIP_MEMORY_SCOPE_AGENT
template <int N> using IC = std::integral_constant<int, N>;

constexpr int SQ = 160, DM = 640, NH = 32, SK = 512, DH = 128;
constexpr int LDS_BYTES = 163840;
constexpr int KV_HS = 32768, V_OFF = 65536;
constexpr int K4_OFF = 131072, V4_OFF = 147456;
__host__ __device__ constexpr int kslot(int sl, int hs) { return sl < 4 ? hs * KV_HS + sl * 8192 : K4_OFF + hs * 8192; }
__host__ __device__ constexpr int vslot(int sl, int hs) { return sl < 4 ? V_OFF + hs * KV_HS + sl * 8192 : V4_OFF + hs * 8192; }
__host__ __device__ constexpr int xoff(int b) { return b * 65536 + 73728; }
__host__ __device__ constexpr int woff(int b) { return b == 0 ? 16384 : b == 1 ? 49152 : b == 2 ? 106496 : 122880; }
constexpr float QSCALE = 0.08838834764831845f * 1.4426950408889634f;
constexpr float BIAS = 10.f;
constexpr unsigned OOB = 0x7ff00000u;
constexpr size_t WS_SLAB = 32u << 20, WS_SYNC = 64u << 20;
constexpr int FS = 32;
constexpr int QB = 13 * 1024;
#define KVN0 1
#define THR 4
#define EPRE 2
#define XNT false
#define WNT true
#define WONT false

DI unsigned cvtpk(float lo, float hi) { f32x2 v = {lo, hi}; bf16x2 b = __builtin_convertvector(v, bf16x2); return __builtin_bit_cast(unsigned, b); }
DI f32x4 mfma(bf16x8 a, bf16x8 b, f32x4 c) { return __builtin_amdgcn_mfma_f32_16x16x32_bf16(a, b, c, 0, 0, 0); }
DI bf16x8 ld128(unsigned addr) { return *(const LDS3 bf16x8*)(size_t)addr; }
DI void st128(unsigned addr, u32x4 v) { *(LDS3 u32x4*)(size_t)addr = v; }
DI bf16x4 ldtr(unsigned addr) { return __builtin_amdgcn_ds_read_tr16_b64_v4bf16((LDS3 bf16x4*)(size_t)addr); }
DI bf16x8 cat(bf16x4 a, bf16x4 b) { return __builtin_shufflevector(a, b, 0, 1, 2, 3, 4, 5, 6, 7); }
DI bf16x8 asb(u32x4 w) { return __builtin_bit_cast(bf16x8, w); }
DI rsrc_t mkr(const void* p, int bytes) { return __builtin_amdgcn_make_buffer_rsrc((void*)p, (short)0, bytes, 0x00020000); }
template <bool NT> DI void dma16(unsigned ldsaddr, unsigned voff, rsrc_t r, unsigned soff) {
  if constexpr (NT) asm volatile("s_mov_b32 m0, %0\n\ts_nop 0\n\tbuffer_load_dwordx4 %1, %2, %3 offen nt lds" :: "s"(ldsaddr), "v"(voff), "s"(r), "s"(soff) : "memory");
  else              asm volatile("s_mov_b32 m0, %0\n\ts_nop 0\n\tbuffer_load_dwordx4 %1, %2, %3 offen lds" :: "s"(ldsaddr), "v"(voff), "s"(r), "s"(soff) : "memory");
}
DI void setm0(unsigned v) { asm volatile("s_mov_b32 m0, %0" :: "s"(v) : "memory"); }
template <int POL, int OFF> DI void dmao(unsigned voff, rsrc_t r, unsigned soff) {
  if constexpr (POL == 1)      asm volatile("s_nop 0\n\tbuffer_load_dwordx4 %0, %1, %2 offen offset:%3 nt lds" :: "v"(voff), "s"(r), "s"(soff), "i"(OFF) : "memory");
  else if constexpr (POL == 2) asm volatile("s_nop 0\n\tbuffer_load_dwordx4 %0, %1, %2 offen offset:%3 sc1 lds" :: "v"(voff), "s"(r), "s"(soff), "i"(OFF) : "memory");
  else                         asm volatile("s_nop 0\n\tbuffer_load_dwordx4 %0, %1, %2 offen offset:%3 lds" :: "v"(voff), "s"(r), "s"(soff), "i"(OFF) : "memory");
}
template <int POL, int OFF> DI void dmao2(unsigned voff, rsrc_t r, unsigned soff) {
  if constexpr (POL == 1)      asm volatile("buffer_load_dwordx4 %0, %1, %2 offen offset:%3 nt lds" :: "v"(voff), "s"(r), "s"(soff), "i"(OFF) : "memory");
  else if constexpr (POL == 2) asm volatile("buffer_load_dwordx4 %0, %1, %2 offen offset:%3 sc1 lds" :: "v"(voff), "s"(r), "s"(soff), "i"(OFF) : "memory");
  else                         asm volatile("buffer_load_dwordx4 %0, %1, %2 offen offset:%3 lds" :: "v"(voff), "s"(r), "s"(soff), "i"(OFF) : "memory");
}
DI void pins(unsigned& v) { v = __builtin_amdgcn_readfirstlane(v); asm volatile("" : "+s"(v)); }
template <int N> DI void wait_vm() { asm volatile("s_waitcnt vmcnt(%0)" :: "i"(N) : "memory"); }
template <int N> DI void wait_vm_bar() { asm volatile("s_waitcnt vmcnt(%0) lgkmcnt(0)\n\ts_barrier" :: "i"(N) : "memory"); }
DI float xmax16(float v) { auto r = __builtin_amdgcn_permlane16_swap(__float_as_uint(v), __float_as_uint(v), false, false); return fmaxf(__uint_as_float(r[0]), __uint_as_float(r[1])); }
DI float xmax32(float v) { auto r = __builtin_amdgcn_permlane32_swap(__float_as_uint(v), __float_as_uint(v), false, false); return fmaxf(__uint_as_float(r[0]), __uint_as_float(r[1])); }
DI void amfma(f32x4& acc, bf16x8 a, bf16x8 b) { asm volatile("v_mfma_f32_16x16x32_bf16 %0, %1, %2, %0" : "+v"(acc) : "v"(a), "v"(b)); }
DI void amfma_c(f32x4& d, bf16x8 a, bf16x8 b, f32x4 cc) { asm volatile("v_mfma_f32_16x16x32_bf16 %0, %1, %2, %3" : "=&v"(d) : "v"(a), "v"(b), "v"(cc)); }
DI void amfma_z(f32x4& d, bf16x8 a, bf16x8 b) { asm volatile("v_mfma_f32_16x16x32_bf16 %0, %1, %2, 0" : "=&v"(d) : "v"(a), "v"(b)); }
template <bool Z> DI void amf(f32x4& d, bf16x8 a, bf16x8 b) { if constexpr (Z) amfma_z(d, a, b); else amfma(d, a, b); }
DI float aexp(float x) { asm volatile("v_exp_f32 %0, %0" : "+v"(x)); return x; }
DI float amul(float a, float b) { float r; asm("v_mul_f32 %0, %1, %2" : "=v"(r) : "s"(b), "v"(a)); return r; }
DI unsigned aor3(unsigned a, unsigned b, unsigned c) { unsigned r; asm volatile("v_or3_b32 %0, %1, %2, %3" : "=v"(r) : "v"(a), "v"(b), "v"(c)); return r; }
DI unsigned acvt(float lo, float hi) { unsigned r; asm volatile("v_cvt_pk_bf16_f32 %0, %1, %2" : "=v"(r) : "v"(lo), "v"(hi)); return r; }
DI void use(bf16x8 f) { asm volatile("" :: "v"(f)); }
DI void aaddu(unsigned& a, int d) { asm volatile("v_add_u32 %0, %1, %0" : "+v"(a) : "s"(d)); }
DI unsigned laneid() { unsigned l; asm volatile("v_mbcnt_lo_u32_b32 %0, -1, 0\n\tv_mbcnt_hi_u32_b32 %0, -1, %0" : "=v"(l)); return l; }
template <class T> DI void pin(T& v) { asm volatile("" : "+v"(v)); }

#define STAMP(i)

struct Ctx {
  rsrc_t rx, rw, rk, rv;
  unsigned lds0;
  int j, hs, blk0, rot, krot, w, id;
  const u16* wo; u16* out; char* ws; unsigned long long code;
};
template <int NB> struct St {
  f32x4 o[NB][8], negm[NB], S0[NB], S1[NB], l[NB];
  bf16x8 qf[NB][4], ones;
  u32x4 pf[NB];
  unsigned orv;
};
template <int NB, int E> DI void exp1(f32x4 (&S)[NB]) { if constexpr (E >= 0 && E < 4 * NB) S[E % NB][E / NB] = aexp(S[E % NB][E / NB]); }
template <int NB, int LO, int HI> DI void exps(f32x4 (&S)[NB]) { if constexpr (LO < HI) { exp1<NB, LO>(S); exps<NB, LO + 1, HI>(S); } }
DI float max8(f32x4 a, f32x4 b) { return fmaxf(fmaxf(fmaxf(a[0], a[1]), fmaxf(a[2], a[3])), fmaxf(fmaxf(b[0], b[1]), fmaxf(b[2], b[3]))); }

template <int NB, bool HAS_NEXT> DI void refix(St<NB>& s, rsrc_t rk, int ksoff) {
  const unsigned ln = laneid();
  const int kvo = (ln & 15) * 256 + ((((ln >> 4) & 1) << 1) | (ln >> 5)) * 16;
  asm volatile("s_nop 7");
#pragma unroll
  for (int nb = 0; nb < NB; ++nb) {
    f32x4 a = s.negm[nb], b = s.negm[nb];
#pragma unroll
    for (int st = 0; st < 4; ++st) {
      a = mfma(asb(__builtin_amdgcn_raw_buffer_load_b128(rk, kvo + st * 64, ksoff, 0)), s.qf[nb][st], a);
      SB();
      b = mfma(asb(__builtin_amdgcn_raw_buffer_load_b128(rk, kvo + st * 64, ksoff + 4096, 0)), s.qf[nb][st], b);
      SB();
    }
    const float d = fmaxf(xmax32(xmax16(max8(a, b))) + BIAS, 0.f);
    const float f = __builtin_amdgcn_exp2f(-d);
    s.negm[nb] -= d; if (HAS_NEXT) s.S0[nb] -= d;
    s.l[nb] *= f;
#pragma unroll
    for (int db = 0; db < 8; ++db) s.o[nb][db] *= f;
#pragma unroll
    for (int r = 0; r < 4; ++r) { a[r] = __builtin_amdgcn_exp2f(a[r] - d); b[r] = __builtin_amdgcn_exp2f(b[r] - d); }
    u32x4 w = {cvtpk(a[0], a[1]), cvtpk(a[2], a[3]), cvtpk(b[0], b[1]), cvtpk(b[2], b[3])};
    s.pf[nb] = w;
    SB();
  }
  asm volatile("s_nop 1");
}
template <int NB, bool HAS_NEXT> DI void check(St<NB>& s, rsrc_t rk, int ksoff) {
  if (__builtin_expect(__any((s.orv & 0x40004000u) != 0u), 0)) refix<NB, HAS_NEXT>(s, rk, ksoff);
}
template <int... I, class F> DI void static_for(std::integer_sequence<int, I...>, F f) { (f(IC<I>{}), ...); }

template <int NB> DI void body(const Ctx& c) {
  const int lane = threadIdx.x & 63, l15 = lane & 15, g = lane >> 4;
  const unsigned lds0 = c.lds0;
  const f32x4 z4 = {0.f, 0.f, 0.f, 0.f};
  auto N0 = [] {};
  unsigned voffK[2], voffV[2];
#pragma unroll
  for (int h = 0; h < 2; ++h) {
    voffK[h] = (lane >> 4) * 256 + (((lane & 15) ^ (8 * (c.j & 1) + 4 * h + (lane >> 4))) * 16);
    voffV[h] = ((lane >> 1) & 7) * 256 + (lane >> 4) * 32 + (lane & 1) * 16 + (h ? 128 : 1024);
  }
  auto tso = [&](int t) { return (unsigned)(((t + c.rot) & 15) * 8192 + c.j * 2048); };
  auto dma_kv = [&](bool isv, unsigned lb0, unsigned lb1, unsigned so, int q) {
    if (q & 1) { if (isv) dmao2<1, 1024>(voffV[1], c.rv, so + (q >> 1) * 131072); else dmao2<1, 1024>(voffK[1], c.rk, so + (q >> 1) * 131072); }
    else { setm0(q ? lb1 : lb0); if (isv) dmao<1, 0>(voffV[0], c.rv, so + (q >> 1) * 131072); else dmao<1, 0>(voffK[0], c.rk, so + (q >> 1) * 131072); }
  };
  const unsigned jb = lds0 + c.j * 2048;
  auto ld_k = [&](int t, int q) { return __builtin_amdgcn_raw_buffer_load_b128(c.rk, voffK[q & 1] + (q & 1) * 1024, tso(t) + (q >> 1) * 131072, 2); };
  auto ld_v = [&](int t, int q) { return __builtin_amdgcn_raw_buffer_load_b128(c.rv, voffV[q & 1] + (q & 1) * 1024, tso(t) + (q >> 1) * 131072, 2); };
  STAMP(0);
  St<NB> s;
  const unsigned long long epoch = __hip_atomic_load((gu64*)(c.ws + WS_SYNC), RLX_AGENT);
  u32x4 kvr[NB == 2 ? 20 : 8];
  {
    unsigned voffS;
    if constexpr (NB == 3) { const int f = (2 * (c.j & 1) + (lane >> 5)) | ((c.j >> 1) << 2); voffS = (lane >> 5) * 8192 + (((lane & 31) ^ (f << 1)) * 16); }
    else voffS = (lane >> 3) * 1280 + (((lane & 7) ^ (4 * (c.j & 1) + g)) * 16);
    auto dma_w = [&](int st, int buf, int n) {
      int ks = st + 2 * c.krot; ks = ks >= 20 ? ks - 20 : ks;
      const int i = (c.j & 1) + 4 * (c.j >> 1) + 2 * (n & 1) + 8 * (n >> 1);
      dma16<WNT>(lds0 + woff(buf) + i * 1024, voffS, c.rw, (ks * 32 + 2 * i) * 8192);
    };
    auto dma_x = [&](int ch, int n) {
      int kc = ch + c.krot; kc = kc >= 10 ? kc - 10 : kc;
      const int i = c.j + 4 * n;
      dma16<XNT>(lds0 + xoff(ch & 1) + i * 1024, voffS, c.rx, i * (8 * 1280) + kc * 128);
    };
    auto extra = [&](int m, int q) {
      if (m == 0 || m == 1) dma_kv(false, jb + kslot(m, 0), jb + kslot(m, 1), tso(m), q);
      else if (m == 2) dma_kv(true, jb + vslot(0, 0), jb + vslot(0, 1), tso(0), q);
      else if (m == 3 || m == 5 || m == 7) kvr[(m - 3) * 4 + q] = ld_k((m + 1) / 2, q);
      else if (m == 4 || m == 6) kvr[(m - 3) * 4 + q] = ld_v((m - 2) / 2, q);
    };
    if constexpr (NB == 3) {
#pragma unroll
      for (int st = 0; st < 3; ++st)
#pragma unroll
        for (int n = 0; n < 4; ++n) dma_w(st, st, n);
    } else {
#pragma unroll
      for (int n = 0; n < 5; ++n) dma_x(0, n);
      {
        const rsrc_t rc = mkr((const void*)c.code, 72 * 1024);
        asm volatile("s_mov_b32 m0, %0\n\ts_nop 0\n\tbuffer_load_dword %1, %2, %3 offen lds" :: "s"(lds0 + 159744), "v"(lane < 5 ? (unsigned)lane * 128u : OOB), "s"(rc), "s"(((c.id >> 3) * 4 + c.j) * 640) : "memory");
      }
      __builtin_amdgcn_s_setprio(1);
    }
    unsigned wa[2][8], xa[4];
    const int qq = l15 >> 2, p = lane & 3, f = qq | ((g & 1) << 2);
#pragma unroll
    for (int db = 0; db < 8; ++db) { wa[0][db] = lds0 + woff(0) + (8 * g + qq) * 512 + c.hs * 256 + ((db ^ f) * 32) + p * 8; wa[1][db] = wa[0][db] + (woff(2) - woff(0)); pin(wa[1][db]); }
#pragma unroll
    for (int u = 0; u < 4; ++u) { xa[u] = lds0 + xoff(u >> 1) + (c.blk0 * 16 + l15) * 128 + (((4 * (u & 1) + g) ^ ((l15 >> 1) & 7)) * 16); pin(xa[u]); }
    bf16x8 w4, w5, w6, w7, xp[NB];
    auto qstep = [&](auto U, auto PH, auto D) {
      constexpr int u = decltype(U)::value, ph = decltype(PH)::value;
      auto rw = [&](int db) { const unsigned a = wa[u >> 1][db] + (woff(u) - woff(u & 2)); return cat(ldtr(a), ldtr(a + 2048)); };
      auto rx = [&](int nb) { return ld128(xa[u] + nb * 2048); };
      auto mm = [&](auto Z, int db, bf16x8& wf, bf16x8 (&xf)[NB], auto g0, auto g1, auto g2) {
        constexpr int z = decltype(Z)::value;
        if constexpr (z == 2) { g0(); g1(); g2(); SB(); }
        else {
          amf<z == 1>(s.o[0][db], wf, xf[0]); SB(); g0(); SB();
          amf<z == 1>(s.o[1][db], wf, xf[1]); SB(); g1(); SB();
          if constexpr (NB == 3) { amf<z == 1>(s.o[2][db], wf, xf[2]); SB(); }
          g2(); SB();
        }
      };
      constexpr IC<ph == 0 ? 2 : ph == 1 ? 1 : 0> H{};
      constexpr IC<ph == 0 ? 1 : 0> C{};
      bf16x8 xf[NB], w0, w1, w2, w3;
      if constexpr (NB == 3) {
        mm(H, 4, w4, xp, [&] { w0 = rw(0); }, [&] { xf[0] = rx(0); xf[1] = rx(1); }, [&] { xf[2] = rx(2); });
        mm(H, 5, w5, xp, [&] { w1 = rw(1); }, [&] { D(0); }, N0);
        mm(H, 6, w6, xp, [&] { w2 = rw(2); }, [&] { w3 = rw(3); }, N0);
        mm(H, 7, w7, xp, [&] { w4 = rw(4); }, [&] { D(1); }, [&] { use(xf[NB - 1]); });
        mm(C, 0, w0, xf, [&] { w5 = rw(5); }, [&] { use(w1); w6 = rw(6); }, N0);
        mm(C, 1, w1, xf, [&] { w7 = rw(7); }, [&] { use(w2); D(2); }, N0);
        mm(C, 2, w2, xf, [&] { use(w3); }, [&] { D(3); }, N0);
        mm(C, 3, w3, xf, [&] { use(w7); }, N0, N0);
      } else {
        mm(H, 4, w4, xp, [&] { w0 = rw(0); }, [&] { xf[0] = rx(0); xf[1] = rx(1); }, N0);
        mm(H, 5, w5, xp, [&] { w1 = rw(1); }, [&] { D(0); }, N0);
        mm(H, 6, w6, xp, [&] { w2 = rw(2); }, [&] { w3 = rw(3); D(1); }, N0);
        mm(H, 7, w7, xp, [&] { w4 = rw(4); }, [&] { D(2); use(xf[NB - 1]); }, N0);
        mm(C, 0, w0, xf, [&] { w5 = rw(5); }, [&] { use(w1); w6 = rw(6); D(3); }, N0);
        mm(C, 1, w1, xf, [&] { w7 = rw(7); }, [&] { use(w2); D(4); }, N0);
        mm(C, 2, w2, xf, [&] { use(w3); }, N0, N0);
        mm(C, 3, w3, xf, [&] { use(w7); }, N0, N0);
      }
#pragma unroll
      for (int nb = 0; nb < NB; ++nb) xp[nb] = xf[nb];
    };
    STAMP(1);
    SB();
    if constexpr (NB == 3) {
      static_for(std::make_integer_sequence<int, 4>{}, [&](auto U) {
        constexpr int u = decltype(U)::value;
        wait_vm_bar<8>(); qstep(U, IC<(u < 2 ? u : 2)>{}, [&](int n) { dma_w(u + 3, (u + 3) & 3, n); });
      });
#pragma unroll 1
      for (int s4 = 4; s4 < 16; s4 += 4)
        static_for(std::make_integer_sequence<int, 4>{}, [&](auto U) {
          constexpr int u = decltype(U)::value;
          wait_vm_bar<8>(); qstep(U, IC<2>{}, [&](int n) { dma_w(s4 + u + 3, (u + 3) & 3, n); });
        });
      wait_vm_bar<8>(); qstep(IC<0>{}, IC<2>{}, [&](int n) { dma_w(19, 3, n); });
      wait_vm_bar<8>(); qstep(IC<1>{}, IC<2>{}, [&](int n) { if (n < 4) kvr[n] = ld_v(3, n); });
      wait_vm_bar<8>(); qstep(IC<2>{}, IC<2>{}, [&](int n) { if (n < 4) kvr[4 + n] = ld_v(4, n); });
      wait_vm_bar<8>(); qstep(IC<3>{}, IC<2>{}, [&](int) {});
    } else {
      static_for(std::make_integer_sequence<int, 20>{}, [&](auto ST) {
        constexpr int st = decltype(ST)::value, mp = (st >> 1) - 1 - KVN0;
        if constexpr (st & 1) wait_vm_bar<63>(); else wait_vm_bar<st == 0 ? 1 : (mp >= 0 && mp < 8) ? 4 : 0>();
        qstep(IC<(st & 3)>{}, IC<(st < 2 ? st : 2)>{}, [&](int n) {
          if constexpr (st & 1) { if (n < 4) extra((st >> 1) - KVN0, n); } else if constexpr ((st >> 1) + 1 < 10) dma_x((st >> 1) + 1, n); });
      });
    }
#pragma unroll
    for (int nb = 0; nb < NB; ++nb) { amfma(s.o[nb][4], w4, xp[nb]); amfma(s.o[nb][5], w5, xp[nb]); amfma(s.o[nb][6], w6, xp[nb]); amfma(s.o[nb][7], w7, xp[nb]); }
    asm volatile("s_nop 7");
    SB();
  }
  STAMP(2);
  auto qconv = [&] {
#pragma unroll
    for (int nb = 0; nb < NB; ++nb)
#pragma unroll
      for (int e = 0; e < 4; ++e) {
        f32x4 a0 = s.o[nb][2 * e], a1 = s.o[nb][2 * e + 1];
#pragma unroll
        for (int r = 0; r < 4; ++r) { a0[r] = amul(a0[r], QSCALE); a1[r] = amul(a1[r], QSCALE); }
        auto r0 = __builtin_amdgcn_permlane16_swap(cvtpk(a0[0], a0[1]), cvtpk(a1[0], a1[1]), false, false);
        auto r1 = __builtin_amdgcn_permlane16_swap(cvtpk(a0[2], a0[3]), cvtpk(a1[2], a1[3]), false, false);
        u32x4 w = {r0[0], r1[0], r0[1], r1[1]};
        s.qf[nb][e] = asb(w); pin(s.qf[nb][e]);
      }
  };
  if constexpr (NB == 2) qconv();
  wait_vm_bar<0>();
  pin(kvr[NB == 2 ? 19 : 7]);
  const unsigned long long tag = (((unsigned long long)__builtin_amdgcn_readfirstlane((unsigned)(epoch >> 32)) << 32) | __builtin_amdgcn_readfirstlane((unsigned)epoch)) + 1ull;
  const int pg = ((g & 1) << 1) | (g >> 1);
  unsigned ka[4];
#pragma unroll
  for (int st = 0; st < 4; ++st) ka[st] = lds0 + c.hs * KV_HS + l15 * 256 + (((4 * st + pg) ^ l15) * 16);
  unsigned va = lds0 + V_OFF + c.hs * KV_HS + (g >> 1) * 2048 + (4 * (g & 1) + (l15 >> 2)) * 32 + (lane & 3) * 8;
  auto rk = [&](int kt, int st) { return ld128(ka[st] + kt * 4096); };
  auto rv = [&](int db) { const unsigned a = va + (db >> 2) * 1024 + (db & 3) * 256; return cat(ldtr(a), ldtr(a + 4096)); };
  {
    bf16x8 kf0[4], kf1[4];
#pragma unroll
    for (int st = 0; st < 4; ++st) { kf0[st] = rk(0, st); kf1[st] = rk(1, st); }
    const unsigned wb = lds0 + c.j * 2048 + lane * 16;
    if constexpr (NB == 3) {
#pragma unroll
      for (int q = 0; q < 8; ++q) st128(wb + vslot(3 + (q >> 2), (q >> 1) & 1) + (q & 1) * 1024, kvr[q]);
      SB();
      qconv();
    }
#pragma unroll
    for (int st = 0; st < 4; ++st)
#pragma unroll
      for (int nb = 0; nb < NB; ++nb) {
        s.S0[nb] = mfma(kf0[st], s.qf[nb][st], st == 0 ? z4 : s.S0[nb]); s.S1[nb] = mfma(kf1[st], s.qf[nb][st], st == 0 ? z4 : s.S1[nb]);
      }
#pragma unroll
    for (int nb = 0; nb < NB; ++nb) {
#pragma unroll
      for (int db = (NB == 3 ? 6 : 5); db < 8; ++db) { s.o[nb][db] = z4; pin(s.o[nb][db]); }
      const float m = xmax32(xmax16(max8(s.S0[nb], s.S1[nb]))) + BIAS;
      s.negm[nb] = z4 - m; s.S0[nb] -= m; s.S1[nb] -= m;
      pin(s.negm[nb]);
    }
    s.ones = asb(u32x4{0x3f803f80u, 0x3f803f80u, 0x3f803f80u, 0x3f803f80u});
    pin(s.ones);
  }
  exps<NB, 0, 4 * NB>(s.S0); exps<NB, 0, 2 * NB>(s.S1);
  bf16x8 v5h, v6, v7;
#pragma unroll
  for (int st = 0; st < 4; ++st) ka[st] += 8192;
  SB();
  auto us = [&](auto m, auto g0, auto g1, auto g2) {
    m(0); SB(); g0(); SB(); m(1); SB(); g1(); SB();
    if constexpr (NB == 3) { m(2); SB(); g2(); SB(); }
  };
  auto QK0 = [&](int st, bf16x8 fr) { return [&s, st, fr](int nb) { if (st == 0) amfma_c(s.S0[nb], fr, s.qf[nb][0], s.negm[nb]); else amfma(s.S0[nb], fr, s.qf[nb][st]); }; };
  auto QK1 = [&](int st, bf16x8 fr) { return [&s, st, fr](int nb) { if (st == 0) amfma_c(s.S1[nb], fr, s.qf[nb][0], s.negm[nb]); else amfma(s.S1[nb], fr, s.qf[nb][st]); }; };
  auto PVH = [&](int db, bf16x8& fr) { return [&s, db, &fr](int nb) { amfma(s.o[nb][db], fr, asb(s.pf[nb])); }; };
  auto N = [] {};
#define E0(e) [&] { exp1<NB, (e)>(s.S0); }
#define E1(e) [&] { exp1<NB, (e)>(s.S1); }
#define CLO(nb) [&] { if constexpr ((nb) < NB) { s.pf[nb][0] = acvt(s.S0[nb][0], s.S0[nb][1]); s.pf[nb][1] = acvt(s.S0[nb][2], s.S0[nb][3]); } }
#define CHI(nb) [&] { if constexpr ((nb) < NB) { s.pf[nb][2] = acvt(s.S1[nb][0], s.S1[nb][1]); s.pf[nb][3] = acvt(s.S1[nb][2], s.S1[nb][3]); } }
#define U(f, ...) [&] { use(f); SB(); (__VA_ARGS__)(); }
  constexpr unsigned ONE = 17;
  const unsigned tabk = ONE | ONE << 6 | ONE << 12 | (29u - 3 * c.hs) << 18 | (0u + 3 * c.hs) << 24;
  const unsigned tabv = (6u + 3 * c.hs) | ONE << 6 | ONE << 12 | ONE << 18 | (23u - 3 * c.hs) << 24;
  auto astep = [&](auto FI, int t, int sl6, auto kvh) {
    constexpr bool FIRST = decltype(FI)::value;
    const int dk = (int)(((tabk >> sl6) & 63) << 13) - 131072, dv = (int)(((tabv >> sl6) & 63) << 13) - 131072;
    if constexpr (NB == 2) wait_vm_bar<8>(); else wait_vm_bar<63>();
    auto PH = [&](int db, bf16x8& fr) { return [&, db](int nb) { if constexpr (!FIRST) amfma(s.o[nb][db], fr, asb(s.pf[nb])); }; };
    auto PV = [&](int db, bf16x8 fr) { return [&s, db, fr](int nb) { amf<FIRST>(s.o[nb][db], fr, asb(s.pf[nb])); }; };
    auto SUM = [&](int nb) { amf<FIRST>(s.l[nb], s.ones, asb(s.pf[nb])); };
    bf16x8 a0, a1, a2, a3, b0, b1, b2, b3, v0, v1, v2, v3, v4;
    unsigned o1, o2;
    if constexpr (NB == 3) {
      bf16x8 v5;
      us(PH(6, v6), [&] { a0 = rk(0, 0); a1 = rk(0, 1); }, [&] { a2 = rk(0, 2); a3 = rk(0, 3); }, E1(6));
      us(PH(7, v7), CLO(0), CLO(1), U(a0, CLO(2)));
      us(QK0(0, a0), [&] { b0 = rk(1, 0); b1 = rk(1, 1); }, E1(7), U(a1, E1(8)));
      us(QK0(1, a1), E1(9), E1(10), U(a2, E1(11)));
      us(QK0(2, a2), [&] { o1 = aor3(s.pf[0][0], s.pf[0][1], s.pf[1][0]); o2 = aor3(s.pf[1][1], s.pf[2][0], s.pf[2][1]); }, CHI(0), U(a3, CHI(1)));
      us(QK0(3, a3), CHI(2), [&] { o1 = aor3(o1, s.pf[0][2], s.pf[0][3]); o2 = aor3(o2, s.pf[1][2], s.pf[1][3]); },
                     U(b0, [&] { o1 = aor3(o1, s.pf[2][2], s.pf[2][3]); s.orv = o1 | o2; }));
      if constexpr (!FIRST) check<NB, true>(s, c.rk, c.hs * 131072 + ((t - 1 + c.rot) & 15) * 8192);
      SB();
      us(QK1(0, b0), [&] { b2 = rk(1, 2); b3 = rk(1, 3); }, [&] { v0 = rv(0); }, U(b1, [&] { aaddu(ka[0], dk); aaddu(ka[1], dk); }));
      us(QK1(1, b1), [&] { v1 = rv(1); }, E0(0), U(b2, E0(1)));
      us(QK1(2, b2), [&] { v2 = rv(2); }, E0(2), U(b3, E0(3)));
      us(QK1(3, b3), [&] { v3 = rv(3); }, E0(4), E0(5));
      us(SUM, E0(6), E0(7), U(v0, E0(8)));
      us(PV(0, v0), [&] { v4 = rv(4); }, E0(9), U(v1, E0(10)));
      us(PV(1, v1), [&] { v5 = rv(5); }, E0(11), U(v2, E1(0)));
      us(PV(2, v2), [&] { v6 = rv(6); }, E1(1), U(v3, E1(2)));
      us(PV(3, v3), [&] { v7 = rv(7); }, E1(3), U(v4, E1(4)));
      us(PV(4, v4), E1(5), [&] { aaddu(ka[2], dk); aaddu(ka[3], dk); }, U(v5, [&] { aaddu(va, dv); }));
      us(PV(5, v5), N, [&] { use(v7); }, N);
    } else {
      us(PH(5, v5h), [&] { a0 = rk(0, 0); a1 = rk(0, 1); }, [&] { a2 = rk(0, 2); a3 = rk(0, 3); }, N);
      us(PH(6, v6), E1(4), [&] { kvh(0); }, N);
      us(PH(7, v7), CLO(0), U(a0, CLO(1)), N);
      us(QK0(0, a0), [&] { b0 = rk(1, 0); b1 = rk(1, 1); }, U(a1, E1(5)), N);
      us(QK0(1, a1), E1(6), U(a2, E1(7)), N);
      us(QK0(2, a2), [&] { o1 = aor3(s.pf[0][0], s.pf[0][1], s.pf[1][0]); kvh(1); }, U(a3, CHI(0)), N);
      us(QK0(3, a3), CHI(1), U(b0, [&] { o1 = aor3(o1, s.pf[1][1], s.pf[0][2]); o2 = aor3(s.pf[0][3], s.pf[1][2], s.pf[1][3]); s.orv = o1 | o2; }), N);
      if constexpr (!FIRST) check<NB, true>(s, c.rk, c.hs * 131072 + ((t - 1 + c.rot) & 15) * 8192);
      SB();
      us(QK1(0, b0), [&] { b2 = rk(1, 2); b3 = rk(1, 3); }, U(b1, [&] { v0 = rv(0); kvh(2); }), N);
      us(QK1(1, b1), [&] { v1 = rv(1); }, U(b2, [&] { aaddu(ka[0], dk); aaddu(ka[1], dk); kvh(3); }), N);
      us(QK1(2, b2), [&] { v2 = rv(2); }, U(b3, E0(0)), N);
      us(QK1(3, b3), [&] { v3 = rv(3); }, [&] { exp1<NB, 1>(s.S0); kvh(4); }, N);
      us(SUM, E0(2), U(v0, E0(3)), N);
      us(PV(0, v0), [&] { v4 = rv(4); }, U(v1, [&] { exp1<NB, 4>(s.S0); kvh(5); }), N);
      us(PV(1, v1), [&] { v5h = rv(5); }, U(v2, E0(5)), N);
      us(PV(2, v2), [&] { v6 = rv(6); aaddu(ka[2], dk); aaddu(ka[3], dk); }, U(v3, [&] { exp1<NB, 6>(s.S0); kvh(6); }), N);
      us(PV(3, v3), [&] { v7 = rv(7); exp1<NB, 7>(s.S0); }, U(v4, [&] { exp1<NB, 0>(s.S1); kvh(7); }), N);
      us(PV(4, v4), [&] { exp1<NB, 1>(s.S1); exp1<NB, 2>(s.S1); }, [&] { exp1<NB, 3>(s.S1); aaddu(va, dv); use(v7); }, N);
    }
  };
  STAMP(3);
  astep(std::true_type{}, 1, 6, [&](int n) { if constexpr (NB == 2) {
    if (n < 4) dma_kv(false, jb + kslot(0, 0), jb + kslot(0, 1), tso(5), n);
#pragma unroll
    for (int i = 0; i < 20; ++i) if (i * 8 / 20 == n) { const int m = i >> 2, q = i & 3;
      st128(jb + lane * 16 + ((m & 1) ? vslot(1 + (m >> 1), q >> 1) : kslot(2 + (m >> 1), q >> 1)) + (q & 1) * 1024, kvr[i]); } } });
  int sl6 = 12;
#pragma unroll 1
  for (int t = 2; t < (NB == 3 ? 16 : 14); ++t) {
    unsigned lk0 = 0, lk1 = 0, lv0 = 0, lv1 = 0, sk = 0, sv = 0;
    if constexpr (NB == 2) {
      auto fld = [&](unsigned tab) { return jb + (((tab >> sl6) & 63) << 13); };
      lk0 = fld(16u | 0u << 6 | 1u << 12 | 2u << 18 | 3u << 24); lk1 = fld(17u | 4u << 6 | 5u << 12 | 6u << 18 | 7u << 24);
      lv0 = fld(11u | 18u << 6 | 8u << 12 | 9u << 18 | 10u << 24); lv1 = fld(15u | 19u << 6 | 12u << 12 | 13u << 18 | 14u << 24);
      sk = t < 12 ? tso(t + 4) : OOB; sv = t < 13 ? tso(t + 3) : OOB;
      pins(lk0); pins(lk1); pins(lv0); pins(lv1); pins(sk); pins(sv);
    }
    astep(std::false_type{}, t, sl6, [&](int n) { if constexpr (NB == 2) { wait_vm<THR>(); if (n < 4) dma_kv(false, lk0, lk1, sk, n); else dma_kv(true, lv0, lv1, sv, n - 4); } });
    sl6 = sl6 == 24 ? 0 : sl6 + 6;
  }
  rsrc_t rof, rwo, rslab; int pr, hg, rotb; gu64* sync; u16* ob;
  {
    int id = c.id; asm volatile("" : "+s"(id));
    const int br = (id >> 2) & 1, b = id >> 5;
    hg = id & 3; pr = (id >> 3) & 3; rotb = b & 3;
    rof = mkr(c.ws + (size_t)((br * 8 + b) * 4 + hg) * 327680, 327680);
    rwo = mkr((const char*)(c.wo + (size_t)hg * 1024 * 640 + pr * 160) - 4096, 1024 * 1280 - pr * 320 + 4096);
    rslab = mkr(c.ws + WS_SLAB + (size_t)((br * 8 + b) * 4 + pr) * (12 * QB), 12 * QB);
    sync = (gu64*)(c.ws + WS_SYNC);
    ob = c.out + (size_t)(b * 160 + (hg & 1) * 80) * 1280 + br * 640 + pr * 160 + (hg >> 1) * 80;
  }
  unsigned lz = 0;
  const int dks = c.j >> 1, dh = c.j & 1;
  auto stg = [](int u) { return (u & 1) * 81920 + ((u ^ (u >> 1)) & 1) * 40960; };
  auto kpair = [&](int i) { return ((pr + (i >> 2)) & 3) * 4 + ((i + rotb) & 3); };
  auto wvo = [&](int n) { const int e = 64 * n + (int)lz, row = e / 20, ch = e - row * 20; return row * 1280 + ((ch ^ (((row >> 2) & 1) << 1)) * 16) + 4096; };
  auto wso = [&](int i) { return (kpair(i) * 64 + dks * 32 + dh * 16) * 1280; };
  auto wld = [&](int i, int n) { return lds0 + stg(i & 3) + dks * 20480 + 10240 + dh * 5120 + n * 1024; };
  u32x4 wr[15];
  if constexpr (NB == 2) {
    lz = laneid();
    unsigned vw[5];
#pragma unroll
    for (int n = 0; n < 5; ++n) vw[n] = wvo(n);
    auto wl = [&](int p) { wait_vm<THR>(); if (p < 15) wr[p] = __builtin_amdgcn_raw_buffer_load_b128(rwo, vw[p % 5], wso(p / 5), WONT ? 2 : 0); };
    SB();
    astep(std::false_type{}, 14, 24, [&](int n) { wl(n); });
    astep(std::false_type{}, 15, 0, [&](int n) { wl(8 + n); });
  }
  STAMP(4);
  if constexpr (NB == 3) wait_vm_bar<63>(); else wait_vm_bar<15>();
  {
    bf16x8 vf[8];
#pragma unroll
    for (int db = 0; db < 4; ++db) vf[db] = rv(db);
    SB();
    if constexpr (NB == 2) us(PVH(5, v5h), N, N, N);
    us(PVH(6, v6), N, N, N);
    exps<NB, 2 * NB, 4 * NB>(s.S1);
    us(PVH(7, v7), CLO(0), CLO(1), CLO(2));
    CHI(0)(); CHI(1)(); CHI(2)();
    s.orv = 0;
#pragma unroll
    for (int nb = 0; nb < NB; ++nb) { s.orv = aor3(s.orv, s.pf[nb][0], s.pf[nb][1]); s.orv = aor3(s.orv, s.pf[nb][2], s.pf[nb][3]); }
    SB();
    check<NB, false>(s, c.rk, c.hs * 131072 + ((15 + c.rot) & 15) * 8192);
    SB();
    if constexpr (NB == 3) lz = laneid();
#pragma unroll
    for (int db = 4; db < 8; ++db) vf[db] = rv(db);
#pragma unroll
    for (int nb = 0; nb < NB; ++nb) s.l[nb] = mfma(s.ones, asb(s.pf[nb]), s.l[nb]);
#pragma unroll
    for (int db = 0; db < 8; ++db)
#pragma unroll
      for (int nb = 0; nb < NB; ++nb) {
        s.o[nb][db] = mfma(vf[db], asb(s.pf[nb]), s.o[nb][db]);
        if constexpr (NB == 2) { const int p = db * 2 + nb; if (p < 10) { SB(); st128(wld(p / 5, p % 5) + lz * 16, wr[p]); SB(); } }
      }
  }
  STAMP(5);
  u32x4 ow[NB][4];
  const unsigned gb = lz * 16 + (pr * 8 + c.hs * 4) * 10240 + c.blk0 * 1024;
  auto ost = [&](int e, int nb) { __builtin_amdgcn_raw_buffer_store_b128(ow[nb][e], rof, gb + e * 10240 + nb * 1024, 0, 16); };
#pragma unroll
  for (int e = 0; e < 4; ++e)
#pragma unroll
    for (int nb = 0; nb < NB; ++nb) {
      const float inv = __builtin_amdgcn_rcpf(s.l[nb][0]);
      const f32x4 a0 = s.o[nb][2 * e] * inv, a1 = s.o[nb][2 * e + 1] * inv;
      ow[nb][e] = u32x4{cvtpk(a0[0], a0[1]), cvtpk(a0[2], a0[3]), cvtpk(a1[0], a1[1]), cvtpk(a1[2], a1[3])};
      if (e < EPRE) ost(e, nb);
    }
  asm volatile("s_waitcnt lgkmcnt(0)\n\ts_barrier" ::: "memory");
#pragma unroll
  for (int eh = 0; eh < 2; ++eh) {
    const unsigned lb = lds0 + stg((c.hs * 2 + eh - rotb) & 3) + c.blk0 * 1024 + lz * 16;
#pragma unroll
    for (int el = 0; el < 2; ++el)
#pragma unroll
      for (int nb = 0; nb < NB; ++nb) st128(lb + el * 20480 + nb * 1024, ow[nb][eh * 2 + el]);
  }
  if constexpr (NB == 2) {
#pragma unroll
    for (int p = 10; p < 15; ++p) st128(wld(2, p - 10) + lz * 16, wr[p]);
  }
  STAMP(6);
  {
    constexpr int NM = NB, M0 = NM == 3 ? 0 : 3;
    const int q = c.j, qm = q & 1, qn = q >> 1, zg = lz >> 4;
    unsigned vo3[5];
#pragma unroll
    for (int n = 0; n < 5; ++n) vo3[n] = NB == 3 ? wvo(n) - (n & 3) * 1024 : lz * 16 + (n >> 2) * 4096;
    const unsigned lb3 = lds0 + dks * 20480 + dh * 5120 + (NB == 3 ? 10240 : 0);
    auto so3 = [&](int i) { unsigned v = i >= 16 ? OOB : NB == 3 ? wso(i) : (kpair(i) * 2 + dks) * 10240 + dh * 5120; pins(v); return v; };
    auto dmal = [&](unsigned so, int u, int n) {
      constexpr int POL = NB == 3 ? (WONT ? 1 : 0) : 2;
      const rsrc_t r = NB == 3 ? rwo : rof;
      const unsigned vo = vo3[NB == 3 ? n : (n >> 2) * 4];
      if (n == 0) { setm0(lb3 + stg(u)); dmao<POL, 0>(vo, r, so); }
      else if (n == 1) dmao2<POL, 1024>(vo, r, so);
      else if (n == 2) dmao2<POL, 2048>(vo, r, so);
      else if (n == 3) dmao2<POL, 3072>(vo, r, so);
      else { setm0(lb3 + stg(u) + 4096); dmao<POL, 0>(vo, r, so); }
    };
    if constexpr (NB == 3) {
      const unsigned so = so3(3);
#pragma unroll
      for (int n = 0; n < 5; ++n) dmal(so, 3, n);
    }
    unsigned oa[2], wa[2][2];
    {
      oa[0] = lds0 + (qm * 5 + M0) * 1024 + lz * 16; oa[1] = oa[0] + 81920;
      const unsigned wb = lds0 + 10240 + (4 * zg + ((lz & 15) >> 2)) * 320 + (lz & 3) * 8 + qn * 160;
      const int sg = (qn ? -32 : 32) * (zg & 1);
      wa[0][0] = wb + sg; wa[0][1] = wb - sg; wa[1][0] = wa[0][0] + 81920; wa[1][1] = wa[0][1] + 81920;
      pin(oa[1]); pin(wa[1][0]); pin(wa[1][1]);
    }
    f32x4 acc[NM][5];
    bf16x8 h2, h3, h4, op[NM];
    auto interval = [&](auto UU, auto D, auto tail) {
      constexpr bool FIRST = decltype(UU)::value < 0;
      constexpr int u = FIRST ? 0 : decltype(UU)::value, so = ((u ^ (u >> 1)) & 1) * 40960;
      auto ro = [&](int ks, int m) { return ld128(oa[u & 1] + so + ks * 20480 + m * 1024); };
      auto rw = [&](int ks, int n) { const unsigned a = wa[u & 1][n & 1] + so + ks * 20480 + n * 32; return cat(ldtr(a), ldtr(a + 5120)); };
      auto mm = [&](auto Z, int n, bf16x8& wf, bf16x8 (&of)[NM], auto g0, auto g1, auto g2) {
        constexpr int z = decltype(Z)::value;
        if constexpr (z == 2) { g0(); g1(); g2(); SB(); }
        else {
          amf<z == 1>(acc[0][n], wf, of[0]); SB(); g0(); SB(); amf<z == 1>(acc[1][n], wf, of[1]); SB(); g1(); SB();
          if constexpr (NM == 3) { amf<z == 1>(acc[2][n], wf, of[2]); SB(); }
          g2(); SB();
        }
      };
      constexpr IC<FIRST ? 2 : 0> H{};
      constexpr IC<FIRST ? 1 : 0> C{};
      constexpr IC<0> P{};
      bf16x8 o0[NM], o1[NM], a0, a1, a2, a3, a4, b0, b1;
      mm(H, 2, h2, op, [&] { a0 = rw(0, 0); }, [&] { o0[0] = ro(0, 0); o0[1] = ro(0, 1); }, [&] { if constexpr (NM == 3) o0[2] = ro(0, 2); });
      mm(H, 3, h3, op, [&] { a1 = rw(0, 1); }, [&] { a2 = rw(0, 2); }, [&] { D(0); });
      mm(H, 4, h4, op, [&] { a3 = rw(0, 3); }, [&] { a4 = rw(0, 4); }, [&] { use(o0[NM - 1]); });
      mm(C, 0, a0, o0, [&] { o1[0] = ro(1, 0); o1[1] = ro(1, 1); }, [&] { use(a1); if constexpr (NM == 3) o1[2] = ro(1, 2); }, [&] { D(1); });
      mm(C, 1, a1, o0, [&] { b0 = rw(1, 0); }, [&] { use(a2); b1 = rw(1, 1); }, N0);
      mm(C, 2, a2, o0, [&] { h2 = rw(1, 2); }, [&] { use(a3); }, [&] { D(2); });
      mm(C, 3, a3, o0, [&] { h3 = rw(1, 3); }, [&] { use(a4); }, N0);
      mm(C, 4, a4, o0, [&] { h4 = rw(1, 4); }, [&] { use(b0); }, [&] { D(3); });
      mm(P, 0, b0, o1, N0, [&] { use(b1); }, [&] { D(4); });
      mm(P, 1, b1, o1, [&] { use(h4); }, N0, N0);
      tail();
#pragma unroll
      for (int m = 0; m < NM; ++m) op[m] = o1[m];
    };
    auto ND = [](int) {};
    SB();
    auto poll = [&](int base) {
      const int pid = c.id ^ (((lz & 3) ? (lz & 3) : 1) << 3);
      for (unsigned spins = 0; spins < (1u << 14); ++spins) {
        if (__all(__hip_atomic_load(sync + (base + pid) * FS, RLX_AGENT) == tag)) break;
        __builtin_amdgcn_s_sleep(1);
      }
    };
    wait_vm_bar<63>();
    interval(IC<-1>{}, [&](int n) {
#pragma unroll
      for (int k = 0; k < (4 - EPRE) * NB; ++k) if (k * 5 / ((4 - EPRE) * NB) == n) ost(EPRE + k / NB, k % NB); }, N0);
    wait_vm_bar<63>();
    STAMP(7);
    { const unsigned so = so3(4); interval(IC<1>{}, [&](int n) { if constexpr (NB == 3) dmal(so, 0, n); }, N0); }
    if constexpr (NB == 3) wait_vm_bar<5>(); else wait_vm_bar<0>();
    if (c.w == 3 && lz == 0) __hip_atomic_store(sync + (1 + c.id) * FS, tag, RLX_AGENT);
    const unsigned so5 = so3(5);
    interval(IC<2>{}, [&](int n) { if constexpr (NB == 3) dmal(so5, 1, n); }, [&] { if (c.w == 2) poll(1); });
    if constexpr (NB == 3) wait_vm_bar<10>(); else wait_vm_bar<63>();
    STAMP(8);
    { const unsigned sa = so3(NB == 3 ? 6 : 4);
      interval(IC<3>{}, [&](int n) { if constexpr (NB == 3) dmal(sa, 2, n);
        else { if (n == 0) { dmal(sa, 0, 0); dmal(sa, 0, 1); } else if (n == 1) { dmal(sa, 0, 2); dmal(sa, 0, 3); } else if (n == 2) { dmal(sa, 0, 4); dmal(so5, 1, 0); }
               else if (n == 3) { dmal(so5, 1, 1); dmal(so5, 1, 2); } else { dmal(so5, 1, 3); dmal(so5, 1, 4); } } }, N0); }
    STAMP(9);
#pragma unroll 1
    for (int i4 = 4; i4 < 16; i4 += 4)
      static_for(std::make_integer_sequence<int, 4>{}, [&](auto UU) {
        constexpr int u = decltype(UU)::value;
        const unsigned so = so3(i4 + u + (NB == 3 ? 3 : 2));
        if constexpr (NB == 3) wait_vm_bar<10>(); else wait_vm_bar<5>();
        interval(UU, [&](int n) { dmal(so, (u + (NB == 3 ? 3 : 2)) & 3, n); }, N0);
      });
#pragma unroll
    for (int m = 0; m < NM; ++m) { amfma(acc[m][2], h2, op[m]); amfma(acc[m][3], h3, op[m]); amfma(acc[m][4], h4, op[m]); }
    asm volatile("s_nop 7");
    SB();
    STAMP(10);
    if (q != hg) {
      const int so = (q * 3 + ((hg - q - 1) & 3)) * QB;
#pragma unroll
      for (int e = 0; e < NM * 5; e += 2) {
        const f32x4 a = acc[e / 5][e % 5], b = e + 1 < NM * 5 ? acc[(e + 1) / 5][(e + 1) % 5] : z4;
        __builtin_amdgcn_raw_buffer_store_b128(u32x4{cvtpk(a[0], a[1]), cvtpk(a[2], a[3]), cvtpk(b[0], b[1]), cvtpk(b[2], b[3])}, rslab, lz * 16 + ((M0 ? 8 : 0) + e / 2) * 1024 + so, 0, 16);
      }
    }
    asm volatile("s_waitcnt vmcnt(0) lgkmcnt(0)\n\ts_barrier" ::: "memory");
    if (c.w == 0 && lz == 0) __hip_atomic_store(sync + (257 + c.id) * FS, tag, RLX_AGENT);
    STAMP(11);
    if (q == hg) {
#pragma unroll
      for (int e = 0; e < NM * 5; ++e) st128(lds0 + lz * 16 + (M0 * 5 + e) * 1024, __builtin_bit_cast(u32x4, acc[e / 5][e % 5]));
    }
    if (c.w == ((hg + 1) & 3)) {
      const int pid = c.id ^ ((lz & 3) ? (lz & 3) : 1);
      for (unsigned spins = 0; spins < (1u << 14); ++spins) {
        if (__all(__hip_atomic_load(sync + (257 + pid) * FS, RLX_AGENT) == tag)) break;
        __builtin_amdgcn_s_sleep(1);
      }
    }
    if (c.id == 0 && c.w == 7) {
      for (unsigned spins = 0; spins < (1u << 14); ++spins) {
        bool ok = true;
#pragma unroll
        for (int k = 0; k < 4; ++k) ok &= __hip_atomic_load(sync + (1 + lz + 64 * k) * FS, RLX_AGENT) == tag;
        if (__all(ok)) break;
        __builtin_amdgcn_s_sleep(1);
      }
      if (lz == 0) __hip_atomic_store(sync, tag, RLX_AGENT);
    }
    asm volatile("s_waitcnt vmcnt(0) lgkmcnt(0)\n\ts_barrier" ::: "memory");
    STAMP(12);
    u16* op16 = ob + (size_t)(lz & 15) * 1280 + 4 * zg;
    u32x4 r[2][3];
#pragma unroll
    for (int k = 0; k < 2; ++k)
#pragma unroll
      for (int sl = 0; sl < 3; ++sl) r[k][sl] = __builtin_amdgcn_raw_buffer_load_b128(rslab, c.w + 8 * k < 13 ? lz * 16 + (c.w + 8 * k) * 1024 : OOB, (hg * 3 + sl) * QB, 16);
#pragma unroll
    for (int k = 0; k < 2; ++k) {
      const int un = c.w + 8 * k;
#pragma unroll
      for (int hh = 0; hh < 2; ++hh) {
        const int e = un < 8 ? 2 * un + hh : 15 + 2 * (un - 8) + hh;
        if (un < 13 && !(un == 7 && hh == 1)) {
          f32x4 v = *(const LDS3 f32x4*)(size_t)(lds0 + lz * 16 + e * 1024);
#pragma unroll
          for (int sl = 0; sl < 3; ++sl) { const unsigned lo = r[k][sl][2 * hh], hi = r[k][sl][2 * hh + 1];
            v += f32x4{__uint_as_float(lo << 16), __uint_as_float(lo & 0xffff0000u), __uint_as_float(hi << 16), __uint_as_float(hi & 0xffff0000u)}; }
          const int m = e / 5, n = e - m * 5;
          *(u32x2*)(op16 + m * (16 * 1280) + n * 16) = u32x2{cvtpk(v[0], v[1]), cvtpk(v[2], v[3])};
        }
      }
    }
  }
}

__global__ __launch_bounds__(512, 2) void fused(const u16* xA, const u16* kA, const u16* vA, const u16* wqA, const u16* woA,
                                                 const u16* xB, const u16* kB, const u16* vB, const u16* wqB, const u16* woB, u16* out, char* ws) {
  extern __shared__ __attribute__((aligned(16))) char smem[];
  const int id = blockIdx.x, xg = id & 7, cc = id >> 3, br = xg >> 2, hg = xg & 3, b = cc >> 2, pr = cc & 3, h0 = hg * 8 + pr * 2;
  const int w = __builtin_amdgcn_readfirstlane(threadIdx.x >> 6);
  asm volatile("s_nop 4");
  Ctx c;
  { unsigned long long pc; asm volatile("s_getpc_b64 %0" : "=s"(pc)); c.code = pc & ~255ull; }
  const u16* x = br ? xB : xA; const u16* k = br ? kB : kA; const u16* v = br ? vB : vA; const u16* wq = br ? wqB : wqA;
  c.rx = mkr(x + (size_t)b * SQ * DM, SQ * DM * 2);
  c.rw = mkr(wq + h0 * DH, DM * NH * DH * 2 - h0 * DH * 2);
  c.rk = mkr(k + (size_t)(b * NH + h0) * SK * DH, 2 * SK * DH * 2);
  c.rv = mkr((const char*)(v + (size_t)(b * NH + h0) * SK * DH) - 1024, 2 * SK * DH * 2 + 1024);
  c.lds0 = (unsigned)(size_t)(LDS3 char*)smem;
  c.hs = w >> 2; c.j = w & 3; c.rot = (id * 5) & 15; c.krot = ((10 * pr + 5 * b) >> 2) % 10;
  c.w = w; c.id = id; c.wo = br ? woB : woA; c.out = out; c.ws = ws;
  const bool big = (c.j < 2) == (c.hs == 0);
  c.blk0 = (0x74208630u >> (4 * w)) & 15;
  if (big) body<3>(c); else body<2>(c);
}

extern "C" void kernel_launch(void* const* d_in, const int* in_sizes, int n_in, void* d_out, int out_size, void* d_ws, size_t ws_size, hipStream_t stream) {
  (void)hipFuncSetAttribute((const void*)fused, hipFuncAttributeMaxDynamicSharedMemorySize, LDS_BYTES);
  fused<<<256, 512, LDS_BYTES, stream>>>((const u16*)d_in[0], (const u16*)d_in[1], (const u16*)d_in[2], (const u16*)d_in[3], (const u16*)d_in[4],
                                         (const u16*)d_in[5], (const u16*)d_in[6], (const u16*)d_in[7], (const u16*)d_in[8], (const u16*)d_in[9], (u16*)d_out, (char*)d_ws);
}
```

```cpp
#include <hip/hip_runtime.h>
#include <hip/hip_bf16.h>
#include <type_traits>

typedef __bf16 bf16x8 __attribute__((ext_vector_type(8)));
typedef __bf16 bf16x4 __attribute__((ext_vector_type(4)));
typedef __bf16 bf16x2 __attribute__((ext_vector_type(2)));
typedef float f32x4 __attribute__((ext_vector_type(4)));
typedef float f32x2 __attribute__((ext_vector_type(2)));
typedef unsigned u32x4 __attribute__((ext_vector_type(4)));
typedef unsigned u32x2 __attribute__((ext_vector_type(2)));
typedef unsigned short u16;
typedef __attribute__((address_space(1))) unsigned long long gu64;
typedef __amdgpu_buffer_rsrc_t rsrc_t;
#define LDS3 __attribute__((address_space(3)))
#define DI __device__ __forceinline__
#define SB() __builtin_amdgcn_sched_barrier(0)
#define RLX_AGENT __ATOMIC_RELAXED, __HIP_MEMORY_SCOPE_AGENT
template <int N> using IC = std::integral_constant<int, N>;

constexpr int SQ = 160, DM = 640, NH = 32, SK = 512, DH = 128;
constexpr int LDS_BYTES = 163840;
__host__ __device__ constexpr int kslot(int sl, int hs) { return hs * 40960 + sl * 8192; }
__host__ __device__ constexpr int vslot(int sl, int hs) { return 81920 + hs * 40960 + sl * 8192; }
__host__ __device__ constexpr int xoff(int b) { return 16384 + b * 40960; }
__host__ __device__ constexpr int woff(int b) { return 90112 + (b & 1) * 16384 + (b >> 1) * 40960; }
constexpr float QSCALE = 0.08838834764831845f * 1.4426950408889634f;
constexpr float BIAS = 10.f;
constexpr unsigned OOB = 0x7ff00000u;
constexpr size_t WS_SLAB = 32u << 20, WS_SYNC = 64u << 20;
constexpr int FS = 32;
constexpr int QB = 13 * 1024;
#define KVN0 1
#define THR 4
#define EPRE 2
#define XNT false
#define WNT 1
#define WONT false

DI unsigned cvtpk(float lo, float hi) { f32x2 v = {lo, hi}; bf16x2 b = __builtin_convertvector(v, bf16x2); return __builtin_bit_cast(unsigned, b); }
DI f32x4 mfma(bf16x8 a, bf16x8 b, f32x4 c) { return __builtin_amdgcn_mfma_f32_16x16x32_bf16(a, b, c, 0, 0, 0); }
DI bf16x8 ld128(unsigned addr) { return *(const LDS3 bf16x8*)(size_t)addr; }
DI void st128(unsigned addr, u32x4 v) { *(LDS3 u32x4*)(size_t)addr = v; }
DI bf16x4 ldtr(unsigned addr) { return __builtin_amdgcn_ds_read_tr16_b64_v4bf16((LDS3 bf16x4*)(size_t)addr); }
DI bf16x8 cat(bf16x4 a, bf16x4 b) { return __builtin_shufflevector(a, b, 0, 1, 2, 3, 4, 5, 6, 7); }
DI bf16x8 asb(u32x4 w) { return __builtin_bit_cast(bf16x8, w); }
DI rsrc_t mkr(const void* p, int bytes) { return __builtin_amdgcn_make_buffer_rsrc((void*)p, (short)0, bytes, 0x00020000); }
template <bool NT> DI void dma16(unsigned ldsaddr, unsigned voff, rsrc_t r, unsigned soff) {
  if constexpr (NT) asm volatile("s_mov_b32 m0, %0\n\ts_nop 0\n\tbuffer_load_dwordx4 %1, %2, %3 offen nt lds" :: "s"(ldsaddr), "v"(voff), "s"(r), "s"(soff) : "memory");
  else              asm volatile("s_mov_b32 m0, %0\n\ts_nop 0\n\tbuffer_load_dwordx4 %1, %2, %3 offen lds" :: "s"(ldsaddr), "v"(voff), "s"(r), "s"(soff) : "memory");
}
DI void setm0(unsigned v) { asm volatile("s_mov_b32 m0, %0" :: "s"(v) : "memory"); }
template <int POL, int OFF> DI void dmao(unsigned voff, rsrc_t r, unsigned soff) {
  if constexpr (POL == 1)      asm volatile("s_nop 0\n\tbuffer_load_dwordx4 %0, %1, %2 offen offset:%3 nt lds" :: "v"(voff), "s"(r), "s"(soff), "i"(OFF) : "memory");
  else if constexpr (POL == 2) asm volatile("s_nop 0\n\tbuffer_load_dwordx4 %0, %1, %2 offen offset:%3 sc1 lds" :: "v"(voff), "s"(r), "s"(soff), "i"(OFF) : "memory");
  else                         asm volatile("s_nop 0\n\tbuffer_load_dwordx4 %0, %1, %2 offen offset:%3 lds" :: "v"(voff), "s"(r), "s"(soff), "i"(OFF) : "memory");
}
template <int POL, int OFF> DI void dmao2(unsigned voff, rsrc_t r, unsigned soff) {
  if constexpr (POL == 1)      asm volatile("buffer_load_dwordx4 %0, %1, %2 offen offset:%3 nt lds" :: "v"(voff), "s"(r), "s"(soff), "i"(OFF) : "memory");
  else if constexpr (POL == 2) asm volatile("buffer_load_dwordx4 %0, %1, %2 offen offset:%3 sc1 lds" :: "v"(voff), "s"(r), "s"(soff), "i"(OFF) : "memory");
  else                         asm volatile("buffer_load_dwordx4 %0, %1, %2 offen offset:%3 lds" :: "v"(voff), "s"(r), "s"(soff), "i"(OFF) : "memory");
}
DI void pins(unsigned& v) { v = __builtin_amdgcn_readfirstlane(v); asm volatile("" : "+s"(v)); }
template <int N> DI void wait_vm() { asm volatile("s_waitcnt vmcnt(%0)" :: "i"(N) : "memory"); }
template <int N> DI void wait_vm_bar() { asm volatile("s_waitcnt vmcnt(%0) lgkmcnt(0)\n\ts_barrier" :: "i"(N) : "memory"); }
DI float xmax16(float v) { auto r = __builtin_amdgcn_permlane16_swap(__float_as_uint(v), __float_as_uint(v), false, false); return fmaxf(__uint_as_float(r[0]), __uint_as_float(r[1])); }
DI float xmax32(float v) { auto r = __builtin_amdgcn_permlane32_swap(__float_as_uint(v), __float_as_uint(v), false, false); return fmaxf(__uint_as_float(r[0]), __uint_as_float(r[1])); }
DI void amfma(f32x4& acc, bf16x8 a, bf16x8 b) { asm volatile("v_mfma_f32_16x16x32_bf16 %0, %1, %2, %0" : "+v"(acc) : "v"(a), "v"(b)); }
DI void amfma_c(f32x4& d, bf16x8 a, bf16x8 b, f32x4 cc) { asm volatile("v_mfma_f32_16x16x32_bf16 %0, %1, %2, %3" : "=&v"(d) : "v"(a), "v"(b), "v"(cc)); }
DI void amfma_z(f32x4& d, bf16x8 a, bf16x8 b) { asm volatile("v_mfma_f32_16x16x32_bf16 %0, %1, %2, 0" : "=&v"(d) : "v"(a), "v"(b)); }
template <bool Z> DI void amf(f32x4& d, bf16x8 a, bf16x8 b) { if constexpr (Z) amfma_z(d, a, b); else amfma(d, a, b); }
DI float aexp(float x) { asm volatile("v_exp_f32 %0, %0" : "+v"(x)); return x; }
DI float amul(float a, float b) { float r; asm("v_mul_f32 %0, %1, %2" : "=v"(r) : "s"(b), "v"(a)); return r; }
DI unsigned aor3(unsigned a, unsigned b, unsigned c) { unsigned r; asm volatile("v_or3_b32 %0, %1, %2, %3" : "=v"(r) : "v"(a), "v"(b), "v"(c)); return r; }
DI unsigned acvt(float lo, float hi) { unsigned r; asm volatile("v_cvt_pk_bf16_f32 %0, %1, %2" : "=v"(r) : "v"(lo), "v"(hi)); return r; }
DI void use(bf16x8 f) { asm volatile("" :: "v"(f)); }
DI void aaddu(unsigned& a, unsigned d) { asm volatile("v_add_u32 %0, %1, %0" : "+v"(a) : "s"(d)); }
DI unsigned laneid() { unsigned l; asm volatile("v_mbcnt_lo_u32_b32 %0, -1, 0\n\tv_mbcnt_hi_u32_b32 %0, -1, %0" : "=v"(l)); return l; }
template <class T> DI void pin(T& v) { asm volatile("" : "+v"(v)); }

#define STAMP(i)

struct Ctx {
  rsrc_t rx, rw, rk, rv;
  unsigned lds0;
  int j, hs, blk0, rot, krot, w, id;
  const u16* wo; u16* out; char* ws; unsigned long long code;
};
template <int NB> struct St {
  f32x4 o[NB][8], negm[NB], S0[NB], S1[NB], l[NB];
  bf16x8 qf[NB][4], ones;
  u32x4 pf[NB];
  unsigned orv;
};
template <int NB, int E> DI void exp1(f32x4 (&S)[NB]) { if constexpr (E >= 0 && E < 4 * NB) S[E % NB][E / NB] = aexp(S[E % NB][E / NB]); }
template <int NB, int LO, int HI> DI void exps(f32x4 (&S)[NB]) { if constexpr (LO < HI) { exp1<NB, LO>(S); exps<NB, LO + 1, HI>(S); } }
DI float max8(f32x4 a, f32x4 b) { return fmaxf(fmaxf(fmaxf(a[0], a[1]), fmaxf(a[2], a[3])), fmaxf(fmaxf(b[0], b[1]), fmaxf(b[2], b[3]))); }

template <int NB, bool HAS_NEXT> DI void refix(St<NB>& s, rsrc_t rk, int ksoff) {
  const unsigned ln = laneid();
  const int kvo = (ln & 15) * 256 + ((((ln >> 4) & 1) << 1) | (ln >> 5)) * 16;
  asm volatile("s_nop 7");
#pragma unroll
  for (int nb = 0; nb < NB; ++nb) {
    f32x4 a = s.negm[nb], b = s.negm[nb];
#pragma unroll
    for (int st = 0; st < 4; ++st) {
      a = mfma(asb(__builtin_amdgcn_raw_buffer_load_b128(rk, kvo + st * 64, ksoff, 0)), s.qf[nb][st], a);
      SB();
      b = mfma(asb(__builtin_amdgcn_raw_buffer_load_b128(rk, kvo + st * 64, ksoff + 4096, 0)), s.qf[nb][st], b);
      SB();
    }
    const float d = fmaxf(xmax32(xmax16(max8(a, b))) + BIAS, 0.f);
    const float f = __builtin_amdgcn_exp2f(-d);
    s.negm[nb] -= d; if (HAS_NEXT) s.S0[nb] -= d;
    s.l[nb] *= f;
#pragma unroll
    for (int db = 0; db < 8; ++db) s.o[nb][db] *= f;
#pragma unroll
    for (int r = 0; r < 4; ++r) { a[r] = __builtin_amdgcn_exp2f(a[r] - d); b[r] = __builtin_amdgcn_exp2f(b[r] - d); }
    u32x4 w = {cvtpk(a[0], a[1]), cvtpk(a[2], a[3]), cvtpk(b[0], b[1]), cvtpk(b[2], b[3])};
    s.pf[nb] = w;
    SB();
  }
  asm volatile("s_nop 1");
}
template <int NB, bool HAS_NEXT> DI void check(St<NB>& s, const Ctx& c, int t) {
  if (__builtin_expect(__any((s.orv & 0x40004000u) != 0u), 0)) { asm volatile("" : "+s"(t)); refix<NB, HAS_NEXT>(s, c.rk, c.hs * 131072 + ((t + c.rot) & 15) * 8192); }
}
template <int... I, class F> DI void static_for(std::integer_sequence<int, I...>, F f) { (f(IC<I>{}), ...); }

template <int NB> DI void body(const Ctx& c) {
  const int lane = threadIdx.x & 63, l15 = lane & 15, g = lane >> 4;
  const unsigned lds0 = c.lds0;
  const f32x4 z4 = {0.f, 0.f, 0.f, 0.f};
  auto N0 = [] {};
  unsigned voffK[2], voffV[2];
#pragma unroll
  for (int h = 0; h < 2; ++h) {
    voffK[h] = (lane >> 4) * 256 + (((lane & 15) ^ (8 * (c.j & 1) + 4 * h + (lane >> 4))) * 16);
    voffV[h] = ((lane >> 1) & 7) * 256 + (lane >> 4) * 32 + (lane & 1) * 16 + (h ? 128 : 1024);
  }
  auto tso = [&](int t) { return (unsigned)(((t + c.rot) & 15) * 8192 + c.j * 2048); };
  auto dma_kv = [&](bool isv, unsigned lb0, unsigned lb1, unsigned so, int q) {
    if (q & 1) { if (isv) dmao2<1, 1024>(voffV[1], c.rv, so + (q >> 1) * 131072); else dmao2<1, 1024>(voffK[1], c.rk, so + (q >> 1) * 131072); }
    else { setm0(q ? lb1 : lb0); if (isv) dmao<1, 0>(voffV[0], c.rv, so + (q >> 1) * 131072); else dmao<1, 0>(voffK[0], c.rk, so + (q >> 1) * 131072); }
  };
  const unsigned jb = lds0 + c.j * 2048;
  auto ld_k = [&](int t, int q) { return __builtin_amdgcn_raw_buffer_load_b128(c.rk, voffK[q & 1] + (q & 1) * 1024, tso(t) + (q >> 1) * 131072, 2); };
  auto ld_v = [&](int t, int q) { return __builtin_amdgcn_raw_buffer_load_b128(c.rv, voffV[q & 1] + (q & 1) * 1024, tso(t) + (q >> 1) * 131072, 2); };
  STAMP(0);
  St<NB> s;
  const unsigned long long epoch = __hip_atomic_load((gu64*)(c.ws + WS_SYNC), RLX_AGENT);
  u32x4 kvr[NB == 2 ? 20 : 8];
  {
    unsigned voffS;
    if constexpr (NB == 3) { const int f = (2 * (c.j & 1) + (lane >> 5)) | ((c.j >> 1) << 2); voffS = (lane >> 5) * 8192 + (((lane & 31) ^ (f << 1)) * 16); }
    else voffS = (lane >> 3) * 1280 + (((lane & 7) ^ (4 * (c.j & 1) + g)) * 16);
    const int wi0 = (c.j & 1) + 4 * (c.j >> 1);
    const unsigned voffS2 = voffS + 4 * 8192 - 2048;
    unsigned wso0 = (2 * c.krot * 32 + 2 * wi0) * 8192;
    auto dma_w = [&](int buf, int n) {
      if (n == 0) { setm0(lds0 + woff(buf) + wi0 * 1024); dmao<WNT, 0>(voffS, c.rw, wso0); }
      else if (n == 1) dmao2<WNT, 2048>(voffS2, c.rw, wso0);
      else if (n == 2) { setm0(lds0 + woff(buf) + wi0 * 1024 + 8192); dmao<WNT, 0>(voffS, c.rw, wso0 + 16 * 8192); }
      else { dmao2<WNT, 2048>(voffS2, c.rw, wso0 + 16 * 8192); wso0 += 32 * 8192; wso0 = wso0 >= 640u * 8192u ? wso0 - 640u * 8192u : wso0; pins(wso0); }
    };
    auto dma_x = [&](int ch, int n) {
      int kc = ch + c.krot; kc = kc >= 10 ? kc - 10 : kc;
      const int i = c.j + 4 * n;
      dma16<XNT>(lds0 + xoff(ch & 1) + i * 1024, voffS, c.rx, i * (8 * 1280) + kc * 128);
    };
    auto extra = [&](int m, int q) {
      if (m == 0 || m == 1) dma_kv(false, jb + kslot(m, 0), jb + kslot(m, 1), tso(m), q);
      else if (m == 2) dma_kv(true, jb + vslot(0, 0), jb + vslot(0, 1), tso(0), q);
      else if (m == 3 || m == 5 || m == 7) kvr[(m - 3) * 4 + q] = ld_k((m + 1) / 2, q);
      else if (m == 4 || m == 6) kvr[(m - 3) * 4 + q] = ld_v((m - 2) / 2, q);
    };
    if constexpr (NB == 3) {
#pragma unroll
      for (int st = 0; st < 3; ++st)
#pragma unroll
        for (int n = 0; n < 4; ++n) dma_w(st, n);
    } else {
#pragma unroll
      for (int n = 0; n < 10; ++n) dma_x(n / 5, n % 5);
      {
        const rsrc_t rc = mkr((const void*)c.code, 100 * 1024);
        asm volatile("s_mov_b32 m0, %0\n\ts_nop 0\n\tbuffer_load_dword %1, %2, %3 offen lds" :: "s"(lds0 + 38912), "v"(lane < 7 ? (unsigned)lane * 128u : OOB), "s"(rc), "s"(((c.id >> 3) * 4 + c.j) * 896) : "memory");
      }
      __builtin_amdgcn_s_setprio(1);
    }
    unsigned wa[8], xa[2];
    const int qq = l15 >> 2, p = lane & 3, f = qq | ((g & 1) << 2);
#pragma unroll
    for (int db = 0; db < 8; ++db) wa[db] = lds0 + woff(0) + (8 * g + qq) * 512 + c.hs * 256 + ((db ^ f) * 32) + p * 8;
#pragma unroll
    for (int u = 0; u < 2; ++u) xa[u] = lds0 + xoff(0) + (c.blk0 * 16 + l15) * 128 + (((4 * u + g) ^ ((l15 >> 1) & 7)) * 16);
    bf16x8 w4, w5, w6, w7, xp[NB];
    auto qstep = [&](auto U, auto PH, auto D) {
      constexpr int u = decltype(U)::value, ph = decltype(PH)::value;
      auto rw = [&](int db) { const unsigned a = wa[db] + (woff(u) - woff(0)); return cat(ldtr(a), ldtr(a + 2048)); };
      auto rx = [&](int nb) { return ld128(xa[u & 1] + (u >> 1) * 40960 + nb * 2048); };
      auto mm = [&](auto Z, int db, bf16x8& wf, bf16x8 (&xf)[NB], auto g0, auto g1, auto g2) {
        constexpr int z = decltype(Z)::value;
        if constexpr (z == 2) { g0(); g1(); g2(); SB(); }
        else {
          amf<z == 1>(s.o[0][db], wf, xf[0]); SB(); g0(); SB();
          amf<z == 1>(s.o[1][db], wf, xf[1]); SB(); g1(); SB();
          if constexpr (NB == 3) { amf<z == 1>(s.o[2][db], wf, xf[2]); SB(); }
          g2(); SB();
        }
      };
      constexpr IC<ph == 0 ? 2 : ph == 1 ? 1 : 0> H{};
      constexpr IC<ph == 0 ? 1 : 0> C{};
      bf16x8 xf[NB], w0, w1, w2, w3;
      if constexpr (NB == 3) {
        mm(H, 4, w4, xp, [&] { w0 = rw(0); }, [&] { xf[0] = rx(0); xf[1] = rx(1); }, [&] { xf[2] = rx(2); });
        mm(H, 5, w5, xp, [&] { w1 = rw(1); }, [&] { D(0); }, N0);
        mm(H, 6, w6, xp, [&] { w2 = rw(2); }, [&] { w3 = rw(3); }, N0);
        mm(H, 7, w7, xp, [&] { w4 = rw(4); }, [&] { D(1); }, [&] { use(xf[NB - 1]); });
        mm(C, 0, w0, xf, [&] { w5 = rw(5); }, [&] { use(w1); w6 = rw(6); }, N0);
        mm(C, 1, w1, xf, [&] { w7 = rw(7); }, [&] { use(w2); D(2); }, N0);
        mm(C, 2, w2, xf, [&] { use(w3); }, [&] { D(3); }, N0);
        mm(C, 3, w3, xf, [&] { use(w7); }, N0, N0);
      } else {
        mm(H, 4, w4, xp, [&] { w0 = rw(0); }, [&] { xf[0] = rx(0); xf[1] = rx(1); }, N0);
        mm(H, 5, w5, xp, [&] { w1 = rw(1); }, [&] { D(0); }, N0);
        mm(H, 6, w6, xp, [&] { w2 = rw(2); }, [&] { w3 = rw(3); D(1); }, N0);
        mm(H, 7, w7, xp, [&] { w4 = rw(4); }, [&] { D(2); use(xf[NB - 1]); }, N0);
        mm(C, 0, w0, xf, [&] { w5 = rw(5); }, [&] { use(w1); w6 = rw(6); D(3); }, N0);
        mm(C, 1, w1, xf, [&] { w7 = rw(7); }, [&] { use(w2); D(4); }, N0);
        mm(C, 2, w2, xf, [&] { use(w3); }, N0, N0);
        mm(C, 3, w3, xf, [&] { use(w7); }, N0, N0);
      }
#pragma unroll
      for (int nb = 0; nb < NB; ++nb) xp[nb] = xf[nb];
    };
    STAMP(1);
    SB();
    if constexpr (NB == 3) {
      static_for(std::make_integer_sequence<int, 4>{}, [&](auto U) {
        constexpr int u = decltype(U)::value;
        wait_vm_bar<8>(); qstep(U, IC<(u < 2 ? u : 2)>{}, [&](int n) { dma_w((u + 3) & 3, n); });
      });
#pragma unroll 1
      for (int s4 = 4; s4 < 16; s4 += 4)
        static_for(std::make_integer_sequence<int, 4>{}, [&](auto U) {
          constexpr int u = decltype(U)::value;
          wait_vm_bar<8>(); qstep(U, IC<2>{}, [&](int n) { dma_w((u + 3) & 3, n); });
        });
      wait_vm_bar<8>(); qstep(IC<0>{}, IC<2>{}, [&](int n) { dma_w(3, n); });
      wait_vm_bar<8>(); qstep(IC<1>{}, IC<2>{}, [&](int n) { if (n < 4) kvr[n] = ld_v(3, n); });
      wait_vm_bar<8>(); qstep(IC<2>{}, IC<2>{}, [&](int n) { if (n < 4) kvr[4 + n] = ld_v(4, n); });
      wait_vm_bar<8>(); qstep(IC<3>{}, IC<2>{}, [&](int) {});
    } else {
      static_for(std::make_integer_sequence<int, 20>{}, [&](auto ST) {
        constexpr int st = decltype(ST)::value, mp = (st >> 1) - 1 - KVN0;
        if constexpr (st & 1) wait_vm_bar<63>(); else wait_vm_bar<st == 0 ? 6 : (mp >= 0 && mp < 8) ? 4 : 0>();
        qstep(IC<(st & 3)>{}, IC<(st < 2 ? st : 2)>{}, [&](int n) {
          if constexpr (st & 1) { if (n < 4) extra((st >> 1) - KVN0, n); } else if constexpr (st > 0 && (st >> 1) + 1 < 10) dma_x((st >> 1) + 1, n); });
      });
    }
#pragma unroll
    for (int nb = 0; nb < NB; ++nb) { amfma(s.o[nb][4], w4, xp[nb]); amfma(s.o[nb][5], w5, xp[nb]); amfma(s.o[nb][6], w6, xp[nb]); amfma(s.o[nb][7], w7, xp[nb]); }
    asm volatile("s_nop 7");
    SB();
  }
  STAMP(2);
  auto qconv = [&] {
#pragma unroll
    for (int nb = 0; nb < NB; ++nb)
#pragma unroll
      for (int e = 0; e < 4; ++e) {
        f32x4 a0 = s.o[nb][2 * e], a1 = s.o[nb][2 * e + 1];
#pragma unroll
        for (int r = 0; r < 4; ++r) { a0[r] = amul(a0[r], QSCALE); a1[r] = amul(a1[r], QSCALE); }
        auto r0 = __builtin_amdgcn_permlane16_swap(cvtpk(a0[0], a0[1]), cvtpk(a1[0], a1[1]), false, false);
        auto r1 = __builtin_amdgcn_permlane16_swap(cvtpk(a0[2], a0[3]), cvtpk(a1[2], a1[3]), false, false);
        u32x4 w = {r0[0], r1[0], r0[1], r1[1]};
        s.qf[nb][e] = asb(w); pin(s.qf[nb][e]);
      }
  };
  if constexpr (NB == 2) qconv();
  wait_vm_bar<0>();
  pin(kvr[NB == 2 ? 19 : 7]);
  const unsigned long long tag = (((unsigned long long)__builtin_amdgcn_readfirstlane((unsigned)(epoch >> 32)) << 32) | __builtin_amdgcn_readfirstlane((unsigned)epoch)) + 1ull;
  const int pg = ((g & 1) << 1) | (g >> 1);
  unsigned ka[4];
#pragma unroll
  for (int st = 0; st < 4; ++st) ka[st] = lds0 + kslot(0, c.hs) + l15 * 256 + (((4 * st + pg) ^ l15) * 16);
  unsigned va = lds0 + vslot(0, c.hs) + (g >> 1) * 2048 + (4 * (g & 1) + (l15 >> 2)) * 32 + (lane & 3) * 8;
  auto rks = [&](int sl, int kt, int st) { return ld128(ka[st] + sl * 8192 + kt * 4096); };
  auto rvs = [&](int sl, int db) { const unsigned a = va + sl * 8192 + (db >> 2) * 1024 + (db & 3) * 256; return cat(ldtr(a), ldtr(a + 4096)); };
  {
    bf16x8 kf0[4], kf1[4];
#pragma unroll
    for (int st = 0; st < 4; ++st) { kf0[st] = rks(0, 0, st); kf1[st] = rks(0, 1, st); }
    const unsigned wb = lds0 + c.j * 2048 + lane * 16;
    if constexpr (NB == 3) {
#pragma unroll
      for (int q = 0; q < 8; ++q) st128(wb + vslot(3 + (q >> 2), (q >> 1) & 1) + (q & 1) * 1024, kvr[q]);
      SB();
      qconv();
    }
#pragma unroll
    for (int st = 0; st < 4; ++st)
#pragma unroll
      for (int nb = 0; nb < NB; ++nb) {
        s.S0[nb] = mfma(kf0[st], s.qf[nb][st], st == 0 ? z4 : s.S0[nb]); s.S1[nb] = mfma(kf1[st], s.qf[nb][st], st == 0 ? z4 : s.S1[nb]);
      }
#pragma unroll
    for (int nb = 0; nb < NB; ++nb) {
#pragma unroll
      for (int db = (NB == 3 ? 6 : 5); db < 8; ++db) { s.o[nb][db] = z4; pin(s.o[nb][db]); }
      const float m = xmax32(xmax16(max8(s.S0[nb], s.S1[nb]))) + BIAS;
      s.negm[nb] = z4 - m; s.S0[nb] -= m; s.S1[nb] -= m;
      pin(s.negm[nb]);
    }
    s.ones = asb(u32x4{0x3f803f80u, 0x3f803f80u, 0x3f803f80u, 0x3f803f80u});
    pin(s.ones);
  }
  exps<NB, 0, 4 * NB>(s.S0); exps<NB, 0, 2 * NB>(s.S1);
  bf16x8 v5h, v6, v7;
  SB();
  auto us = [&](auto m, auto g0, auto g1, auto g2) {
    m(0); SB(); g0(); SB(); m(1); SB(); g1(); SB();
    if constexpr (NB == 3) { m(2); SB(); g2(); SB(); }
  };
  auto QK0 = [&](int st, bf16x8 fr) { return [&s, st, fr](int nb) { if (st == 0) amfma_c(s.S0[nb], fr, s.qf[nb][0], s.negm[nb]); else amfma(s.S0[nb], fr, s.qf[nb][st]); }; };
  auto QK1 = [&](int st, bf16x8 fr) { return [&s, st, fr](int nb) { if (st == 0) amfma_c(s.S1[nb], fr, s.qf[nb][0], s.negm[nb]); else amfma(s.S1[nb], fr, s.qf[nb][st]); }; };
  auto PVH = [&](int db, bf16x8& fr) { return [&s, db, &fr](int nb) { amfma(s.o[nb][db], fr, asb(s.pf[nb])); }; };
  auto N = [] {};
#define E0(e) [&] { exp1<NB, (e)>(s.S0); }
#define E1(e) [&] { exp1<NB, (e)>(s.S1); }
#define CLO(nb) [&] { if constexpr ((nb) < NB) { s.pf[nb][0] = acvt(s.S0[nb][0], s.S0[nb][1]); s.pf[nb][1] = acvt(s.S0[nb][2], s.S0[nb][3]); } }
#define CHI(nb) [&] { if constexpr ((nb) < NB) { s.pf[nb][2] = acvt(s.S1[nb][0], s.S1[nb][1]); s.pf[nb][3] = acvt(s.S1[nb][2], s.S1[nb][3]); } }
#define U(f, ...) [&] { use(f); SB(); (__VA_ARGS__)(); }
  unsigned dk = 8192, dv = 8192, cnt = 1;
  if constexpr (NB == 3) {
#pragma unroll
    for (int st = 0; st < 4; ++st) ka[st] += 8192;
  }
  auto astep = [&](auto FI, auto SL, int t, auto kvh) {
    constexpr bool FIRST = decltype(FI)::value;
    constexpr int sl = decltype(SL)::value;
    auto rk = [&](int kt, int st) { return rks(NB == 3 ? 0 : sl, kt, st); };
    auto rv = [&](int db) { return rvs(NB == 3 ? 0 : (sl + 4) % 5, db); };
    if constexpr (NB == 2) wait_vm_bar<6>(); else wait_vm_bar<63>();
    auto PH = [&](int db, bf16x8& fr) { return [&, db](int nb) { if constexpr (!FIRST) amfma(s.o[nb][db], fr, asb(s.pf[nb])); }; };
    auto PV = [&](int db, bf16x8 fr) { return [&s, db, fr](int nb) { amf<FIRST>(s.o[nb][db], fr, asb(s.pf[nb])); }; };
    auto SUM = [&](int nb) { amf<FIRST>(s.l[nb], s.ones, asb(s.pf[nb])); };
    bf16x8 a0, a1, a2, a3, b0, b1, b2, b3, v0, v1, v2, v3, v4;
    unsigned o1, o2;
    if constexpr (NB == 3) {
      bf16x8 v5;
      us(PH(6, v6), [&] { a0 = rk(0, 0); a1 = rk(0, 1); }, [&] { a2 = rk(0, 2); a3 = rk(0, 3); }, E1(6));
      us(PH(7, v7), CLO(0), CLO(1), U(a0, CLO(2)));
      us(QK0(0, a0), [&] { b0 = rk(1, 0); b1 = rk(1, 1); }, E1(7), U(a1, E1(8)));
      us(QK0(1, a1), E1(9), E1(10), U(a2, E1(11)));
      us(QK0(2, a2), [&] { o1 = aor3(s.pf[0][0], s.pf[0][1], s.pf[1][0]); o2 = aor3(s.pf[1][1], s.pf[2][0], s.pf[2][1]); }, CHI(0), U(a3, CHI(1)));
      us(QK0(3, a3), CHI(2), [&] { o1 = aor3(o1, s.pf[0][2], s.pf[0][3]); o2 = aor3(o2, s.pf[1][2], s.pf[1][3]); },
                     U(b0, [&] { o1 = aor3(o1, s.pf[2][2], s.pf[2][3]); s.orv = o1 | o2; }));
      if constexpr (!FIRST) check<NB, true>(s, c, t - 1);
      SB();
      us(QK1(0, b0), [&] { b2 = rk(1, 2); b3 = rk(1, 3); }, [&] { v0 = rv(0); }, U(b1, [&] { aaddu(ka[0], dk); aaddu(ka[1], dk); }));
      us(QK1(1, b1), [&] { v1 = rv(1); }, E0(0), U(b2, E0(1)));
      us(QK1(2, b2), [&] { v2 = rv(2); }, E0(2), U(b3, E0(3)));
      us(QK1(3, b3), [&] { v3 = rv(3); }, E0(4), E0(5));
      us(SUM, E0(6), E0(7), U(v0, E0(8)));
      us(PV(0, v0), [&] { v4 = rv(4); }, E0(9), U(v1, E0(10)));
      us(PV(1, v1), [&] { v5 = rv(5); }, E0(11), U(v2, E1(0)));
      us(PV(2, v2), [&] { v6 = rv(6); }, E1(1), U(v3, E1(2)));
      us(PV(3, v3), [&] { v7 = rv(7); }, E1(3), U(v4, E1(4)));
      us(PV(4, v4), E1(5), [&] { aaddu(ka[2], dk); aaddu(ka[3], dk); }, U(v5, [&] { aaddu(va, dv); }));
      us(PV(5, v5), [&] { dv = dk; cnt = cnt == 4 ? 0 : cnt + 1; pins(cnt); }, [&] { use(v7); dk = cnt == 4 ? -32768 : 8192; pins(dk); }, N);
    } else {
      us(PH(5, v5h), [&] { a0 = rk(0, 0); a1 = rk(0, 1); }, [&] { a2 = rk(0, 2); a3 = rk(0, 3); }, N);
      us(PH(6, v6), E1(4), [&] { kvh(0); }, N);
      us(PH(7, v7), CLO(0), U(a0, CLO(1)), N);
      us(QK0(0, a0), [&] { b0 = rk(1, 0); b1 = rk(1, 1); }, U(a1, E1(5)), N);
      us(QK0(1, a1), E1(6), U(a2, E1(7)), N);
      us(QK0(2, a2), [&] { o1 = aor3(s.pf[0][0], s.pf[0][1], s.pf[1][0]); kvh(1); }, U(a3, CHI(0)), N);
      us(QK0(3, a3), CHI(1), U(b0, [&] { o1 = aor3(o1, s.pf[1][1], s.pf[0][2]); o2 = aor3(s.pf[0][3], s.pf[1][2], s.pf[1][3]); s.orv = o1 | o2; }), N);
      if constexpr (!FIRST) check<NB, true>(s, c, t - 1);
      SB();
      us(QK1(0, b0), [&] { b2 = rk(1, 2); b3 = rk(1, 3); }, U(b1, [&] { v0 = rv(0); kvh(2); }), N);
      us(QK1(1, b1), [&] { v1 = rv(1); }, U(b2, [&] { kvh(3); }), N);
      us(QK1(2, b2), [&] { v2 = rv(2); }, U(b3, E0(0)), N);
      us(QK1(3, b3), [&] { v3 = rv(3); }, [&] { exp1<NB, 1>(s.S0); kvh(4); }, N);
      us(SUM, E0(2), U(v0, E0(3)), N);
      us(PV(0, v0), [&] { v4 = rv(4); }, U(v1, [&] { exp1<NB, 4>(s.S0); kvh(5); }), N);
      us(PV(1, v1), [&] { v5h = rv(5); }, U(v2, E0(5)), N);
      us(PV(2, v2), [&] { v6 = rv(6); }, U(v3, [&] { exp1<NB, 6>(s.S0); kvh(6); }), N);
      us(PV(3, v3), [&] { v7 = rv(7); exp1<NB, 7>(s.S0); }, U(v4, [&] { exp1<NB, 0>(s.S1); kvh(7); }), N);
      us(PV(4, v4), [&] { exp1<NB, 1>(s.S1); exp1<NB, 2>(s.S1); }, [&] { exp1<NB, 3>(s.S1); use(v7); }, N);
    }
  };
  STAMP(3);
  astep(std::true_type{}, IC<1>{}, 1, [&](int n) { if constexpr (NB == 2) {
    if (n < 4) dma_kv(false, jb + kslot(0, 0), jb + kslot(0, 1), tso(5), n);
#pragma unroll
    for (int i = 0; i < 20; ++i) if (i * 8 / 20 == n) { const int m = i >> 2, q = i & 3;
      st128(jb + lane * 16 + ((m & 1) ? vslot(1 + (m >> 1), q >> 1) : kslot(2 + (m >> 1), q >> 1)) + (q & 1) * 1024, kvr[i]); } } });
  constexpr std::false_type F{};
  if constexpr (NB == 3) {
    auto NH = [](int) {};
#pragma unroll 1
    for (int t = 2; t < 16; ++t) astep(F, IC<0>{}, t, NH);
  } else {
    static_for(std::make_integer_sequence<int, 12>{}, [&](auto TT) {
      constexpr int t = decltype(TT)::value + 2, u0 = (7 * (t - 2) + 1) / 2;
      astep(F, IC<t % 5>{}, t, [&](int n) {
        const int k = (t & 1) ? (n == 0 ? 0 : n == 3 ? 1 : n == 5 ? 2 : -1) : ((n & 1) ? -1 : n >> 1);
        if (k >= 0) {
          const int un = u0 + k, u = un + (un >= 40 ? 2 : 0), h = u & 1, v = (u >> 1) & 1, tn = 6 + (u >> 2) - v;
          setm0(jb + (v ? vslot(tn % 5, h) : kslot(tn % 5, h)));
          const unsigned so = tso(tn) + h * 131072;
          wait_vm<THR>(); dmao2<1, 0>(v ? voffV[0] : voffK[0], v ? c.rv : c.rk, so);
          wait_vm<THR>(); dmao2<1, 1024>(v ? voffV[1] : voffK[1], v ? c.rv : c.rk, so);
        }
      });
    });
  }
  rsrc_t rof, rwo, rslab; int pr, hg, rotb; gu64* sync; u16* ob;
  {
    int id = c.id; asm volatile("" : "+s"(id));
    const int br = (id >> 2) & 1, b = id >> 5;
    hg = id & 3; pr = (id >> 3) & 3; rotb = b & 3;
    rof = mkr(c.ws + (size_t)((br * 8 + b) * 4 + hg) * 327680, 327680);
    rwo = mkr((const char*)(c.wo + (size_t)hg * 1024 * 640 + pr * 160) - 4096, 1024 * 1280 - pr * 320 + 4096);
    rslab = mkr(c.ws + WS_SLAB + (size_t)((br * 8 + b) * 4 + pr) * (12 * QB), 12 * QB);
    sync = (gu64*)(c.ws + WS_SYNC);
    ob = c.out + (size_t)(b * 160 + (hg & 1) * 80) * 1280 + br * 640 + pr * 160 + (hg >> 1) * 80;
  }
  unsigned lz = 0;
  const int dks = c.j >> 1, dh = c.j & 1;
  auto stg = [](int u) { return u * 40960; };
  auto kpair = [&](int i) { return ((pr + (i >> 2)) & 3) * 4 + ((i + rotb) & 3); };
  auto wvo = [&](int n) { const int e = 64 * n + (int)lz, row = e / 20, ch = e - row * 20; return row * 1280 + ((ch ^ (((row >> 2) & 1) << 1)) * 16) + 4096; };
  auto wso = [&](int i) { return (kpair(i) * 64 + dks * 32 + dh * 16) * 1280; };
  auto wld = [&](int i, int n) { return lds0 + stg(i & 3) + 20480 + dks * 10240 + dh * 5120 + n * 1024; };
  u32x4 wr[15];
  if constexpr (NB == 2) {
    lz = laneid();
    unsigned vw[5];
#pragma unroll
    for (int n = 0; n < 5; ++n) vw[n] = wvo(n);
    auto wl = [&](int p) { wait_vm<4>(); if (p < 15) wr[p] = __builtin_amdgcn_raw_buffer_load_b128(rwo, vw[p % 5], wso(p / 5), WONT ? 2 : 0); };
    SB();
    astep(F, IC<4>{}, 14, [&](int n) { wl(n); });
    astep(F, IC<0>{}, 15, [&](int n) { wl(8 + n); });
  }
  STAMP(4);
  if constexpr (NB == 3) wait_vm_bar<63>(); else wait_vm_bar<15>();
  {
    bf16x8 vf[8];
#pragma unroll
    for (int db = 0; db < 4; ++db) vf[db] = rvs(0, db);
    SB();
    if constexpr (NB == 2) us(PVH(5, v5h), N, N, N);
    us(PVH(6, v6), N, N, N);
    exps<NB, 2 * NB, 4 * NB>(s.S1);
    us(PVH(7, v7), CLO(0), CLO(1), CLO(2));
    CHI(0)(); CHI(1)(); CHI(2)();
    s.orv = 0;
#pragma unroll
    for (int nb = 0; nb < NB; ++nb) { s.orv = aor3(s.orv, s.pf[nb][0], s.pf[nb][1]); s.orv = aor3(s.orv, s.pf[nb][2], s.pf[nb][3]); }
    SB();
    check<NB, false>(s, c, 15);
    SB();
    if constexpr (NB == 3) lz = laneid();
#pragma unroll
    for (int db = 4; db < 8; ++db) vf[db] = rvs(0, db);
#pragma unroll
    for (int nb = 0; nb < NB; ++nb) s.l[nb] = mfma(s.ones, asb(s.pf[nb]), s.l[nb]);
#pragma unroll
    for (int db = 0; db < 8; ++db)
#pragma unroll
      for (int nb = 0; nb < NB; ++nb) {
        s.o[nb][db] = mfma(vf[db], asb(s.pf[nb]), s.o[nb][db]);
        if constexpr (NB == 2) { const int p = db * 2 + nb; if (p < 15) { SB(); st128(wld(p / 5, p % 5) + lz * 16, wr[p]); SB(); } }
      }
  }
  STAMP(5);
  u32x4 ow[NB][4];
  const unsigned gb = lz * 16 + (pr * 8 + c.hs * 4) * 10240 + c.blk0 * 1024;
  auto ost = [&](int e, int nb) { __builtin_amdgcn_raw_buffer_store_b128(ow[nb][e], rof, gb + e * 10240 + nb * 1024, 0, 16); };
#pragma unroll
  for (int e = 0; e < 4; ++e)
#pragma unroll
    for (int nb = 0; nb < NB; ++nb) {
      const float inv = __builtin_amdgcn_rcpf(s.l[nb][0]);
      const f32x4 a0 = s.o[nb][2 * e] * inv, a1 = s.o[nb][2 * e + 1] * inv;
      ow[nb][e] = u32x4{cvtpk(a0[0], a0[1]), cvtpk(a0[2], a0[3]), cvtpk(a1[0], a1[1]), cvtpk(a1[2], a1[3])};
      if (e < EPRE) ost(e, nb);
    }
  asm volatile("s_waitcnt lgkmcnt(0)\n\ts_barrier" ::: "memory");
#pragma unroll
  for (int eh = 0; eh < 2; ++eh) {
    const unsigned lb = lds0 + stg((c.hs * 2 + eh - rotb) & 3) + c.blk0 * 1024 + lz * 16;
#pragma unroll
    for (int el = 0; el < 2; ++el)
#pragma unroll
      for (int nb = 0; nb < NB; ++nb) st128(lb + el * 10240 + nb * 1024, ow[nb][eh * 2 + el]);
  }
  STAMP(6);
  {
    constexpr int NM = NB, M0 = NM == 3 ? 0 : 3;
    const int q = c.j, qm = q & 1, qn = q >> 1, zg = lz >> 4;
    unsigned vo3[5];
#pragma unroll
    for (int n = 0; n < 5; ++n) vo3[n] = NB == 3 ? wvo(n) - (n & 3) * 1024 : lz * 16 + (n >> 2) * 4096;
    const unsigned lb3 = lds0 + dks * 10240 + dh * 5120 + (NB == 3 ? 20480 : 0);
    auto so3 = [&](int i) { unsigned v = (NB == 3 ? wso(i) : (kpair(i) * 2 + dks) * 10240 + dh * 5120) + (unsigned)(i >> 4) * OOB; pins(v); return v; };
    auto dmal = [&](unsigned so, int u, int n) {
      constexpr int POL = NB == 3 ? (WONT ? 1 : 0) : 2;
      const rsrc_t r = NB == 3 ? rwo : rof;
      const unsigned vo = vo3[NB == 3 ? n : (n >> 2) * 4];
      if (n == 0) { setm0(lb3 + stg(u)); dmao<POL, 0>(vo, r, so); }
      else if (n == 1) dmao2<POL, 1024>(vo, r, so);
      else if (n == 2) dmao2<POL, 2048>(vo, r, so);
      else if (n == 3) dmao2<POL, 3072>(vo, r, so);
      else { setm0(lb3 + stg(u) + 4096); dmao<POL, 0>(vo, r, so); }
    };
    if constexpr (NB == 3) {
      const unsigned so = so3(3);
#pragma unroll
      for (int n = 0; n < 5; ++n) dmal(so, 3, n);
    }
    unsigned oa[2], wa[2][2];
    {
      oa[0] = lds0 + (qm * 5 + M0) * 1024 + lz * 16; oa[1] = oa[0] + 81920;
      const unsigned wb = lds0 + 20480 + (4 * zg + ((lz & 15) >> 2)) * 320 + (lz & 3) * 8 + qn * 160;
      const int sg = (qn ? -32 : 32) * (zg & 1);
      wa[0][0] = wb + sg; wa[0][1] = wb - sg; wa[1][0] = wa[0][0] + 81920; wa[1][1] = wa[0][1] + 81920;
      pin(oa[1]); pin(wa[1][0]); pin(wa[1][1]);
    }
    f32x4 acc[NM][5];
    bf16x8 h2, h3, h4, op[NM];
    auto interval = [&](auto UU, auto D, auto tail) {
      constexpr bool FIRST = decltype(UU)::value < 0;
      constexpr int u = FIRST ? 0 : decltype(UU)::value, so = (u & 1) * 40960;
      auto ro = [&](int ks, int m) { return ld128(oa[u >> 1] + so + ks * 10240 + m * 1024); };
      auto rw = [&](int ks, int n) { const unsigned a = wa[u >> 1][n & 1] + so + ks * 10240 + n * 32; return cat(ldtr(a), ldtr(a + 5120)); };
      auto mm = [&](auto Z, int n, bf16x8& wf, bf16x8 (&of)[NM], auto g0, auto g1, auto g2) {
        constexpr int z = decltype(Z)::value;
        if constexpr (z == 2) { g0(); g1(); g2(); SB(); }
        else {
          amf<z == 1>(acc[0][n], wf, of[0]); SB(); g0(); SB(); amf<z == 1>(acc[1][n], wf, of[1]); SB(); g1(); SB();
          if constexpr (NM == 3) { amf<z == 1>(acc[2][n], wf, of[2]); SB(); }
          g2(); SB();
        }
      };
      constexpr IC<FIRST ? 2 : 0> H{};
      constexpr IC<FIRST ? 1 : 0> C{};
      constexpr IC<0> P{};
      bf16x8 o0[NM], o1[NM], a0, a1, a2, a3, a4, b0, b1;
      mm(H, 2, h2, op, [&] { a0 = rw(0, 0); }, [&] { o0[0] = ro(0, 0); o0[1] = ro(0, 1); }, [&] { if constexpr (NM == 3) o0[2] = ro(0, 2); });
      mm(H, 3, h3, op, [&] { a1 = rw(0, 1); }, [&] { a2 = rw(0, 2); }, [&] { D(0); });
      mm(H, 4, h4, op, [&] { a3 = rw(0, 3); }, [&] { a4 = rw(0, 4); }, [&] { use(o0[NM - 1]); });
      mm(C, 0, a0, o0, [&] { o1[0] = ro(1, 0); o1[1] = ro(1, 1); }, [&] { use(a1); if constexpr (NM == 3) o1[2] = ro(1, 2); }, [&] { D(1); });
      mm(C, 1, a1, o0, [&] { b0 = rw(1, 0); }, [&] { use(a2); b1 = rw(1, 1); }, N0);
      mm(C, 2, a2, o0, [&] { h2 = rw(1, 2); }, [&] { use(a3); }, [&] { D(2); });
      mm(C, 3, a3, o0, [&] { h3 = rw(1, 3); }, [&] { use(a4); }, N0);
      mm(C, 4, a4, o0, [&] { h4 = rw(1, 4); }, [&] { use(b0); }, [&] { D(3); });
      mm(P, 0, b0, o1, N0, [&] { use(b1); }, [&] { D(4); });
      mm(P, 1, b1, o1, [&] { use(h4); }, N0, N0);
      tail();
#pragma unroll
      for (int m = 0; m < NM; ++m) op[m] = o1[m];
    };
    auto ND = [](int) {};
    SB();
    auto poll = [&](int base) {
      const int pid = c.id ^ (((lz & 3) ? (lz & 3) : 1) << 3);
      for (unsigned spins = 0; spins < (1u << 14); ++spins) {
        if (__all(__hip_atomic_load(sync + (base + pid) * FS, RLX_AGENT) == tag)) break;
        __builtin_amdgcn_s_sleep(1);
      }
    };
    wait_vm_bar<63>();
    interval(IC<-1>{}, [&](int n) {
#pragma unroll
      for (int k = 0; k < (4 - EPRE) * NB; ++k) if (k * 5 / ((4 - EPRE) * NB) == n) ost(EPRE + k / NB, k % NB); }, N0);
    wait_vm_bar<63>();
    STAMP(7);
    { const unsigned so = so3(4); interval(IC<1>{}, [&](int n) { if constexpr (NB == 3) dmal(so, 0, n); }, N0); }
    if constexpr (NB == 3) wait_vm_bar<5>(); else wait_vm_bar<0>();
    if (c.w == 3 && lz == 0) __hip_atomic_store(sync + (1 + c.id) * FS, tag, RLX_AGENT);
    const unsigned so5 = so3(5);
    interval(IC<2>{}, [&](int n) { if constexpr (NB == 3) dmal(so5, 1, n); }, [&] { if (c.w == 2) poll(1); });
    if constexpr (NB == 3) wait_vm_bar<10>(); else wait_vm_bar<63>();
    STAMP(8);
    { const unsigned sa = so3(NB == 3 ? 6 : 4);
      interval(IC<3>{}, [&](int n) { if constexpr (NB == 3) dmal(sa, 2, n);
        else { if (n == 0) { dmal(sa, 0, 0); dmal(sa, 0, 1); } else if (n == 1) { dmal(sa, 0, 2); dmal(sa, 0, 3); } else if (n == 2) { dmal(sa, 0, 4); dmal(so5, 1, 0); }
               else if (n == 3) { dmal(so5, 1, 1); dmal(so5, 1, 2); } else { dmal(so5, 1, 3); dmal(so5, 1, 4); } } }, N0); }
    STAMP(9);
    unsigned son = so3(NB == 3 ? 7 : 6);
#pragma unroll 1
    for (int i4 = 4; i4 < 16; i4 += 4)
      static_for(std::make_integer_sequence<int, 4>{}, [&](auto UU) {
        constexpr int u = decltype(UU)::value;
        if constexpr (NB == 3) wait_vm_bar<10>(); else wait_vm_bar<5>();
        interval(UU, [&](int n) { dmal(son, (u + (NB == 3 ? 3 : 2)) & 3, n); if (n == 4) son = so3(i4 + u + (NB == 3 ? 4 : 3)); }, N0);
      });
#pragma unroll
    for (int m = 0; m < NM; ++m) { amfma(acc[m][2], h2, op[m]); amfma(acc[m][3], h3, op[m]); amfma(acc[m][4], h4, op[m]); }
    asm volatile("s_nop 7");
    SB();
    STAMP(10);
    if (q != hg) {
      const int so = (q * 3 + ((hg - q - 1) & 3)) * QB;
#pragma unroll
      for (int e = 0; e < NM * 5; e += 2) {
        const f32x4 a = acc[e / 5][e % 5], b = e + 1 < NM * 5 ? acc[(e + 1) / 5][(e + 1) % 5] : z4;
        __builtin_amdgcn_raw_buffer_store_b128(u32x4{cvtpk(a[0], a[1]), cvtpk(a[2], a[3]), cvtpk(b[0], b[1]), cvtpk(b[2], b[3])}, rslab, lz * 16 + ((M0 ? 8 : 0) + e / 2) * 1024 + so, 0, 16);
      }
    }
    asm volatile("s_waitcnt vmcnt(0) lgkmcnt(0)\n\ts_barrier" ::: "memory");
    if (c.w == 0 && lz == 0) __hip_atomic_store(sync + (257 + c.id) * FS, tag, RLX_AGENT);
    STAMP(11);
    if (q == hg) {
#pragma unroll
      for (int e = 0; e < NM * 5; ++e) st128(lds0 + lz * 16 + (M0 * 5 + e) * 1024, __builtin_bit_cast(u32x4, acc[e / 5][e % 5]));
    }
    if (c.w == ((hg + 1) & 3)) {
      const int pid = c.id ^ ((lz & 3) ? (lz & 3) : 1);
      for (unsigned spins = 0; spins < (1u << 14); ++spins) {
        if (__all(__hip_atomic_load(sync + (257 + pid) * FS, RLX_AGENT) == tag)) break;
        __builtin_amdgcn_s_sleep(1);
      }
    }
    if (c.id == 0 && c.w == 7) {
      for (unsigned spins = 0; spins < (1u << 14); ++spins) {
        bool ok = true;
#pragma unroll
        for (int k = 0; k < 4; ++k) ok &= __hip_atomic_load(sync + (1 + lz + 64 * k) * FS, RLX_AGENT) == tag;
        if (__all(ok)) break;
        __builtin_amdgcn_s_sleep(1);
      }
      if (lz == 0) __hip_atomic_store(sync, tag, RLX_AGENT);
    }
    asm volatile("s_waitcnt vmcnt(0) lgkmcnt(0)\n\ts_barrier" ::: "memory");
    STAMP(12);
    u16* op16 = ob + (size_t)(lz & 15) * 1280 + 4 * zg;
    u32x4 r[2][3];
#pragma unroll
    for (int k = 0; k < 2; ++k)
#pragma unroll
      for (int sl = 0; sl < 3; ++sl) r[k][sl] = __builtin_amdgcn_raw_buffer_load_b128(rslab, c.w + 8 * k < 13 ? lz * 16 + (c.w + 8 * k) * 1024 : OOB, (hg * 3 + sl) * QB, 16);
#pragma unroll
    for (int k = 0; k < 2; ++k) {
      const int un = c.w + 8 * k;
#pragma unroll
      for (int hh = 0; hh < 2; ++hh) {
        const int e = un < 8 ? 2 * un + hh : 15 + 2 * (un - 8) + hh;
        if (un < 13 && !(un == 7 && hh == 1)) {
          f32x4 v = *(const LDS3 f32x4*)(size_t)(lds0 + lz * 16 + e * 1024);
#pragma unroll
          for (int sl = 0; sl < 3; ++sl) { const unsigned lo = r[k][sl][2 * hh], hi = r[k][sl][2 * hh + 1];
            v += f32x4{__uint_as_float(lo << 16), __uint_as_float(lo & 0xffff0000u), __uint_as_float(hi << 16), __uint_as_float(hi & 0xffff0000u)}; }
          const int m = e / 5, n = e - m * 5;
          *(u32x2*)(op16 + m * (16 * 1280) + n * 16) = u32x2{cvtpk(v[0], v[1]), cvtpk(v[2], v[3])};
        }
      }
    }
  }
}

__global__ __launch_bounds__(512, 2) void fused(const u16* xA, const u16* kA, const u16* vA, const u16* wqA, const u16* woA,
                                                 const u16* xB, const u16* kB, const u16* vB, const u16* wqB, const u16* woB, u16* out, char* ws) {
  extern __shared__ __attribute__((aligned(16))) char smem[];
  const int id = blockIdx.x, xg = id & 7, cc = id >> 3, br = xg >> 2, hg = xg & 3, b = cc >> 2, pr = cc & 3, h0 = hg * 8 + pr * 2;
  const int w = __builtin_amdgcn_readfirstlane(threadIdx.x >> 6);
  asm volatile("s_nop 4");
  Ctx c;
  { unsigned long long pc; asm volatile("s_getpc_b64 %0" : "=s"(pc)); c.code = pc & ~255ull; }
  const u16* x = br ? xB : xA; const u16* k = br ? kB : kA; const u16* v = br ? vB : vA; const u16* wq = br ? wqB : wqA;
  c.rx = mkr(x + (size_t)b * SQ * DM, SQ * DM * 2);
  c.rw = mkr(wq + h0 * DH, DM * NH * DH * 2 - h0 * DH * 2);
  c.rk = mkr(k + (size_t)(b * NH + h0) * SK * DH, 2 * SK * DH * 2);
  c.rv = mkr((const char*)(v + (size_t)(b * NH + h0) * SK * DH) - 1024, 2 * SK * DH * 2 + 1024);
  c.lds0 = (unsigned)(size_t)(LDS3 char*)smem;
  c.hs = w >> 2; c.j = w & 3; c.rot = (id * 5) & 15; c.krot = ((10 * pr + 5 * b) >> 2) % 10;
  c.w = w; c.id = id; c.wo = br ? woB : woA; c.out = out; c.ws = ws;
  const bool big = (c.j < 2) == (c.hs == 0);
  c.blk0 = (0x74208630u >> (4 * w)) & 15;
  if (big) body<3>(c); else body<2>(c);
}

extern "C" void kernel_launch(void* const* d_in, const int* in_sizes, int n_in, void* d_out, int out_size, void* d_ws, size_t ws_size, hipStream_t stream) {
  (void)hipFuncSetAttribute((const void*)fused, hipFuncAttributeMaxDynamicSharedMemorySize, LDS_BYTES);
  fused<<<256, 512, LDS_BYTES, stream>>>((const u16*)d_in[0], (const u16*)d_in[1], (const u16*)d_in[2], (const u16*)d_in[3], (const u16*)d_in[4],
                                         (const u16*)d_in[5], (const u16*)d_in[6], (const u16*)d_in[7], (const u16*)d_in[8], (const u16*)d_in[9], (u16*)d_out, (char*)d_ws);
}
```

```cpp
#include <hip/hip_runtime.h>
#include <hip/hip_bf16.h>
#include <type_traits>

typedef __bf16 bf16x8 __attribute__((ext_vector_type(8)));
typedef __bf16 bf16x4 __attribute__((ext_vector_type(4)));
typedef __bf16 bf16x2 __attribute__((ext_vector_type(2)));
typedef float f32x4 __attribute__((ext_vector_type(4)));
typedef float f32x2 __attribute__((ext_vector_type(2)));
typedef unsigned u32x4 __attribute__((ext_vector_type(4)));
typedef unsigned u32x2 __attribute__((ext_vector_type(2)));
typedef unsigned short u16;
typedef __attribute__((address_space(1))) unsigned long long gu64;
typedef __amdgpu_buffer_rsrc_t rsrc_t;
#define LDS3 __attribute__((address_space(3)))
#define DI __device__ __forceinline__
#define SB() __builtin_amdgcn_sched_barrier(0)
#define RLX_AGENT __ATOMIC_RELAXED, __HIP_MEMORY_SCOPE_AGENT
template <int N> using IC = std::integral_constant<int, N>;

constexpr int SQ = 160, DM = 640, NH = 32, SK = 512, DH = 128;
constexpr int LDS_BYTES = 163840;
__host__ __device__ constexpr int kslot(int sl, int hs) { return hs * 40960 + sl * 8192; }
__host__ __device__ constexpr int vslot(int sl, int hs) { return 81920 + hs * 40960 + sl * 8192; }
__host__ __device__ constexpr int xoff(int b) { return 16384 + b * 40960; }
__host__ __device__ constexpr int woff(int b) { return 90112 + (b & 1) * 16384 + (b >> 1) * 40960; }
constexpr float QSCALE = 0.08838834764831845f * 1.4426950408889634f;
constexpr float BIAS = 10.f;
constexpr unsigned OOB = 0x7ff00000u;
constexpr size_t WS_SLAB = 32u << 20, WS_SYNC = 64u << 20;
constexpr int FS = 32;
constexpr int QB = 13 * 1024;
#define KVN0 1
#define THR 3
#define EPRE 2
#define XNT false
#define WNT 1
#define WONT false

DI unsigned cvtpk(float lo, float hi) { f32x2 v = {lo, hi}; bf16x2 b = __builtin_convertvector(v, bf16x2); return __builtin_bit_cast(unsigned, b); }
DI f32x4 mfma(bf16x8 a, bf16x8 b, f32x4 c) { return __builtin_amdgcn_mfma_f32_16x16x32_bf16(a, b, c, 0, 0, 0); }
DI bf16x8 ld128(unsigned addr) { return *(const LDS3 bf16x8*)(size_t)addr; }
DI void st128(unsigned addr, u32x4 v) { *(LDS3 u32x4*)(size_t)addr = v; }
DI bf16x4 ldtr(unsigned addr) { return __builtin_amdgcn_ds_read_tr16_b64_v4bf16((LDS3 bf16x4*)(size_t)addr); }
DI bf16x8 cat(bf16x4 a, bf16x4 b) { return __builtin_shufflevector(a, b, 0, 1, 2, 3, 4, 5, 6, 7); }
DI bf16x8 asb(u32x4 w) { return __builtin_bit_cast(bf16x8, w); }
DI rsrc_t mkr(const void* p, int bytes) { return __builtin_amdgcn_make_buffer_rsrc((void*)p, (short)0, bytes, 0x00020000); }
template <bool NT> DI void dma16(unsigned ldsaddr, unsigned voff, rsrc_t r, unsigned soff) {
  if constexpr (NT) asm volatile("s_mov_b32 m0, %0\n\ts_nop 0\n\tbuffer_load_dwordx4 %1, %2, %3 offen nt lds" :: "s"(ldsaddr), "v"(voff), "s"(r), "s"(soff) : "memory");
  else              asm volatile("s_mov_b32 m0, %0\n\ts_nop 0\n\tbuffer_load_dwordx4 %1, %2, %3 offen lds" :: "s"(ldsaddr), "v"(voff), "s"(r), "s"(soff) : "memory");
}
DI void setm0(unsigned v) { asm volatile("s_mov_b32 m0, %0" :: "s"(v) : "memory"); }
template <int POL, int OFF> DI void dmao(unsigned voff, rsrc_t r, unsigned soff) {
  if constexpr (POL == 1)      asm volatile("s_nop 0\n\tbuffer_load_dwordx4 %0, %1, %2 offen offset:%3 nt lds" :: "v"(voff), "s"(r), "s"(soff), "i"(OFF) : "memory");
  else if constexpr (POL == 2) asm volatile("s_nop 0\n\tbuffer_load_dwordx4 %0, %1, %2 offen offset:%3 sc1 lds" :: "v"(voff), "s"(r), "s"(soff), "i"(OFF) : "memory");
  else                         asm volatile("s_nop 0\n\tbuffer_load_dwordx4 %0, %1, %2 offen offset:%3 lds" :: "v"(voff), "s"(r), "s"(soff), "i"(OFF) : "memory");
}
template <int POL, int OFF> DI void dmao2(unsigned voff, rsrc_t r, unsigned soff) {
  if constexpr (POL == 1)      asm volatile("buffer_load_dwordx4 %0, %1, %2 offen offset:%3 nt lds" :: "v"(voff), "s"(r), "s"(soff), "i"(OFF) : "memory");
  else if constexpr (POL == 2) asm volatile("buffer_load_dwordx4 %0, %1, %2 offen offset:%3 sc1 lds" :: "v"(voff), "s"(r), "s"(soff), "i"(OFF) : "memory");
  else                         asm volatile("buffer_load_dwordx4 %0, %1, %2 offen offset:%3 lds" :: "v"(voff), "s"(r), "s"(soff), "i"(OFF) : "memory");
}
DI void pins(unsigned& v) { v = __builtin_amdgcn_readfirstlane(v); asm volatile("" : "+s"(v)); }
template <int N> DI void wait_vm() { asm volatile("s_waitcnt vmcnt(%0)" :: "i"(N) : "memory"); }
template <int N> DI void wait_vm_bar() { asm volatile("s_waitcnt vmcnt(%0) lgkmcnt(0)\n\ts_barrier" :: "i"(N) : "memory"); }
DI float xmax16(float v) { auto r = __builtin_amdgcn_permlane16_swap(__float_as_uint(v), __float_as_uint(v), false, false); return fmaxf(__uint_as_float(r[0]), __uint_as_float(r[1])); }
DI float xmax32(float v) { auto r = __builtin_amdgcn_permlane32_swap(__float_as_uint(v), __float_as_uint(v), false, false); return fmaxf(__uint_as_float(r[0]), __uint_as_float(r[1])); }
DI void amfma(f32x4& acc, bf16x8 a, bf16x8 b) { asm volatile("v_mfma_f32_16x16x32_bf16 %0, %1, %2, %0" : "+v"(acc) : "v"(a), "v"(b)); }
DI void amfma_c(f32x4& d, bf16x8 a, bf16x8 b, f32x4 cc) { asm volatile("v_mfma_f32_16x16x32_bf16 %0, %1, %2, %3" : "=&v"(d) : "v"(a), "v"(b), "v"(cc)); }
DI void amfma_z(f32x4& d, bf16x8 a, bf16x8 b) { asm volatile("v_mfma_f32_16x16x32_bf16 %0, %1, %2, 0" : "=&v"(d) : "v"(a), "v"(b)); }
template <bool Z> DI void amf(f32x4& d, bf16x8 a, bf16x8 b) { if constexpr (Z) amfma_z(d, a, b); else amfma(d, a, b); }
DI float aexp(float x) { asm volatile("v_exp_f32 %0, %0" : "+v"(x)); return x; }
DI float amul(float a, float b) { float r; asm("v_mul_f32 %0, %1, %2" : "=v"(r) : "s"(b), "v"(a)); return r; }
DI unsigned aor3(unsigned a, unsigned b, unsigned c) { unsigned r; asm volatile("v_or3_b32 %0, %1, %2, %3" : "=v"(r) : "v"(a), "v"(b), "v"(c)); return r; }
DI unsigned acvt(float lo, float hi) { unsigned r; asm volatile("v_cvt_pk_bf16_f32 %0, %1, %2" : "=v"(r) : "v"(lo), "v"(hi)); return r; }
DI void use(bf16x8 f) { asm volatile("" :: "v"(f)); }
DI void aaddu(unsigned& a, unsigned d) { asm volatile("v_add_u32 %0, %1, %0" : "+v"(a) : "s"(d)); }
DI unsigned laneid() { unsigned l; asm volatile("v_mbcnt_lo_u32_b32 %0, -1, 0\n\tv_mbcnt_hi_u32_b32 %0, -1, %0" : "=v"(l)); return l; }
template <class T> DI void pin(T& v) { asm volatile("" : "+v"(v)); }

#define STAMP(i)

struct Ctx {
  rsrc_t rx, rw, rk, rv;
  unsigned lds0;
  int j, hs, blk0, rot, krot, w, id;
  const u16* wo; u16* out; char* ws; unsigned long long code;
};
template <int NB> struct St {
  f32x4 o[NB][8], negm[NB], S0[NB], S1[NB], l[NB];
  bf16x8 qf[NB][4], ones;
  u32x4 pf[NB];
  unsigned orv;
};
template <int NB, int E> DI void exp1(f32x4 (&S)[NB]) { if constexpr (E >= 0 && E < 4 * NB) S[E % NB][E / NB] = aexp(S[E % NB][E / NB]); }
template <int NB, int LO, int HI> DI void exps(f32x4 (&S)[NB]) { if constexpr (LO < HI) { exp1<NB, LO>(S); exps<NB, LO + 1, HI>(S); } }
DI float max8(f32x4 a, f32x4 b) { return fmaxf(fmaxf(fmaxf(a[0], a[1]), fmaxf(a[2], a[3])), fmaxf(fmaxf(b[0], b[1]), fmaxf(b[2], b[3]))); }

template <int NB, bool HAS_NEXT> DI void refix(St<NB>& s, rsrc_t rk, int ksoff) {
  const unsigned ln = laneid();
  const int kvo = (ln & 15) * 256 + ((((ln >> 4) & 1) << 1) | (ln >> 5)) * 16;
  asm volatile("s_nop 7");
#pragma unroll
  for (int nb = 0; nb < NB; ++nb) {
    f32x4 a = s.negm[nb], b = s.negm[nb];
#pragma unroll
    for (int st = 0; st < 4; ++st) {
      a = mfma(asb(__builtin_amdgcn_raw_buffer_load_b128(rk, kvo + st * 64, ksoff, 0)), s.qf[nb][st], a);
      SB();
      b = mfma(asb(__builtin_amdgcn_raw_buffer_load_b128(rk, kvo + st * 64, ksoff + 4096, 0)), s.qf[nb][st], b);
      SB();
    }
    const float d = fmaxf(xmax32(xmax16(max8(a, b))) + BIAS, 0.f);
    const float f = __builtin_amdgcn_exp2f(-d);
    s.negm[nb] -= d; if (HAS_NEXT) s.S0[nb] -= d;
    s.l[nb] *= f;
#pragma unroll
    for (int db = 0; db < 8; ++db) s.o[nb][db] *= f;
#pragma unroll
    for (int r = 0; r < 4; ++r) { a[r] = __builtin_amdgcn_exp2f(a[r] - d); b[r] = __builtin_amdgcn_exp2f(b[r] - d); }
    u32x4 w = {cvtpk(a[0], a[1]), cvtpk(a[2], a[3]), cvtpk(b[0], b[1]), cvtpk(b[2], b[3])};
    s.pf[nb] = w;
    SB();
  }
  asm volatile("s_nop 1");
}
template <int NB, bool HAS_NEXT> DI void check(St<NB>& s, const Ctx& c, int t) {
  if (__builtin_expect(__any((s.orv & 0x40004000u) != 0u), 0)) { asm volatile("" : "+s"(t)); refix<NB, HAS_NEXT>(s, c.rk, c.hs * 131072 + ((t + c.rot) & 15) * 8192); }
}
template <int... I, class F> DI void static_for(std::integer_sequence<int, I...>, F f) { (f(IC<I>{}), ...); }

template <int NB> DI void body(const Ctx& c) {
  const int lane = threadIdx.x & 63, l15 = lane & 15, g = lane >> 4;
  const unsigned lds0 = c.lds0;
  const f32x4 z4 = {0.f, 0.f, 0.f, 0.f};
  auto N0 = [] {};
  unsigned voffK[2], voffV[2];
#pragma unroll
  for (int h = 0; h < 2; ++h) {
    voffK[h] = (lane >> 4) * 256 + (((lane & 15) ^ (8 * (c.j & 1) + 4 * h + (lane >> 4))) * 16);
    voffV[h] = ((lane >> 1) & 7) * 256 + (lane >> 4) * 32 + (lane & 1) * 16 + (h ? 128 : 1024);
  }
  auto tso = [&](int t) { return (unsigned)(((t + c.rot) & 15) * 8192 + c.j * 2048); };
  auto dma_kv = [&](bool isv, unsigned lb0, unsigned lb1, unsigned so, int q) {
    if (q & 1) { if (isv) dmao2<1, 1024>(voffV[1], c.rv, so + (q >> 1) * 131072); else dmao2<1, 1024>(voffK[1], c.rk, so + (q >> 1) * 131072); }
    else { setm0(q ? lb1 : lb0); if (isv) dmao<1, 0>(voffV[0], c.rv, so + (q >> 1) * 131072); else dmao<1, 0>(voffK[0], c.rk, so + (q >> 1) * 131072); }
  };
  const unsigned jb = lds0 + c.j * 2048;
  auto ld_k = [&](int t, int q) { return __builtin_amdgcn_raw_buffer_load_b128(c.rk, voffK[q & 1] + (q & 1) * 1024, tso(t) + (q >> 1) * 131072, 2); };
  auto ld_v = [&](int t, int q) { return __builtin_amdgcn_raw_buffer_load_b128(c.rv, voffV[q & 1] + (q & 1) * 1024, tso(t) + (q >> 1) * 131072, 2); };
  STAMP(0);
  St<NB> s;
  const unsigned long long epoch = __hip_atomic_load((gu64*)(c.ws + WS_SYNC), RLX_AGENT);
  u32x4 kvr[NB == 2 ? 20 : 8];
  {
    unsigned voffS;
    if constexpr (NB == 3) { const int f = (2 * (c.j & 1) + (lane >> 5)) | ((c.j >> 1) << 2); voffS = (lane >> 5) * 8192 + (((lane & 31) ^ (f << 1)) * 16); }
    else voffS = (lane >> 3) * 1280 + (((lane & 7) ^ (4 * (c.j & 1) + g)) * 16);
    const int wi0 = (c.j & 1) + 4 * (c.j >> 1);
    const unsigned voffS2 = voffS + 4 * 8192 - 2048;
    unsigned wso0 = (2 * c.krot * 32 + 2 * wi0) * 8192;
    auto dma_w = [&](int buf, int n) {
      if (n == 0) { setm0(lds0 + woff(buf) + wi0 * 1024); dmao<WNT, 0>(voffS, c.rw, wso0); }
      else if (n == 1) dmao2<WNT, 2048>(voffS2, c.rw, wso0);
      else if (n == 2) { setm0(lds0 + woff(buf) + wi0 * 1024 + 8192); dmao<WNT, 0>(voffS, c.rw, wso0 + 16 * 8192); }
      else { dmao2<WNT, 2048>(voffS2, c.rw, wso0 + 16 * 8192); wso0 += 32 * 8192; wso0 = wso0 >= 640u * 8192u ? wso0 - 640u * 8192u : wso0; pins(wso0); }
    };
    auto dma_x = [&](int ch, int n) {
      int kc = ch + c.krot; kc = kc >= 10 ? kc - 10 : kc;
      const int i = c.j + 4 * n;
      dma16<XNT>(lds0 + xoff(ch & 1) + i * 1024, voffS, c.rx, i * (8 * 1280) + kc * 128);
    };
    auto extra = [&](int m, int q) {
      if (m == 0 || m == 1) dma_kv(false, jb + kslot(m, 0), jb + kslot(m, 1), tso(m), q);
      else if (m == 2) dma_kv(true, jb + vslot(0, 0), jb + vslot(0, 1), tso(0), q);
      else if (m == 3 || m == 5 || m == 7) kvr[(m - 3) * 4 + q] = ld_k((m + 1) / 2, q);
      else if (m == 4 || m == 6) kvr[(m - 3) * 4 + q] = ld_v((m - 2) / 2, q);
    };
    if constexpr (NB == 3) {
#pragma unroll
      for (int st = 0; st < 3; ++st)
#pragma unroll
        for (int n = 0; n < 4; ++n) dma_w(st, n);
    } else {
#pragma unroll
      for (int n = 0; n < 5; ++n) dma_x(0, n);
      {
        const rsrc_t rc = mkr((const void*)c.code, 100 * 1024);
        asm volatile("s_mov_b32 m0, %0\n\ts_nop 0\n\tbuffer_load_dword %1, %2, %3 offen lds" :: "s"(lds0 + 38912), "v"(lane < 7 ? (unsigned)lane * 128u : OOB), "s"(rc), "s"(((c.id >> 3) * 4 + c.j) * 896) : "memory");
      }
      __builtin_amdgcn_s_setprio(1);
    }
    unsigned wa[8], xa[2];
    const int qq = l15 >> 2, p = lane & 3, f = qq | ((g & 1) << 2);
#pragma unroll
    for (int db = 0; db < 8; ++db) wa[db] = lds0 + woff(0) + (8 * g + qq) * 512 + c.hs * 256 + ((db ^ f) * 32) + p * 8;
#pragma unroll
    for (int u = 0; u < 2; ++u) xa[u] = lds0 + xoff(0) + (c.blk0 * 16 + l15) * 128 + (((4 * u + g) ^ ((l15 >> 1) & 7)) * 16);
    bf16x8 w4, w5, w6, w7, xp[NB];
    auto qstep = [&](auto U, auto PH, auto D) {
      constexpr int u = decltype(U)::value, ph = decltype(PH)::value;
      auto rw = [&](int db) { const unsigned a = wa[db] + (woff(u) - woff(0)); return cat(ldtr(a), ldtr(a + 2048)); };
      auto rx = [&](int nb) { return ld128(xa[u & 1] + (u >> 1) * 40960 + nb * 2048); };
      auto mm = [&](auto Z, int db, bf16x8& wf, bf16x8 (&xf)[NB], auto g0, auto g1, auto g2) {
        constexpr int z = decltype(Z)::value;
        if constexpr (z == 2) { g0(); g1(); g2(); SB(); }
        else {
          amf<z == 1>(s.o[0][db], wf, xf[0]); SB(); g0(); SB();
          amf<z == 1>(s.o[1][db], wf, xf[1]); SB(); g1(); SB();
          if constexpr (NB == 3) { amf<z == 1>(s.o[2][db], wf, xf[2]); SB(); }
          g2(); SB();
        }
      };
      constexpr IC<ph == 0 ? 2 : ph == 1 ? 1 : 0> H{};
      constexpr IC<ph == 0 ? 1 : 0> C{};
      bf16x8 xf[NB], w0, w1, w2, w3;
      if constexpr (NB == 3) {
        mm(H, 4, w4, xp, [&] { w0 = rw(0); }, [&] { xf[0] = rx(0); xf[1] = rx(1); }, [&] { xf[2] = rx(2); });
        mm(H, 5, w5, xp, [&] { w1 = rw(1); }, [&] { D(0); }, N0);
        mm(H, 6, w6, xp, [&] { w2 = rw(2); }, [&] { w3 = rw(3); }, N0);
        mm(H, 7, w7, xp, [&] { w4 = rw(4); }, [&] { D(1); }, [&] { use(xf[NB - 1]); });
        mm(C, 0, w0, xf, [&] { w5 = rw(5); }, [&] { use(w1); w6 = rw(6); }, N0);
        mm(C, 1, w1, xf, [&] { w7 = rw(7); }, [&] { use(w2); D(2); }, N0);
        mm(C, 2, w2, xf, [&] { use(w3); }, [&] { D(3); }, N0);
        mm(C, 3, w3, xf, [&] { use(w7); }, N0, N0);
      } else {
        mm(H, 4, w4, xp, [&] { w0 = rw(0); }, [&] { xf[0] = rx(0); xf[1] = rx(1); }, N0);
        mm(H, 5, w5, xp, [&] { w1 = rw(1); }, [&] { D(0); }, N0);
        mm(H, 6, w6, xp, [&] { w2 = rw(2); }, [&] { w3 = rw(3); D(1); }, N0);
        mm(H, 7, w7, xp, [&] { w4 = rw(4); }, [&] { D(2); use(xf[NB - 1]); }, N0);
        mm(C, 0, w0, xf, [&] { w5 = rw(5); }, [&] { use(w1); w6 = rw(6); D(3); }, N0);
        mm(C, 1, w1, xf, [&] { w7 = rw(7); }, [&] { use(w2); D(4); }, N0);
        mm(C, 2, w2, xf, [&] { use(w3); }, N0, N0);
        mm(C, 3, w3, xf, [&] { use(w7); }, N0, N0);
      }
#pragma unroll
      for (int nb = 0; nb < NB; ++nb) xp[nb] = xf[nb];
    };
    STAMP(1);
    SB();
    if constexpr (NB == 3) {
      static_for(std::make_integer_sequence<int, 4>{}, [&](auto U) {
        constexpr int u = decltype(U)::value;
        wait_vm_bar<8>(); qstep(U, IC<(u < 2 ? u : 2)>{}, [&](int n) { dma_w((u + 3) & 3, n); });
      });
#pragma unroll 1
      for (int s4 = 4; s4 < 16; s4 += 4)
        static_for(std::make_integer_sequence<int, 4>{}, [&](auto U) {
          constexpr int u = decltype(U)::value;
          wait_vm_bar<8>(); qstep(U, IC<2>{}, [&](int n) { dma_w((u + 3) & 3, n); });
        });
      wait_vm_bar<8>(); qstep(IC<0>{}, IC<2>{}, [&](int n) { dma_w(3, n); });
      wait_vm_bar<8>(); qstep(IC<1>{}, IC<2>{}, [&](int n) { if (n < 4) kvr[n] = ld_v(3, n); });
      wait_vm_bar<8>(); qstep(IC<2>{}, IC<2>{}, [&](int n) { if (n < 4) kvr[4 + n] = ld_v(4, n); });
      wait_vm_bar<8>(); qstep(IC<3>{}, IC<2>{}, [&](int) {});
    } else {
      static_for(std::make_integer_sequence<int, 20>{}, [&](auto ST) {
        constexpr int st = decltype(ST)::value, mp = (st >> 1) - 1 - KVN0;
        if constexpr (st & 1) wait_vm_bar<63>(); else wait_vm_bar<st == 0 ? 1 : (mp >= 0 && mp < 8) ? 4 : 0>();
        qstep(IC<(st & 3)>{}, IC<(st < 2 ? st : 2)>{}, [&](int n) {
          if constexpr (st & 1) { if (n < 4) extra((st >> 1) - KVN0, n); } else if constexpr ((st >> 1) + 1 < 10) dma_x((st >> 1) + 1, n); });
      });
    }
#pragma unroll
    for (int nb = 0; nb < NB; ++nb) { amfma(s.o[nb][4], w4, xp[nb]); amfma(s.o[nb][5], w5, xp[nb]); amfma(s.o[nb][6], w6, xp[nb]); amfma(s.o[nb][7], w7, xp[nb]); }
    asm volatile("s_nop 7");
    SB();
  }
  STAMP(2);
  auto qconv = [&] {
#pragma unroll
    for (int nb = 0; nb < NB; ++nb)
#pragma unroll
      for (int e = 0; e < 4; ++e) {
        f32x4 a0 = s.o[nb][2 * e], a1 = s.o[nb][2 * e + 1];
#pragma unroll
        for (int r = 0; r < 4; ++r) { a0[r] = amul(a0[r], QSCALE); a1[r] = amul(a1[r], QSCALE); }
        auto r0 = __builtin_amdgcn_permlane16_swap(cvtpk(a0[0], a0[1]), cvtpk(a1[0], a1[1]), false, false);
        auto r1 = __builtin_amdgcn_permlane16_swap(cvtpk(a0[2], a0[3]), cvtpk(a1[2], a1[3]), false, false);
        u32x4 w = {r0[0], r1[0], r0[1], r1[1]};
        s.qf[nb][e] = asb(w); pin(s.qf[nb][e]);
      }
  };
  if constexpr (NB == 2) qconv();
  wait_vm_bar<0>();
  pin(kvr[NB == 2 ? 19 : 7]);
  const unsigned long long tag = (((unsigned long long)__builtin_amdgcn_readfirstlane((unsigned)(epoch >> 32)) << 32) | __builtin_amdgcn_readfirstlane((unsigned)epoch)) + 1ull;
  const int pg = ((g & 1) << 1) | (g >> 1);
  unsigned ka[4];
#pragma unroll
  for (int st = 0; st < 4; ++st) ka[st] = lds0 + kslot(0, c.hs) + l15 * 256 + (((4 * st + pg) ^ l15) * 16);
  unsigned va = lds0 + vslot(0, c.hs) + (g >> 1) * 2048 + (4 * (g & 1) + (l15 >> 2)) * 32 + (lane & 3) * 8;
  auto rks = [&](int sl, int kt, int st) { return ld128(ka[st] + sl * 8192 + kt * 4096); };
  auto rvs = [&](int sl, int db) { const unsigned a = va + sl * 8192 + (db >> 2) * 1024 + (db & 3) * 256; return cat(ldtr(a), ldtr(a + 4096)); };
  {
    bf16x8 kf0[4], kf1[4];
#pragma unroll
    for (int st = 0; st < 4; ++st) { kf0[st] = rks(0, 0, st); kf1[st] = rks(0, 1, st); }
    const unsigned wb = lds0 + c.j * 2048 + lane * 16;
    if constexpr (NB == 3) {
#pragma unroll
      for (int q = 0; q < 8; ++q) st128(wb + vslot(3 + (q >> 2), (q >> 1) & 1) + (q & 1) * 1024, kvr[q]);
      SB();
      qconv();
    }
#pragma unroll
    for (int st = 0; st < 4; ++st)
#pragma unroll
      for (int nb = 0; nb < NB; ++nb) {
        s.S0[nb] = mfma(kf0[st], s.qf[nb][st], st == 0 ? z4 : s.S0[nb]); s.S1[nb] = mfma(kf1[st], s.qf[nb][st], st == 0 ? z4 : s.S1[nb]);
      }
#pragma unroll
    for (int nb = 0; nb < NB; ++nb) {
#pragma unroll
      for (int db = (NB == 3 ? 6 : 5); db < 8; ++db) { s.o[nb][db] = z4; pin(s.o[nb][db]); }
      const float m = xmax32(xmax16(max8(s.S0[nb], s.S1[nb]))) + BIAS;
      s.negm[nb] = z4 - m; s.S0[nb] -= m; s.S1[nb] -= m;
      pin(s.negm[nb]);
    }
    s.ones = asb(u32x4{0x3f803f80u, 0x3f803f80u, 0x3f803f80u, 0x3f803f80u});
    pin(s.ones);
  }
  exps<NB, 0, 4 * NB>(s.S0); exps<NB, 0, 2 * NB>(s.S1);
  bf16x8 v5h, v6, v7;
  SB();
  auto us = [&](auto m, auto g0, auto g1, auto g2) {
    m(0); SB(); g0(); SB(); m(1); SB(); g1(); SB();
    if constexpr (NB == 3) { m(2); SB(); g2(); SB(); }
  };
  auto QK0 = [&](int st, bf16x8 fr) { return [&s, st, fr](int nb) { if (st == 0) amfma_c(s.S0[nb], fr, s.qf[nb][0], s.negm[nb]); else amfma(s.S0[nb], fr, s.qf[nb][st]); }; };
  auto QK1 = [&](int st, bf16x8 fr) { return [&s, st, fr](int nb) { if (st == 0) amfma_c(s.S1[nb], fr, s.qf[nb][0], s.negm[nb]); else amfma(s.S1[nb], fr, s.qf[nb][st]); }; };
  auto PVH = [&](int db, bf16x8& fr) { return [&s, db, &fr](int nb) { amfma(s.o[nb][db], fr, asb(s.pf[nb])); }; };
  auto N = [] {};
#define E0(e) [&] { exp1<NB, (e)>(s.S0); }
#define E1(e) [&] { exp1<NB, (e)>(s.S1); }
#define CLO(nb) [&] { if constexpr ((nb) < NB) { s.pf[nb][0] = acvt(s.S0[nb][0], s.S0[nb][1]); s.pf[nb][1] = acvt(s.S0[nb][2], s.S0[nb][3]); } }
#define CHI(nb) [&] { if constexpr ((nb) < NB) { s.pf[nb][2] = acvt(s.S1[nb][0], s.S1[nb][1]); s.pf[nb][3] = acvt(s.S1[nb][2], s.S1[nb][3]); } }
#define U(f, ...) [&] { use(f); SB(); (__VA_ARGS__)(); }
  unsigned dk = 8192, dv = 8192, cnt = 1;
  if constexpr (NB == 3) {
#pragma unroll
    for (int st = 0; st < 4; ++st) ka[st] += 8192;
  }
  auto astep = [&](auto FI, auto SL, int t, auto kvh) {
    constexpr bool FIRST = decltype(FI)::value;
    constexpr int sl = decltype(SL)::value;
    auto rk = [&](int kt, int st) { return rks(NB == 3 ? 0 : sl, kt, st); };
    auto rv = [&](int db) { return rvs(NB == 3 ? 0 : (sl + 4) % 5, db); };
    if constexpr (NB == 2) wait_vm_bar<6>(); else wait_vm_bar<63>();
    auto PH = [&](int db, bf16x8& fr) { return [&, db](int nb) { if constexpr (!FIRST) amfma(s.o[nb][db], fr, asb(s.pf[nb])); }; };
    auto PV = [&](int db, bf16x8 fr) { return [&s, db, fr](int nb) { amf<FIRST>(s.o[nb][db], fr, asb(s.pf[nb])); }; };
    auto SUM = [&](int nb) { amf<FIRST>(s.l[nb], s.ones, asb(s.pf[nb])); };
    bf16x8 a0, a1, a2, a3, b0, b1, b2, b3, v0, v1, v2, v3, v4;
    unsigned o1, o2;
    if constexpr (NB == 3) {
      bf16x8 v5;
      us(PH(6, v6), [&] { a0 = rk(0, 0); a1 = rk(0, 1); }, [&] { a2 = rk(0, 2); a3 = rk(0, 3); }, E1(6));
      us(PH(7, v7), CLO(0), CLO(1), U(a0, CLO(2)));
      us(QK0(0, a0), [&] { b0 = rk(1, 0); b1 = rk(1, 1); }, E1(7), U(a1, E1(8)));
      us(QK0(1, a1), E1(9), E1(10), U(a2, E1(11)));
      us(QK0(2, a2), [&] { o1 = aor3(s.pf[0][0], s.pf[0][1], s.pf[1][0]); o2 = aor3(s.pf[1][1], s.pf[2][0], s.pf[2][1]); }, CHI(0), U(a3, CHI(1)));
      us(QK0(3, a3), CHI(2), [&] { o1 = aor3(o1, s.pf[0][2], s.pf[0][3]); o2 = aor3(o2, s.pf[1][2], s.pf[1][3]); },
                     U(b0, [&] { o1 = aor3(o1, s.pf[2][2], s.pf[2][3]); s.orv = o1 | o2; }));
      if constexpr (!FIRST) check<NB, true>(s, c, t - 1);
      SB();
      us(QK1(0, b0), [&] { b2 = rk(1, 2); b3 = rk(1, 3); }, [&] { v0 = rv(0); }, U(b1, [&] { aaddu(ka[0], dk); aaddu(ka[1], dk); }));
      us(QK1(1, b1), [&] { v1 = rv(1); }, E0(0), U(b2, E0(1)));
      us(QK1(2, b2), [&] { v2 = rv(2); }, E0(2), U(b3, E0(3)));
      us(QK1(3, b3), [&] { v3 = rv(3); }, E0(4), E0(5));
      us(SUM, E0(6), E0(7), U(v0, E0(8)));
      us(PV(0, v0), [&] { v4 = rv(4); }, E0(9), U(v1, E0(10)));
      us(PV(1, v1), [&] { v5 = rv(5); }, E0(11), U(v2, E1(0)));
      us(PV(2, v2), [&] { v6 = rv(6); }, E1(1), U(v3, E1(2)));
      us(PV(3, v3), [&] { v7 = rv(7); }, E1(3), U(v4, E1(4)));
      us(PV(4, v4), E1(5), [&] { aaddu(ka[2], dk); aaddu(ka[3], dk); }, U(v5, [&] { aaddu(va, dv); }));
      us(PV(5, v5), [&] { dv = dk; cnt = cnt == 4 ? 0 : cnt + 1; pins(cnt); }, [&] { use(v7); dk = cnt == 4 ? -32768 : 8192; pins(dk); }, N);
    } else {
      us(PH(5, v5h), [&] { a0 = rk(0, 0); a1 = rk(0, 1); }, [&] { a2 = rk(0, 2); a3 = rk(0, 3); }, N);
      us(PH(6, v6), E1(4), [&] { kvh(0); }, N);
      us(PH(7, v7), CLO(0), U(a0, CLO(1)), N);
      us(QK0(0, a0), [&] { b0 = rk(1, 0); b1 = rk(1, 1); }, U(a1, E1(5)), N);
      us(QK0(1, a1), E1(6), U(a2, E1(7)), N);
      us(QK0(2, a2), [&] { o1 = aor3(s.pf[0][0], s.pf[0][1], s.pf[1][0]); kvh(1); }, U(a3, CHI(0)), N);
      us(QK0(3, a3), CHI(1), U(b0, [&] { o1 = aor3(o1, s.pf[1][1], s.pf[0][2]); o2 = aor3(s.pf[0][3], s.pf[1][2], s.pf[1][3]); s.orv = o1 | o2; }), N);
      if constexpr (!FIRST) check<NB, true>(s, c, t - 1);
      SB();
      us(QK1(0, b0), [&] { b2 = rk(1, 2); b3 = rk(1, 3); }, U(b1, [&] { v0 = rv(0); kvh(2); }), N);
      us(QK1(1, b1), [&] { v1 = rv(1); }, U(b2, [&] { kvh(3); }), N);
      us(QK1(2, b2), [&] { v2 = rv(2); }, U(b3, E0(0)), N);
      us(QK1(3, b3), [&] { v3 = rv(3); }, [&] { exp1<NB, 1>(s.S0); kvh(4); }, N);
      us(SUM, E0(2), U(v0, E0(3)), N);
      us(PV(0, v0), [&] { v4 = rv(4); }, U(v1, [&] { exp1<NB, 4>(s.S0); kvh(5); }), N);
      us(PV(1, v1), [&] { v5h = rv(5); }, U(v2, E0(5)), N);
      us(PV(2, v2), [&] { v6 = rv(6); }, U(v3, [&] { exp1<NB, 6>(s.S0); kvh(6); }), N);
      us(PV(3, v3), [&] { v7 = rv(7); exp1<NB, 7>(s.S0); }, U(v4, [&] { exp1<NB, 0>(s.S1); kvh(7); }), N);
      us(PV(4, v4), [&] { exp1<NB, 1>(s.S1); exp1<NB, 2>(s.S1); }, [&] { exp1<NB, 3>(s.S1); use(v7); }, N);
    }
  };
  STAMP(3);
  astep(std::true_type{}, IC<1>{}, 1, [&](int n) { if constexpr (NB == 2) {
    if (n < 4) dma_kv(false, jb + kslot(0, 0), jb + kslot(0, 1), tso(5), n);
#pragma unroll
    for (int i = 0; i < 20; ++i) if (i * 8 / 20 == n) { const int m = i >> 2, q = i & 3;
      st128(jb + lane * 16 + ((m & 1) ? vslot(1 + (m >> 1), q >> 1) : kslot(2 + (m >> 1), q >> 1)) + (q & 1) * 1024, kvr[i]); } } });
  constexpr std::false_type F{};
  if constexpr (NB == 3) {
    auto NH = [](int) {};
#pragma unroll 1
    for (int t = 2; t < 16; ++t) astep(F, IC<0>{}, t, NH);
  } else {
    static_for(std::make_integer_sequence<int, 12>{}, [&](auto TT) {
      constexpr int t = decltype(TT)::value + 2;
      astep(F, IC<t % 5>{}, t, [&](int n) {
        if (n < 7) {
          const int p = 7 * (t - 2) + n, un = p >> 1, u = un + (un >= 40 ? 2 : 0), h = u & 1, v = (u >> 1) & 1, tn = 6 + (u >> 2) - v;
          const unsigned so = tso(tn) + h * 131072;
          if (p & 1) { wait_vm<THR>(); dmao2<1, 1024>(v ? voffV[1] : voffK[1], v ? c.rv : c.rk, so); }
          else { setm0(jb + (v ? vslot(tn % 5, h) : kslot(tn % 5, h))); wait_vm<THR>(); dmao2<1, 0>(v ? voffV[0] : voffK[0], v ? c.rv : c.rk, so); }
        }
      });
    });
  }
  rsrc_t rof, rwo, rslab; int pr, hg, rotb; gu64* sync; u16* ob;
  {
    int id = c.id; asm volatile("" : "+s"(id));
    const int br = (id >> 2) & 1, b = id >> 5;
    hg = id & 3; pr = (id >> 3) & 3; rotb = b & 3;
    rof = mkr(c.ws + (size_t)((br * 8 + b) * 4 + hg) * 327680, 327680);
    rwo = mkr((const char*)(c.wo + (size_t)hg * 1024 * 640 + pr * 160) - 4096, 1024 * 1280 - pr * 320 + 4096);
    rslab = mkr(c.ws + WS_SLAB + (size_t)((br * 8 + b) * 4 + pr) * (12 * QB), 12 * QB);
    sync = (gu64*)(c.ws + WS_SYNC);
    ob = c.out + (size_t)(b * 160 + (hg & 1) * 80) * 1280 + br * 640 + pr * 160 + (hg >> 1) * 80;
  }
  unsigned lz = 0;
  const int dks = c.j >> 1, dh = c.j & 1;
  auto stg = [](int u) { return u * 40960; };
  auto kpair = [&](int i) { return ((pr + (i >> 2)) & 3) * 4 + ((i + rotb) & 3); };
  auto wvo = [&](int n) { const int e = 64 * n + (int)lz, row = e / 20, ch = e - row * 20; return row * 1280 + ((ch ^ (((row >> 2) & 1) << 1)) * 16) + 4096; };
  auto wso = [&](int i) { return (kpair(i) * 64 + dks * 32 + dh * 16) * 1280; };
  auto wld = [&](int i, int n) { return lds0 + stg(i & 3) + 20480 + dks * 10240 + dh * 5120 + n * 1024; };
  u32x4 wr[15];
  if constexpr (NB == 2) {
    lz = laneid();
    unsigned vw[5];
#pragma unroll
    for (int n = 0; n < 5; ++n) vw[n] = wvo(n);
    auto wl = [&](int p) { wait_vm<4>(); if (p < 15) wr[p] = __builtin_amdgcn_raw_buffer_load_b128(rwo, vw[p % 5], wso(p / 5), WONT ? 2 : 0); };
    SB();
    astep(F, IC<4>{}, 14, [&](int n) { wl(n); });
    astep(F, IC<0>{}, 15, [&](int n) { wl(8 + n); });
  }
  STAMP(4);
  if constexpr (NB == 3) wait_vm_bar<63>(); else wait_vm_bar<15>();
  {
    bf16x8 vf[8];
#pragma unroll
    for (int db = 0; db < 4; ++db) vf[db] = rvs(0, db);
    SB();
    if constexpr (NB == 2) us(PVH(5, v5h), N, N, N);
    us(PVH(6, v6), N, N, N);
    exps<NB, 2 * NB, 4 * NB>(s.S1);
    us(PVH(7, v7), CLO(0), CLO(1), CLO(2));
    CHI(0)(); CHI(1)(); CHI(2)();
    s.orv = 0;
#pragma unroll
    for (int nb = 0; nb < NB; ++nb) { s.orv = aor3(s.orv, s.pf[nb][0], s.pf[nb][1]); s.orv = aor3(s.orv, s.pf[nb][2], s.pf[nb][3]); }
    SB();
    check<NB, false>(s, c, 15);
    SB();
    if constexpr (NB == 3) lz = laneid();
#pragma unroll
    for (int db = 4; db < 8; ++db) vf[db] = rvs(0, db);
#pragma unroll
    for (int nb = 0; nb < NB; ++nb) s.l[nb] = mfma(s.ones, asb(s.pf[nb]), s.l[nb]);
#pragma unroll
    for (int db = 0; db < 8; ++db)
#pragma unroll
      for (int nb = 0; nb < NB; ++nb) {
        s.o[nb][db] = mfma(vf[db], asb(s.pf[nb]), s.o[nb][db]);
        if constexpr (NB == 2) { const int p = db * 2 + nb; if (p < 15) { SB(); st128(wld(p / 5, p % 5) + lz * 16, wr[p]); SB(); } }
      }
  }
  STAMP(5);
  u32x4 ow[NB][4];
  const unsigned gb = lz * 16 + (pr * 8 + c.hs * 4) * 10240 + c.blk0 * 1024;
  auto ost = [&](int e, int nb) { __builtin_amdgcn_raw_buffer_store_b128(ow[nb][e], rof, gb + e * 10240 + nb * 1024, 0, 16); };
#pragma unroll
  for (int e = 0; e < 4; ++e)
#pragma unroll
    for (int nb = 0; nb < NB; ++nb) {
      const float inv = __builtin_amdgcn_rcpf(s.l[nb][0]);
      const f32x4 a0 = s.o[nb][2 * e] * inv, a1 = s.o[nb][2 * e + 1] * inv;
      ow[nb][e] = u32x4{cvtpk(a0[0], a0[1]), cvtpk(a0[2], a0[3]), cvtpk(a1[0], a1[1]), cvtpk(a1[2], a1[3])};
      if (e < EPRE) ost(e, nb);
    }
  asm volatile("s_waitcnt lgkmcnt(0)\n\ts_barrier" ::: "memory");
#pragma unroll
  for (int eh = 0; eh < 2; ++eh) {
    const unsigned lb = lds0 + stg((c.hs * 2 + eh - rotb) & 3) + c.blk0 * 1024 + lz * 16;
#pragma unroll
    for (int el = 0; el < 2; ++el)
#pragma unroll
      for (int nb = 0; nb < NB; ++nb) st128(lb + el * 10240 + nb * 1024, ow[nb][eh * 2 + el]);
  }
  STAMP(6);
  {
    constexpr int NM = NB, M0 = NM == 3 ? 0 : 3;
    const int q = c.j, qm = q & 1, qn = q >> 1, zg = lz >> 4;
    unsigned vo3[5];
#pragma unroll
    for (int n = 0; n < 5; ++n) vo3[n] = NB == 3 ? wvo(n) - (n & 3) * 1024 : lz * 16 + (n >> 2) * 4096;
    const unsigned lb3 = lds0 + dks * 10240 + dh * 5120 + (NB == 3 ? 20480 : 0);
    auto so3 = [&](int i) { unsigned v = (NB == 3 ? wso(i) : (kpair(i) * 2 + dks) * 10240 + dh * 5120) + (unsigned)(i >> 4) * OOB; pins(v); return v; };
    auto dmal = [&](unsigned so, int u, int n) {
      constexpr int POL = NB == 3 ? (WONT ? 1 : 0) : 2;
      const rsrc_t r = NB == 3 ? rwo : rof;
      const unsigned vo = vo3[NB == 3 ? n : (n >> 2) * 4];
      if (n == 0) { setm0(lb3 + stg(u)); dmao<POL, 0>(vo, r, so); }
      else if (n == 1) dmao2<POL, 1024>(vo, r, so);
      else if (n == 2) dmao2<POL, 2048>(vo, r, so);
      else if (n == 3) dmao2<POL, 3072>(vo, r, so);
      else { setm0(lb3 + stg(u) + 4096); dmao<POL, 0>(vo, r, so); }
    };
    if constexpr (NB == 3) {
      const unsigned so = so3(3);
#pragma unroll
      for (int n = 0; n < 5; ++n) dmal(so, 3, n);
    }
    unsigned oa[2], wa[2][2];
    {
      oa[0] = lds0 + (qm * 5 + M0) * 1024 + lz * 16; oa[1] = oa[0] + 81920;
      const unsigned wb = lds0 + 20480 + (4 * zg + ((lz & 15) >> 2)) * 320 + (lz & 3) * 8 + qn * 160;
      const int sg = (qn ? -32 : 32) * (zg & 1);
      wa[0][0] = wb + sg; wa[0][1] = wb - sg; wa[1][0] = wa[0][0] + 81920; wa[1][1] = wa[0][1] + 81920;
      pin(oa[1]); pin(wa[1][0]); pin(wa[1][1]);
    }
    f32x4 acc[NM][5];
    bf16x8 h2, h3, h4, op[NM];
    auto interval = [&](auto UU, auto D, auto tail) {
      constexpr bool FIRST = decltype(UU)::value < 0;
      constexpr int u = FIRST ? 0 : decltype(UU)::value, so = (u & 1) * 40960;
      auto ro = [&](int ks, int m) { return ld128(oa[u >> 1] + so + ks * 10240 + m * 1024); };
      auto rw = [&](int ks, int n) { const unsigned a = wa[u >> 1][n & 1] + so + ks * 10240 + n * 32; return cat(ldtr(a), ldtr(a + 5120)); };
      auto mm = [&](auto Z, int n, bf16x8& wf, bf16x8 (&of)[NM], auto g0, auto g1, auto g2) {
        constexpr int z = decltype(Z)::value;
        if constexpr (z == 2) { g0(); g1(); g2(); SB(); }
        else {
          amf<z == 1>(acc[0][n], wf, of[0]); SB(); g0(); SB(); amf<z == 1>(acc[1][n], wf, of[1]); SB(); g1(); SB();
          if constexpr (NM == 3) { amf<z == 1>(acc[2][n], wf, of[2]); SB(); }
          g2(); SB();
        }
      };
      constexpr IC<FIRST ? 2 : 0> H{};
      constexpr IC<FIRST ? 1 : 0> C{};
      constexpr IC<0> P{};
      bf16x8 o0[NM], o1[NM], a0, a1, a2, a3, a4, b0, b1;
      mm(H, 2, h2, op, [&] { a0 = rw(0, 0); }, [&] { o0[0] = ro(0, 0); o0[1] = ro(0, 1); }, [&] { if constexpr (NM == 3) o0[2] = ro(0, 2); });
      mm(H, 3, h3, op, [&] { a1 = rw(0, 1); }, [&] { a2 = rw(0, 2); }, [&] { D(0); });
      mm(H, 4, h4, op, [&] { a3 = rw(0, 3); }, [&] { a4 = rw(0, 4); }, [&] { use(o0[NM - 1]); });
      mm(C, 0, a0, o0, [&] { o1[0] = ro(1, 0); o1[1] = ro(1, 1); }, [&] { use(a1); if constexpr (NM == 3) o1[2] = ro(1, 2); }, [&] { D(1); });
      mm(C, 1, a1, o0, [&] { b0 = rw(1, 0); }, [&] { use(a2); b1 = rw(1, 1); }, N0);
      mm(C, 2, a2, o0, [&] { h2 = rw(1, 2); }, [&] { use(a3); }, [&] { D(2); });
      mm(C, 3, a3, o0, [&] { h3 = rw(1, 3); }, [&] { use(a4); }, N0);
      mm(C, 4, a4, o0, [&] { h4 = rw(1, 4); }, [&] { use(b0); }, [&] { D(3); });
      mm(P, 0, b0, o1, N0, [&] { use(b1); }, [&] { D(4); });
      mm(P, 1, b1, o1, [&] { use(h4); }, N0, N0);
      tail();
#pragma unroll
      for (int m = 0; m < NM; ++m) op[m] = o1[m];
    };
    auto ND = [](int) {};
    SB();
    auto poll = [&](int base) {
      const int pid = c.id ^ (((lz & 3) ? (lz & 3) : 1) << 3);
      for (unsigned spins = 0; spins < (1u << 14); ++spins) {
        if (__all(__hip_atomic_load(sync + (base + pid) * FS, RLX_AGENT) == tag)) break;
        __builtin_amdgcn_s_sleep(1);
      }
    };
    wait_vm_bar<63>();
    interval(IC<-1>{}, [&](int n) {
#pragma unroll
      for (int k = 0; k < (4 - EPRE) * NB; ++k) if (k * 5 / ((4 - EPRE) * NB) == n) ost(EPRE + k / NB, k % NB); }, N0);
    wait_vm_bar<63>();
    STAMP(7);
    { const unsigned so = so3(4); interval(IC<1>{}, [&](int n) { if constexpr (NB == 3) dmal(so, 0, n); }, N0); }
    if constexpr (NB == 3) wait_vm_bar<5>(); else wait_vm_bar<0>();
    if (c.w == 3 && lz == 0) __hip_atomic_store(sync + (1 + c.id) * FS, tag, RLX_AGENT);
    const unsigned so5 = so3(5);
    interval(IC<2>{}, [&](int n) { if constexpr (NB == 3) dmal(so5, 1, n); }, [&] { if (c.w == 2) poll(1); });
    if constexpr (NB == 3) wait_vm_bar<10>(); else wait_vm_bar<63>();
    STAMP(8);
    { const unsigned sa = so3(NB == 3 ? 6 : 4);
      interval(IC<3>{}, [&](int n) { if constexpr (NB == 3) dmal(sa, 2, n);
        else { if (n == 0) { dmal(sa, 0, 0); dmal(sa, 0, 1); } else if (n == 1) { dmal(sa, 0, 2); dmal(sa, 0, 3); } else if (n == 2) { dmal(sa, 0, 4); dmal(so5, 1, 0); }
               else if (n == 3) { dmal(so5, 1, 1); dmal(so5, 1, 2); } else { dmal(so5, 1, 3); dmal(so5, 1, 4); } } }, N0); }
    STAMP(9);
    unsigned son = so3(NB == 3 ? 7 : 6);
#pragma unroll 1
    for (int i4 = 4; i4 < 16; i4 += 4)
      static_for(std::make_integer_sequence<int, 4>{}, [&](auto UU) {
        constexpr int u = decltype(UU)::value;
        if constexpr (NB == 3) wait_vm_bar<10>(); else wait_vm_bar<5>();
        interval(UU, [&](int n) { dmal(son, (u + (NB == 3 ? 3 : 2)) & 3, n); if (n == 4) son = so3(i4 + u + (NB == 3 ? 4 : 3)); }, N0);
      });
#pragma unroll
    for (int m = 0; m < NM; ++m) { amfma(acc[m][2], h2, op[m]); amfma(acc[m][3], h3, op[m]); amfma(acc[m][4], h4, op[m]); }
    asm volatile("s_nop 7");
    SB();
    STAMP(10);
    if (q != hg) {
      const int so = (q * 3 + ((hg - q - 1) & 3)) * QB;
#pragma unroll
      for (int e = 0; e < NM * 5; e += 2) {
        const f32x4 a = acc[e / 5][e % 5], b = e + 1 < NM * 5 ? acc[(e + 1) / 5][(e + 1) % 5] : z4;
        __builtin_amdgcn_raw_buffer_store_b128(u32x4{cvtpk(a[0], a[1]), cvtpk(a[2], a[3]), cvtpk(b[0], b[1]), cvtpk(b[2], b[3])}, rslab, lz * 16 + ((M0 ? 8 : 0) + e / 2) * 1024 + so, 0, 16);
      }
    }
    asm volatile("s_waitcnt vmcnt(0) lgkmcnt(0)\n\ts_barrier" ::: "memory");
    if (c.w == 0 && lz == 0) __hip_atomic_store(sync + (257 + c.id) * FS, tag, RLX_AGENT);
    STAMP(11);
    if (q == hg) {
#pragma unroll
      for (int e = 0; e < NM * 5; ++e) st128(lds0 + lz * 16 + (M0 * 5 + e) * 1024, __builtin_bit_cast(u32x4, acc[e / 5][e % 5]));
    }
    if (c.w == ((hg + 1) & 3)) {
      const int pid = c.id ^ ((lz & 3) ? (lz & 3) : 1);
      for (unsigned spins = 0; spins < (1u << 14); ++spins) {
        if (__all(__hip_atomic_load(sync + (257 + pid) * FS, RLX_AGENT) == tag)) break;
        __builtin_amdgcn_s_sleep(1);
      }
    }
    if (c.id == 0 && c.w == 7) {
      for (unsigned spins = 0; spins < (1u << 14); ++spins) {
        bool ok = true;
#pragma unroll
        for (int k = 0; k < 4; ++k) ok &= __hip_atomic_load(sync + (1 + lz + 64 * k) * FS, RLX_AGENT) == tag;
        if (__all(ok)) break;
        __builtin_amdgcn_s_sleep(1);
      }
      if (lz == 0) __hip_atomic_store(sync, tag, RLX_AGENT);
    }
    asm volatile("s_waitcnt vmcnt(0) lgkmcnt(0)\n\ts_barrier" ::: "memory");
    STAMP(12);
    u16* op16 = ob + (size_t)(lz & 15) * 1280 + 4 * zg;
    u32x4 r[2][3];
#pragma unroll
    for (int k = 0; k < 2; ++k)
#pragma unroll
      for (int sl = 0; sl < 3; ++sl) r[k][sl] = __builtin_amdgcn_raw_buffer_load_b128(rslab, c.w + 8 * k < 13 ? lz * 16 + (c.w + 8 * k) * 1024 : OOB, (hg * 3 + sl) * QB, 16);
#pragma unroll
    for (int k = 0; k < 2; ++k) {
      const int un = c.w + 8 * k;
#pragma unroll
      for (int hh = 0; hh < 2; ++hh) {
        const int e = un < 8 ? 2 * un + hh : 15 + 2 * (un - 8) + hh;
        if (un < 13 && !(un == 7 && hh == 1)) {
          f32x4 v = *(const LDS3 f32x4*)(size_t)(lds0 + lz * 16 + e * 1024);
#pragma unroll
          for (int sl = 0; sl < 3; ++sl) { const unsigned lo = r[k][sl][2 * hh], hi = r[k][sl][2 * hh + 1];
            v += f32x4{__uint_as_float(lo << 16), __uint_as_float(lo & 0xffff0000u), __uint_as_float(hi << 16), __uint_as_float(hi & 0xffff0000u)}; }
          const int m = e / 5, n = e - m * 5;
          *(u32x2*)(op16 + m * (16 * 1280) + n * 16) = u32x2{cvtpk(v[0], v[1]), cvtpk(v[2], v[3])};
        }
      }
    }
  }
}

__global__ __launch_bounds__(512, 2) void fused(const u16* xA, const u16* kA, const u16* vA, const u16* wqA, const u16* woA,
                                                 const u16* xB, const u16* kB, const u16* vB, const u16* wqB, const u16* woB, u16* out, char* ws) {
  extern __shared__ __attribute__((aligned(16))) char smem[];
  const int id = blockIdx.x, xg = id & 7, cc = id >> 3, br = xg >> 2, hg = xg & 3, b = cc >> 2, pr = cc & 3, h0 = hg * 8 + pr * 2;
  const int w = __builtin_amdgcn_readfirstlane(threadIdx.x >> 6);
  asm volatile("s_nop 4");
  Ctx c;
  { unsigned long long pc; asm volatile("s_getpc_b64 %0" : "=s"(pc)); c.code = pc & ~255ull; }
  const u16* x = br ? xB : xA; const u16* k = br ? kB : kA; const u16* v = br ? vB : vA; const u16* wq = br ? wqB : wqA;
  c.rx = mkr(x + (size_t)b * SQ * DM, SQ * DM * 2);
  c.rw = mkr(wq + h0 * DH, DM * NH * DH * 2 - h0 * DH * 2);
  c.rk = mkr(k + (size_t)(b * NH + h0) * SK * DH, 2 * SK * DH * 2);
  c.rv = mkr((const char*)(v + (size_t)(b * NH + h0) * SK * DH) - 1024, 2 * SK * DH * 2 + 1024);
  c.lds0 = (unsigned)(size_t)(LDS3 char*)smem;
  c.hs = w >> 2; c.j = w & 3; c.rot = (id * 5) & 15; c.krot = ((10 * pr + 5 * b) >> 2) % 10;
  c.w = w; c.id = id; c.wo = br ? woB : woA; c.out = out; c.ws = ws;
  const bool big = (c.j < 2) == (c.hs == 0);
  c.blk0 = (0x74208630u >> (4 * w)) & 15;
  if (big) body<3>(c); else body<2>(c);
}

extern "C" void kernel_launch(void* const* d_in, const int* in_sizes, int n_in, void* d_out, int out_size, void* d_ws, size_t ws_size, hipStream_t stream) {
  (void)hipFuncSetAttribute((const void*)fused, hipFuncAttributeMaxDynamicSharedMemorySize, LDS_BYTES);
  fused<<<256, 512, LDS_BYTES, stream>>>((const u16*)d_in[0], (const u16*)d_in[1], (const u16*)d_in[2], (const u16*)d_in[3], (const u16*)d_in[4],
                                         (const u16*)d_in[5], (const u16*)d_in[6], (const u16*)d_in[7], (const u16*)d_in[8], (const u16*)d_in[9], (u16*)d_out, (char*)d_ws);
}
```

```cpp
#include <hip/hip_runtime.h>
#include <hip/hip_bf16.h>
#include <type_traits>

typedef __bf16 bf16x8 __attribute__((ext_vector_type(8)));
typedef __bf16 bf16x4 __attribute__((ext_vector_type(4)));
typedef __bf16 bf16x2 __attribute__((ext_vector_type(2)));
typedef float f32x4 __attribute__((ext_vector_type(4)));
typedef float f32x2 __attribute__((ext_vector_type(2)));
typedef unsigned u32x4 __attribute__((ext_vector_type(4)));
typedef unsigned u32x2 __attribute__((ext_vector_type(2)));
typedef unsigned short u16;
typedef __attribute__((address_space(1))) unsigned long long gu64;
typedef __amdgpu_buffer_rsrc_t rsrc_t;
#define LDS3 __attribute__((address_space(3)))
#define DI __device__ __forceinline__
#define SB() __builtin_amdgcn_sched_barrier(0)
#define RLX_AGENT __ATOMIC_RELAXED, __HIP_MEMORY_SCOPE_AGENT
template <int N> using IC = std::integral_constant<int, N>;

constexpr int SQ = 160, DM = 640, NH = 32, SK = 512, DH = 128;
constexpr int LDS_BYTES = 163840;
__host__ __device__ constexpr int kslot(int sl, int hs) { return hs * 40960 + sl * 8192; }
__host__ __device__ constexpr int vslot(int sl, int hs) { return 81920 + hs * 40960 + sl * 8192; }
__host__ __device__ constexpr int xoff(int b) { return 16384 + b * 40960; }
__host__ __device__ constexpr int woff(int b) { return 90112 + (b & 1) * 16384 + (b >> 1) * 40960; }
constexpr float QSCALE = 0.08838834764831845f * 1.4426950408889634f;
constexpr float BIAS = 10.f;
constexpr unsigned OOB = 0x7ff00000u;
constexpr size_t WS_SLAB = 32u << 20, WS_SYNC = 64u << 20;
constexpr int FS = 32;
constexpr int QB = 13 * 1024;
constexpr int KVN0 = 1;
constexpr int NG = 8;
constexpr int PP2 = 15;
constexpr int THR = 3;
constexpr int EPRE = 2;
constexpr unsigned GOVQ = 55, GOVA = 110;
constexpr unsigned SPINS = 1u << 17;
constexpr int CODE_KB = 100;

DI unsigned cvtpk(float lo, float hi) { f32x2 v = {lo, hi}; bf16x2 b = __builtin_convertvector(v, bf16x2); return __builtin_bit_cast(unsigned, b); }
DI f32x4 mfma(bf16x8 a, bf16x8 b, f32x4 c) { return __builtin_amdgcn_mfma_f32_16x16x32_bf16(a, b, c, 0, 0, 0); }
DI bf16x8 ld128(unsigned addr) { return *(const LDS3 bf16x8*)(size_t)addr; }
DI void st128(unsigned addr, u32x4 v) { *(LDS3 u32x4*)(size_t)addr = v; }
DI bf16x4 ldtr(unsigned addr) { return __builtin_amdgcn_ds_read_tr16_b64_v4bf16((LDS3 bf16x4*)(size_t)addr); }
DI bf16x8 cat(bf16x4 a, bf16x4 b) { return __builtin_shufflevector(a, b, 0, 1, 2, 3, 4, 5, 6, 7); }
DI bf16x8 asb(u32x4 w) { return __builtin_bit_cast(bf16x8, w); }
DI rsrc_t mkr(const void* p, int bytes) { return __builtin_amdgcn_make_buffer_rsrc((void*)p, (short)0, bytes, 0x00020000); }
template <bool NT> DI void dma16(unsigned ldsaddr, unsigned voff, rsrc_t r, unsigned soff) {
  if constexpr (NT) asm volatile("s_mov_b32 m0, %0\n\ts_nop 0\n\tbuffer_load_dwordx4 %1, %2, %3 offen nt lds" :: "s"(ldsaddr), "v"(voff), "s"(r), "s"(soff) : "memory");
  else              asm volatile("s_mov_b32 m0, %0\n\ts_nop 0\n\tbuffer_load_dwordx4 %1, %2, %3 offen lds" :: "s"(ldsaddr), "v"(voff), "s"(r), "s"(soff) : "memory");
}
DI void setm0(unsigned v) { asm volatile("s_mov_b32 m0, %0" :: "s"(v) : "memory"); }
template <int POL, int OFF> DI void dmao(unsigned voff, rsrc_t r, unsigned soff) {
  if constexpr (POL == 1)      asm volatile("s_nop 0\n\tbuffer_load_dwordx4 %0, %1, %2 offen offset:%3 nt lds" :: "v"(voff), "s"(r), "s"(soff), "i"(OFF) : "memory");
  else if constexpr (POL == 2) asm volatile("s_nop 0\n\tbuffer_load_dwordx4 %0, %1, %2 offen offset:%3 sc1 lds" :: "v"(voff), "s"(r), "s"(soff), "i"(OFF) : "memory");
  else                         asm volatile("s_nop 0\n\tbuffer_load_dwordx4 %0, %1, %2 offen offset:%3 lds" :: "v"(voff), "s"(r), "s"(soff), "i"(OFF) : "memory");
}
template <int POL, int OFF> DI void dmao2(unsigned voff, rsrc_t r, unsigned soff) {
  if constexpr (POL == 1)      asm volatile("buffer_load_dwordx4 %0, %1, %2 offen offset:%3 nt lds" :: "v"(voff), "s"(r), "s"(soff), "i"(OFF) : "memory");
  else if constexpr (POL == 2) asm volatile("buffer_load_dwordx4 %0, %1, %2 offen offset:%3 sc1 lds" :: "v"(voff), "s"(r), "s"(soff), "i"(OFF) : "memory");
  else                         asm volatile("buffer_load_dwordx4 %0, %1, %2 offen offset:%3 lds" :: "v"(voff), "s"(r), "s"(soff), "i"(OFF) : "memory");
}
DI void pins(unsigned& v) { v = __builtin_amdgcn_readfirstlane(v); asm volatile("" : "+s"(v)); }
template <int N> DI void wait_vm() { asm volatile("s_waitcnt vmcnt(%0)" :: "i"(N) : "memory"); }
template <int N> DI void wait_vm_bar() { asm volatile("s_waitcnt vmcnt(%0) lgkmcnt(0)\n\ts_barrier" :: "i"(N) : "memory"); }
DI float xmax16(float v) { auto r = __builtin_amdgcn_permlane16_swap(__float_as_uint(v), __float_as_uint(v), false, false); return fmaxf(__uint_as_float(r[0]), __uint_as_float(r[1])); }
DI float xmax32(float v) { auto r = __builtin_amdgcn_permlane32_swap(__float_as_uint(v), __float_as_uint(v), false, false); return fmaxf(__uint_as_float(r[0]), __uint_as_float(r[1])); }
DI void amfma(f32x4& acc, bf16x8 a, bf16x8 b) { asm volatile("v_mfma_f32_16x16x32_bf16 %0, %1, %2, %0" : "+v"(acc) : "v"(a), "v"(b)); }
DI void amfma_c(f32x4& d, bf16x8 a, bf16x8 b, f32x4 cc) { asm volatile("v_mfma_f32_16x16x32_bf16 %0, %1, %2, %3" : "=&v"(d) : "v"(a), "v"(b), "v"(cc)); }
DI void amfma_z(f32x4& d, bf16x8 a, bf16x8 b) { asm volatile("v_mfma_f32_16x16x32_bf16 %0, %1, %2, 0" : "=&v"(d) : "v"(a), "v"(b)); }
template <bool Z> DI void amf(f32x4& d, bf16x8 a, bf16x8 b) { if constexpr (Z) amfma_z(d, a, b); else amfma(d, a, b); }
DI float aexp(float x) { asm volatile("v_exp_f32 %0, %0" : "+v"(x)); return x; }
DI float amul(float a, float b) { float r; asm("v_mul_f32 %0, %1, %2" : "=v"(r) : "s"(b), "v"(a)); return r; }
DI unsigned aor3(unsigned a, unsigned b, unsigned c) { unsigned r; asm volatile("v_or3_b32 %0, %1, %2, %3" : "=v"(r) : "v"(a), "v"(b), "v"(c)); return r; }
DI unsigned acvt(float lo, float hi) { unsigned r; asm volatile("v_cvt_pk_bf16_f32 %0, %1, %2" : "=v"(r) : "v"(lo), "v"(hi)); return r; }
DI void use(bf16x8 f) { asm volatile("" :: "v"(f)); }
DI void aaddu(unsigned& a, unsigned d) { asm volatile("v_add_u32 %0, %1, %0" : "+v"(a) : "s"(d)); }
DI unsigned laneid() { unsigned l; asm volatile("v_mbcnt_lo_u32_b32 %0, -1, 0\n\tv_mbcnt_hi_u32_b32 %0, -1, %0" : "=v"(l)); return l; }
template <class T> DI void pin(T& v) { asm volatile("" : "+v"(v)); }


struct Ctx {
  rsrc_t rx, rw, rk, rv;
  unsigned lds0;
  int j, hs, blk0, rot, krot, w, id;
  const u16* wo; u16* out; char* ws; unsigned long long code;
};
template <int NB> struct St {
  f32x4 o[NB][8], negm[NB], S0[NB], S1[NB], l[NB];
  bf16x8 qf[NB][4], ones;
  u32x4 pf[NB];
  unsigned orv;
};
template <int NB, int E> DI void exp1(f32x4 (&S)[NB]) { if constexpr (E >= 0 && E < 4 * NB) S[E % NB][E / NB] = aexp(S[E % NB][E / NB]); }
template <int NB, int LO, int HI> DI void exps(f32x4 (&S)[NB]) { if constexpr (LO < HI) { exp1<NB, LO>(S); exps<NB, LO + 1, HI>(S); } }
DI float max8(f32x4 a, f32x4 b) { return fmaxf(fmaxf(fmaxf(a[0], a[1]), fmaxf(a[2], a[3])), fmaxf(fmaxf(b[0], b[1]), fmaxf(b[2], b[3]))); }

template <int NB, bool HAS_NEXT> DI void refix(St<NB>& s, rsrc_t rk, int ksoff) {
  const unsigned ln = laneid();
  const int kvo = (ln & 15) * 256 + ((((ln >> 4) & 1) << 1) | (ln >> 5)) * 16;
  asm volatile("s_nop 7");
#pragma unroll
  for (int nb = 0; nb < NB; ++nb) {
    f32x4 a = s.negm[nb], b = s.negm[nb];
#pragma unroll
    for (int st = 0; st < 4; ++st) {
      a = mfma(asb(__builtin_amdgcn_raw_buffer_load_b128(rk, kvo + st * 64, ksoff, 0)), s.qf[nb][st], a);
      SB();
      b = mfma(asb(__builtin_amdgcn_raw_buffer_load_b128(rk, kvo + st * 64, ksoff + 4096, 0)), s.qf[nb][st], b);
      SB();
    }
    const float d = fmaxf(xmax32(xmax16(max8(a, b))) + BIAS, 0.f);
    const float f = __builtin_amdgcn_exp2f(-d);
    s.negm[nb] -= d; if (HAS_NEXT) s.S0[nb] -= d;
    s.l[nb] *= f;
#pragma unroll
    for (int db = 0; db < 8; ++db) s.o[nb][db] *= f;
#pragma unroll
    for (int r = 0; r < 4; ++r) { a[r] = __builtin_amdgcn_exp2f(a[r] - d); b[r] = __builtin_amdgcn_exp2f(b[r] - d); }
    u32x4 w = {cvtpk(a[0], a[1]), cvtpk(a[2], a[3]), cvtpk(b[0], b[1]), cvtpk(b[2], b[3])};
    s.pf[nb] = w;
    SB();
  }
  asm volatile("s_nop 1");
}
template <int NB, bool HAS_NEXT> DI void check(St<NB>& s, const Ctx& c, int t) {
  if (__builtin_expect(__any((s.orv & 0x40004000u) != 0u), 0)) { asm volatile("" : "+s"(t)); refix<NB, HAS_NEXT>(s, c.rk, c.hs * 131072 + ((t + c.rot) & 15) * 8192); }
}
template <int... I, class F> DI void static_for(std::integer_sequence<int, I...>, F f) { (f(IC<I>{}), ...); }

template <int NB> DI void body(const Ctx& c) {
  const int lane = threadIdx.x & 63, l15 = lane & 15, g = lane >> 4;
  const unsigned lds0 = c.lds0;
  const f32x4 z4 = {0.f, 0.f, 0.f, 0.f};
  auto N0 = [] {};
  unsigned voffK[2], voffV[2];
#pragma unroll
  for (int h = 0; h < 2; ++h) {
    voffK[h] = (lane >> 4) * 256 + (((lane & 15) ^ (8 * (c.j & 1) + 4 * h + (lane >> 4))) * 16);
    voffV[h] = ((lane >> 1) & 7) * 256 + (lane >> 4) * 32 + (lane & 1) * 16 + (h ? 128 : 1024);
  }
  auto tso = [&](int t) { return (unsigned)(((t + c.rot) & 15) * 8192 + c.j * 2048); };
  auto dma_kv = [&](bool isv, unsigned lb0, unsigned lb1, unsigned so, int q) {
    if (q & 1) { if (isv) dmao2<1, 1024>(voffV[1], c.rv, so + (q >> 1) * 131072); else dmao2<1, 1024>(voffK[1], c.rk, so + (q >> 1) * 131072); }
    else { setm0(q ? lb1 : lb0); if (isv) dmao<1, 0>(voffV[0], c.rv, so + (q >> 1) * 131072); else dmao<1, 0>(voffK[0], c.rk, so + (q >> 1) * 131072); }
  };
  const unsigned jb = lds0 + c.j * 2048;
  auto ld_k = [&](int t, int q) { return __builtin_amdgcn_raw_buffer_load_b128(c.rk, voffK[q & 1] + (q & 1) * 1024, tso(t) + (q >> 1) * 131072, 2); };
  auto ld_v = [&](int t, int q) { return __builtin_amdgcn_raw_buffer_load_b128(c.rv, voffV[q & 1] + (q & 1) * 1024, tso(t) + (q >> 1) * 131072, 2); };
  St<NB> s;
  unsigned gdl = 0;
  auto gov = [&](unsigned ts) {
    if (c.w == 2) {
      unsigned now = (unsigned)__builtin_amdgcn_s_memrealtime();
      while (now - gdl < ts) { __builtin_amdgcn_s_sleep(1); now = (unsigned)__builtin_amdgcn_s_memrealtime(); }
      gdl = now;
    }
  };
  const unsigned long long epoch = __hip_atomic_load((gu64*)(c.ws + WS_SYNC), RLX_AGENT);
  u32x4 kvr[(NG - 3) * 4];
  {
    unsigned voffS;
    if constexpr (NB == 3) { const int f = (2 * (c.j & 1) + (lane >> 5)) | ((c.j >> 1) << 2); voffS = (lane >> 5) * 8192 + (((lane & 31) ^ (f << 1)) * 16); }
    else voffS = (lane >> 3) * 1280 + (((lane & 7) ^ (4 * (c.j & 1) + g)) * 16);
    const int wi0 = (c.j & 1) + 4 * (c.j >> 1);
    const unsigned voffS2 = voffS + 4 * 8192 - 2048;
    unsigned wso0 = (2 * c.krot * 32 + 2 * wi0) * 8192;
    auto dma_w = [&](int buf, int n) {
      if (n == 0) { setm0(lds0 + woff(buf) + wi0 * 1024); dmao<1, 0>(voffS, c.rw, wso0); }
      else if (n == 1) dmao2<1, 2048>(voffS2, c.rw, wso0);
      else if (n == 2) { setm0(lds0 + woff(buf) + wi0 * 1024 + 8192); dmao<1, 0>(voffS, c.rw, wso0 + 16 * 8192); }
      else { dmao2<1, 2048>(voffS2, c.rw, wso0 + 16 * 8192); wso0 += 32 * 8192; wso0 = wso0 >= 640u * 8192u ? wso0 - 640u * 8192u : wso0; pins(wso0); }
    };
    auto dma_x = [&](int ch, int n) {
      int kc = ch + c.krot; kc = kc >= 10 ? kc - 10 : kc;
      const int i = c.j + 4 * n;
      dma16<false>(lds0 + xoff(ch & 1) + i * 1024, voffS, c.rx, i * (8 * 1280) + kc * 128);
    };
    auto extra = [&](int m, int q) {
      if (m == 0 || m == 1) dma_kv(false, jb + kslot(m, 0), jb + kslot(m, 1), tso(m), q);
      else if (m == 2) dma_kv(true, jb + vslot(0, 0), jb + vslot(0, 1), tso(0), q);
      else if (m > 2 && m < NG) kvr[(m - 3) * 4 + q] = (m & 1) ? ld_k((m + 1) / 2, q) : ld_v((m - 2) / 2, q);
    };
    if constexpr (NB == 3) {
#pragma unroll
      for (int st = 0; st < 3; ++st)
#pragma unroll
        for (int n = 0; n < 4; ++n) dma_w(st, n);
    } else {
#pragma unroll
      for (int n = 0; n < 5; ++n) dma_x(0, n);
      {
        const rsrc_t rc = mkr((const void*)c.code, CODE_KB * 1024);
        asm volatile("s_mov_b32 m0, %0\n\ts_nop 0\n\tbuffer_load_dword %1, %2, %3 offen lds" :: "s"(lds0 + 38912), "v"(lane < 7 ? (unsigned)lane * 128u : OOB), "s"(rc), "s"(((c.id >> 3) * 4 + c.j) * 896) : "memory");
      }
      __builtin_amdgcn_s_setprio(1);
    }
    unsigned wa[8], xa[2];
    const int qq = l15 >> 2, p = lane & 3, f = qq | ((g & 1) << 2);
#pragma unroll
    for (int db = 0; db < 8; ++db) wa[db] = lds0 + woff(0) + (8 * g + qq) * 512 + c.hs * 256 + ((db ^ f) * 32) + p * 8;
#pragma unroll
    for (int u = 0; u < 2; ++u) xa[u] = lds0 + xoff(0) + (c.blk0 * 16 + l15) * 128 + (((4 * u + g) ^ ((l15 >> 1) & 7)) * 16);
    bf16x8 w4, w5, w6, w7, xp[NB];
    auto qstep = [&](auto U, auto PH, auto D) {
      constexpr int u = decltype(U)::value, ph = decltype(PH)::value;
      auto rw = [&](int db) { const unsigned a = wa[db] + (woff(u) - woff(0)); return cat(ldtr(a), ldtr(a + 2048)); };
      auto rx = [&](int nb) { return ld128(xa[u & 1] + (u >> 1) * 40960 + nb * 2048); };
      auto mm = [&](auto Z, int db, bf16x8& wf, bf16x8 (&xf)[NB], auto g0, auto g1, auto g2) {
        constexpr int z = decltype(Z)::value;
        if constexpr (z == 2) { g0(); g1(); g2(); SB(); }
        else {
          amf<z == 1>(s.o[0][db], wf, xf[0]); SB(); g0(); SB();
          amf<z == 1>(s.o[1][db], wf, xf[1]); SB(); g1(); SB();
          if constexpr (NB == 3) { amf<z == 1>(s.o[2][db], wf, xf[2]); SB(); }
          g2(); SB();
        }
      };
      constexpr IC<ph == 0 ? 2 : ph == 1 ? 1 : 0> H{};
      constexpr IC<ph == 0 ? 1 : 0> C{};
      bf16x8 xf[NB], w0, w1, w2, w3;
      if constexpr (NB == 3) {
        mm(H, 4, w4, xp, [&] { w0 = rw(0); }, [&] { xf[0] = rx(0); xf[1] = rx(1); }, [&] { xf[2] = rx(2); });
        mm(H, 5, w5, xp, [&] { w1 = rw(1); }, [&] { D(0); }, N0);
        mm(H, 6, w6, xp, [&] { w2 = rw(2); }, [&] { w3 = rw(3); }, N0);
        mm(H, 7, w7, xp, [&] { w4 = rw(4); }, [&] { D(1); }, [&] { use(xf[NB - 1]); });
        mm(C, 0, w0, xf, [&] { w5 = rw(5); }, [&] { use(w1); w6 = rw(6); }, N0);
        mm(C, 1, w1, xf, [&] { w7 = rw(7); }, [&] { use(w2); D(2); }, N0);
        mm(C, 2, w2, xf, [&] { use(w3); }, [&] { D(3); }, N0);
        mm(C, 3, w3, xf, [&] { use(w7); }, N0, N0);
      } else {
        mm(H, 4, w4, xp, [&] { w0 = rw(0); }, [&] { xf[0] = rx(0); xf[1] = rx(1); }, N0);
        mm(H, 5, w5, xp, [&] { w1 = rw(1); }, [&] { D(0); }, N0);
        mm(H, 6, w6, xp, [&] { w2 = rw(2); }, [&] { w3 = rw(3); D(1); }, N0);
        mm(H, 7, w7, xp, [&] { w4 = rw(4); }, [&] { D(2); use(xf[NB - 1]); }, N0);
        mm(C, 0, w0, xf, [&] { w5 = rw(5); }, [&] { use(w1); w6 = rw(6); D(3); }, N0);
        mm(C, 1, w1, xf, [&] { w7 = rw(7); }, [&] { use(w2); D(4); }, N0);
        mm(C, 2, w2, xf, [&] { use(w3); }, N0, N0);
        mm(C, 3, w3, xf, [&] { use(w7); }, N0, N0);
      }
#pragma unroll
      for (int nb = 0; nb < NB; ++nb) xp[nb] = xf[nb];
    };
    SB();
    if constexpr (NB == 3) {
      static_for(std::make_integer_sequence<int, 4>{}, [&](auto U) {
        constexpr int u = decltype(U)::value;
        wait_vm_bar<8>(); qstep(U, IC<(u < 2 ? u : 2)>{}, [&](int n) { dma_w((u + 3) & 3, n); });
      });
#pragma unroll 1
      for (int s4 = 4; s4 < 16; s4 += 4)
        static_for(std::make_integer_sequence<int, 4>{}, [&](auto U) {
          constexpr int u = decltype(U)::value;
          wait_vm_bar<8>(); qstep(U, IC<2>{}, [&](int n) { dma_w((u + 3) & 3, n); });
        });
      wait_vm_bar<8>(); qstep(IC<0>{}, IC<2>{}, [&](int n) { dma_w(3, n); });
      wait_vm_bar<8>(); qstep(IC<1>{}, IC<2>{}, [&](int) {});
      wait_vm_bar<4>(); qstep(IC<2>{}, IC<2>{}, [&](int) {});
      wait_vm_bar<0>(); qstep(IC<3>{}, IC<2>{}, [&](int) {});
    } else {
      static_for(std::make_integer_sequence<int, 20>{}, [&](auto ST) {
        constexpr int st = decltype(ST)::value, mp = (st >> 1) - 1 - KVN0;
        gov(GOVQ);
        if constexpr (st & 1) wait_vm_bar<63>(); else wait_vm_bar<st == 0 ? 1 : (mp >= 0 && mp < NG) ? 4 : 0>();
        qstep(IC<(st & 3)>{}, IC<(st < 2 ? st : 2)>{}, [&](int n) {
          if constexpr (st & 1) { if (n < 4) extra((st >> 1) - KVN0, n); } else if constexpr ((st >> 1) + 1 < 10) dma_x((st >> 1) + 1, n); });
      });
    }
#pragma unroll
    for (int nb = 0; nb < NB; ++nb) { amfma(s.o[nb][4], w4, xp[nb]); amfma(s.o[nb][5], w5, xp[nb]); amfma(s.o[nb][6], w6, xp[nb]); amfma(s.o[nb][7], w7, xp[nb]); }
    asm volatile("s_nop 7");
    SB();
  }
  auto qconv = [&] {
#pragma unroll
    for (int nb = 0; nb < NB; ++nb)
#pragma unroll
      for (int e = 0; e < 4; ++e) {
        f32x4 a0 = s.o[nb][2 * e], a1 = s.o[nb][2 * e + 1];
#pragma unroll
        for (int r = 0; r < 4; ++r) { a0[r] = amul(a0[r], QSCALE); a1[r] = amul(a1[r], QSCALE); }
        auto r0 = __builtin_amdgcn_permlane16_swap(cvtpk(a0[0], a0[1]), cvtpk(a1[0], a1[1]), false, false);
        auto r1 = __builtin_amdgcn_permlane16_swap(cvtpk(a0[2], a0[3]), cvtpk(a1[2], a1[3]), false, false);
        u32x4 w = {r0[0], r1[0], r0[1], r1[1]};
        s.qf[nb][e] = asb(w); pin(s.qf[nb][e]);
      }
  };
  if constexpr (NB == 2) qconv();
  wait_vm_bar<0>();
  if constexpr (NB == 2) pin(kvr[(NG - 3) * 4 - 1]);
  const unsigned long long tag = (((unsigned long long)__builtin_amdgcn_readfirstlane((unsigned)(epoch >> 32)) << 32) | __builtin_amdgcn_readfirstlane((unsigned)epoch)) + 1ull;
  const int pg = ((g & 1) << 1) | (g >> 1);
  unsigned ka[4];
#pragma unroll
  for (int st = 0; st < 4; ++st) ka[st] = lds0 + kslot(0, c.hs) + l15 * 256 + (((4 * st + pg) ^ l15) * 16);
  unsigned va = lds0 + vslot(0, c.hs) + (g >> 1) * 2048 + (4 * (g & 1) + (l15 >> 2)) * 32 + (lane & 3) * 8;
  auto rks = [&](int sl, int kt, int st) { return ld128(ka[st] + sl * 8192 + kt * 4096); };
  auto rvs = [&](int sl, int db) { const unsigned a = va + sl * 8192 + (db >> 2) * 1024 + (db & 3) * 256; return cat(ldtr(a), ldtr(a + 4096)); };
  {
    bf16x8 kf0[4], kf1[4];
#pragma unroll
    for (int st = 0; st < 4; ++st) { kf0[st] = rks(0, 0, st); kf1[st] = rks(0, 1, st); }
    if constexpr (NB == 3) qconv();
#pragma unroll
    for (int st = 0; st < 4; ++st)
#pragma unroll
      for (int nb = 0; nb < NB; ++nb) {
        s.S0[nb] = mfma(kf0[st], s.qf[nb][st], st == 0 ? z4 : s.S0[nb]); s.S1[nb] = mfma(kf1[st], s.qf[nb][st], st == 0 ? z4 : s.S1[nb]);
      }
#pragma unroll
    for (int nb = 0; nb < NB; ++nb) {
#pragma unroll
      for (int db = (NB == 3 ? 6 : 5); db < 8; ++db) { s.o[nb][db] = z4; pin(s.o[nb][db]); }
      const float m = xmax32(xmax16(max8(s.S0[nb], s.S1[nb]))) + BIAS;
      s.negm[nb] = z4 - m; s.S0[nb] -= m; s.S1[nb] -= m;
      pin(s.negm[nb]);
    }
    s.ones = asb(u32x4{0x3f803f80u, 0x3f803f80u, 0x3f803f80u, 0x3f803f80u});
    pin(s.ones);
  }
  exps<NB, 0, 4 * NB>(s.S0); exps<NB, 0, 2 * NB>(s.S1);
  bf16x8 v5h, v6, v7;
  SB();
  auto us = [&](auto m, auto g0, auto g1, auto g2) {
    m(0); SB(); g0(); SB(); m(1); SB(); g1(); SB();
    if constexpr (NB == 3) { m(2); SB(); g2(); SB(); }
  };
  auto QK0 = [&](int st, bf16x8 fr) { return [&s, st, fr](int nb) { if (st == 0) amfma_c(s.S0[nb], fr, s.qf[nb][0], s.negm[nb]); else amfma(s.S0[nb], fr, s.qf[nb][st]); }; };
  auto QK1 = [&](int st, bf16x8 fr) { return [&s, st, fr](int nb) { if (st == 0) amfma_c(s.S1[nb], fr, s.qf[nb][0], s.negm[nb]); else amfma(s.S1[nb], fr, s.qf[nb][st]); }; };
  auto PVH = [&](int db, bf16x8& fr) { return [&s, db, &fr](int nb) { amfma(s.o[nb][db], fr, asb(s.pf[nb])); }; };
  auto N = [] {};
#define E0(e) [&] { exp1<NB, (e)>(s.S0); }
#define E1(e) [&] { exp1<NB, (e)>(s.S1); }
#define CLO(nb) [&] { if constexpr ((nb) < NB) { s.pf[nb][0] = acvt(s.S0[nb][0], s.S0[nb][1]); s.pf[nb][1] = acvt(s.S0[nb][2], s.S0[nb][3]); } }
#define CHI(nb) [&] { if constexpr ((nb) < NB) { s.pf[nb][2] = acvt(s.S1[nb][0], s.S1[nb][1]); s.pf[nb][3] = acvt(s.S1[nb][2], s.S1[nb][3]); } }
#define U(f, ...) [&] { use(f); SB(); (__VA_ARGS__)(); }
  unsigned dk = 8192, dv = 8192, cnt = 1;
  if constexpr (NB == 3) {
#pragma unroll
    for (int st = 0; st < 4; ++st) ka[st] += 8192;
  }
  auto astep = [&](auto FI, auto SL, int t, auto kvh) {
    constexpr bool FIRST = decltype(FI)::value;
    constexpr int sl = decltype(SL)::value;
    auto rk = [&](int kt, int st) { return rks(NB == 3 ? 0 : sl, kt, st); };
    auto rv = [&](int db) { return rvs(NB == 3 ? 0 : (sl + 4) % 5, db); };
    if constexpr (NB == 2) { gov(GOVA); wait_vm_bar<6>(); } else wait_vm_bar<63>();
    auto PH = [&](int db, bf16x8& fr) { return [&, db](int nb) { if constexpr (!FIRST) amfma(s.o[nb][db], fr, asb(s.pf[nb])); }; };
    auto PV = [&](int db, bf16x8 fr) { return [&s, db, fr](int nb) { amf<FIRST>(s.o[nb][db], fr, asb(s.pf[nb])); }; };
    auto SUM = [&](int nb) { amf<FIRST>(s.l[nb], s.ones, asb(s.pf[nb])); };
    bf16x8 a0, a1, a2, a3, b0, b1, b2, b3, v0, v1, v2, v3, v4;
    unsigned o1, o2;
    if constexpr (NB == 3) {
      bf16x8 v5;
      us(PH(6, v6), [&] { a0 = rk(0, 0); a1 = rk(0, 1); }, [&] { a2 = rk(0, 2); a3 = rk(0, 3); }, E1(6));
      us(PH(7, v7), CLO(0), CLO(1), U(a0, CLO(2)));
      us(QK0(0, a0), [&] { b0 = rk(1, 0); b1 = rk(1, 1); }, E1(7), U(a1, E1(8)));
      us(QK0(1, a1), E1(9), E1(10), U(a2, E1(11)));
      us(QK0(2, a2), [&] { o1 = aor3(s.pf[0][0], s.pf[0][1], s.pf[1][0]); o2 = aor3(s.pf[1][1], s.pf[2][0], s.pf[2][1]); }, CHI(0), U(a3, CHI(1)));
      us(QK0(3, a3), CHI(2), [&] { o1 = aor3(o1, s.pf[0][2], s.pf[0][3]); o2 = aor3(o2, s.pf[1][2], s.pf[1][3]); },
                     U(b0, [&] { o1 = aor3(o1, s.pf[2][2], s.pf[2][3]); s.orv = o1 | o2; }));
      if constexpr (!FIRST) check<NB, true>(s, c, t - 1);
      SB();
      us(QK1(0, b0), [&] { b2 = rk(1, 2); b3 = rk(1, 3); }, [&] { v0 = rv(0); }, U(b1, [&] { aaddu(ka[0], dk); aaddu(ka[1], dk); }));
      us(QK1(1, b1), [&] { v1 = rv(1); }, E0(0), U(b2, E0(1)));
      us(QK1(2, b2), [&] { v2 = rv(2); }, E0(2), U(b3, E0(3)));
      us(QK1(3, b3), [&] { v3 = rv(3); }, E0(4), E0(5));
      us(SUM, E0(6), E0(7), U(v0, E0(8)));
      us(PV(0, v0), [&] { v4 = rv(4); }, E0(9), U(v1, E0(10)));
      us(PV(1, v1), [&] { v5 = rv(5); }, E0(11), U(v2, E1(0)));
      us(PV(2, v2), [&] { v6 = rv(6); }, E1(1), U(v3, E1(2)));
      us(PV(3, v3), [&] { v7 = rv(7); }, E1(3), U(v4, E1(4)));
      us(PV(4, v4), E1(5), [&] { aaddu(ka[2], dk); aaddu(ka[3], dk); }, U(v5, [&] { aaddu(va, dv); }));
      us(PV(5, v5), [&] { dv = dk; cnt = cnt == 4 ? 0 : cnt + 1; pins(cnt); }, [&] { use(v7); dk = cnt == 4 ? -32768 : 8192; pins(dk); }, N);
    } else {
      us(PH(5, v5h), [&] { a0 = rk(0, 0); a1 = rk(0, 1); }, [&] { a2 = rk(0, 2); a3 = rk(0, 3); }, N);
      us(PH(6, v6), E1(4), [&] { kvh(0); }, N);
      us(PH(7, v7), CLO(0), U(a0, CLO(1)), N);
      us(QK0(0, a0), [&] { b0 = rk(1, 0); b1 = rk(1, 1); }, U(a1, E1(5)), N);
      us(QK0(1, a1), E1(6), U(a2, E1(7)), N);
      us(QK0(2, a2), [&] { o1 = aor3(s.pf[0][0], s.pf[0][1], s.pf[1][0]); kvh(1); }, U(a3, CHI(0)), N);
      us(QK0(3, a3), CHI(1), U(b0, [&] { o1 = aor3(o1, s.pf[1][1], s.pf[0][2]); o2 = aor3(s.pf[0][3], s.pf[1][2], s.pf[1][3]); s.orv = o1 | o2; }), N);
      if constexpr (!FIRST) check<NB, true>(s, c, t - 1);
      SB();
      us(QK1(0, b0), [&] { b2 = rk(1, 2); b3 = rk(1, 3); }, U(b1, [&] { v0 = rv(0); kvh(2); }), N);
      us(QK1(1, b1), [&] { v1 = rv(1); }, U(b2, [&] { kvh(3); }), N);
      us(QK1(2, b2), [&] { v2 = rv(2); }, U(b3, E0(0)), N);
      us(QK1(3, b3), [&] { v3 = rv(3); }, [&] { exp1<NB, 1>(s.S0); kvh(4); }, N);
      us(SUM, E0(2), U(v0, E0(3)), N);
      us(PV(0, v0), [&] { v4 = rv(4); }, U(v1, [&] { exp1<NB, 4>(s.S0); kvh(5); }), N);
      us(PV(1, v1), [&] { v5h = rv(5); }, U(v2, E0(5)), N);
      us(PV(2, v2), [&] { v6 = rv(6); }, U(v3, [&] { exp1<NB, 6>(s.S0); kvh(6); }), N);
      us(PV(3, v3), [&] { v7 = rv(7); exp1<NB, 7>(s.S0); }, U(v4, [&] { exp1<NB, 0>(s.S1); kvh(7); }), N);
      us(PV(4, v4), [&] { exp1<NB, 1>(s.S1); exp1<NB, 2>(s.S1); }, [&] { exp1<NB, 3>(s.S1); use(v7); }, N);
    }
  };
  auto piece = [&](int t, int n) {
    const int p0 = (PP2 * (t - 1) + 1) / 2, p = p0 + n;
    if (p < (PP2 * t + 1) / 2 && p < (32 - NG) * 4) {
      const int it = NG + (p >> 2), h = (p >> 1) & 1, v = it == 31 || !(it & 1), tn = it == 31 ? 15 : v ? (it - 2) / 2 : (it + 1) / 2;
      const unsigned so = tso(tn) + h * 131072;
      if (p & 1) { wait_vm<THR>(); dmao2<1, 1024>(v ? voffV[1] : voffK[1], v ? c.rv : c.rk, so); }
      else { setm0(jb + (v ? vslot(tn % 5, h) : kslot(tn % 5, h))); wait_vm<THR>(); dmao2<1, 0>(v ? voffV[0] : voffK[0], v ? c.rv : c.rk, so); }
    }
  };
  astep(std::true_type{}, IC<1>{}, 1, [&](int n) { if constexpr (NB == 2) {
    piece(1, n);
#pragma unroll
    for (int i = 0; i < (NG - 3) * 4; ++i) if (i * 8 / ((NG - 3) * 4) == n) { const int m = i >> 2, q = i & 3;
      st128(jb + lane * 16 + ((m & 1) ? vslot(1 + (m >> 1), q >> 1) : kslot(2 + (m >> 1), q >> 1)) + (q & 1) * 1024, kvr[i]); } } });
  constexpr std::false_type F{};
  if constexpr (NB == 3) {
    auto NH = [](int) {};
#pragma unroll 1
    for (int t = 2; t < 16; ++t) astep(F, IC<0>{}, t, NH);
  } else {
    static_for(std::make_integer_sequence<int, 12>{}, [&](auto TT) {
      constexpr int t = decltype(TT)::value + 2;
      astep(F, IC<t % 5>{}, t, [&](int n) { piece(t, n); });
    });
  }
  rsrc_t rof, rwo, rslab; int pr, hg, rotb; gu64* sync; u16* ob;
  {
    int id = c.id; asm volatile("" : "+s"(id));
    const int br = (id >> 2) & 1, b = id >> 5;
    hg = id & 3; pr = (id >> 3) & 3; rotb = b & 3;
    rof = mkr(c.ws + (size_t)((br * 8 + b) * 4 + hg) * 327680, 327680);
    rwo = mkr((const char*)(c.wo + (size_t)hg * 1024 * 640 + pr * 160) - 4096, 1024 * 1280 - pr * 320 + 4096);
    rslab = mkr(c.ws + WS_SLAB + (size_t)((br * 8 + b) * 4 + pr) * (12 * QB), 12 * QB);
    sync = (gu64*)(c.ws + WS_SYNC);
    ob = c.out + (size_t)(b * 160 + (hg & 1) * 80) * 1280 + br * 640 + pr * 160 + (hg >> 1) * 80;
  }
  unsigned lz = 0;
  const int dks = c.j >> 1, dh = c.j & 1;
  auto stg = [](int u) { return u * 40960; };
  auto kpair = [&](int i) { return ((pr + (i >> 2)) & 3) * 4 + ((i + rotb) & 3); };
  auto wvo = [&](int n) { const int e = 64 * n + (int)lz, row = e / 20, ch = e - row * 20; return row * 1280 + ((ch ^ (((row >> 2) & 1) << 1)) * 16) + 4096; };
  auto wso = [&](int i) { return (kpair(i) * 64 + dks * 32 + dh * 16) * 1280; };
  auto wld = [&](int i, int n) { return lds0 + stg(i & 3) + 20480 + dks * 10240 + dh * 5120 + n * 1024; };
  u32x4 wr[15];
  if constexpr (NB == 2) {
    lz = laneid();
    unsigned vw[5];
#pragma unroll
    for (int n = 0; n < 5; ++n) vw[n] = wvo(n);
    auto wl = [&](int p) { wait_vm<4>(); if (p < 15) wr[p] = __builtin_amdgcn_raw_buffer_load_b128(rwo, vw[p % 5], wso(p / 5), 0); };
    SB();
    astep(F, IC<4>{}, 14, [&](int n) { wl(n); });
    astep(F, IC<0>{}, 15, [&](int n) { wl(8 + n); });
  }
  if constexpr (NB == 3) wait_vm_bar<63>(); else wait_vm_bar<15>();
  u32x4 ow[NB][4];
  unsigned gb;
  auto ost = [&](int e, int nb) { __builtin_amdgcn_raw_buffer_store_b128(ow[nb][e], rof, gb + e * 10240 + nb * 1024, 0, 16); };
  {
    bf16x8 vf[8];
#pragma unroll
    for (int db = 0; db < 4; ++db) vf[db] = rvs(0, db);
    SB();
    if constexpr (NB == 2) us(PVH(5, v5h), N, N, N);
    us(PVH(6, v6), N, N, N);
    exps<NB, 2 * NB, 4 * NB>(s.S1);
    us(PVH(7, v7), CLO(0), CLO(1), CLO(2));
    CHI(0)(); CHI(1)(); CHI(2)();
    s.orv = 0;
#pragma unroll
    for (int nb = 0; nb < NB; ++nb) { s.orv = aor3(s.orv, s.pf[nb][0], s.pf[nb][1]); s.orv = aor3(s.orv, s.pf[nb][2], s.pf[nb][3]); }
    SB();
    check<NB, false>(s, c, 15);
    SB();
    if constexpr (NB == 3) lz = laneid();
#pragma unroll
    for (int db = 4; db < 8; ++db) vf[db] = rvs(0, db);
    gb = lz * 16 + (pr * 8 + c.hs * 4) * 10240 + c.blk0 * 1024;
#pragma unroll
    for (int nb = 0; nb < NB; ++nb) s.l[nb] = mfma(s.ones, asb(s.pf[nb]), s.l[nb]);
    float inv[NB];
    auto norm = [&](int e, int nb) {
      f32x4 a0 = s.o[nb][2 * e], a1 = s.o[nb][2 * e + 1];
#pragma unroll
      for (int r = 0; r < 4; ++r) { float x = a0[r] * inv[nb], y = a1[r] * inv[nb]; pin(x); pin(y); a0[r] = x; a1[r] = y; }
      ow[nb][e] = u32x4{cvtpk(a0[0], a0[1]), cvtpk(a0[2], a0[3]), cvtpk(a1[0], a1[1]), cvtpk(a1[2], a1[3])};
      if (e < EPRE) ost(e, nb);
    };
#pragma unroll
    for (int e = 0; e < 4; ++e) {
#pragma unroll
      for (int nb = 0; nb < NB; ++nb) {
        s.o[nb][2 * e] = mfma(vf[2 * e], asb(s.pf[nb]), s.o[nb][2 * e]);
        s.o[nb][2 * e + 1] = mfma(vf[2 * e + 1], asb(s.pf[nb]), s.o[nb][2 * e + 1]);
        if (e == 0) inv[nb] = __builtin_amdgcn_rcpf(s.l[nb][0]); else norm(e - 1, nb);
        if constexpr (NB == 2) { const int p = (e * 2 + nb) * 2; st128(wld(p / 5, p % 5) + lz * 16, wr[p]); if (p + 1 < 15) st128(wld((p + 1) / 5, (p + 1) % 5) + lz * 16, wr[p + 1]); }
        SB();
      }
    }
#pragma unroll
    for (int nb = 0; nb < NB; ++nb) norm(3, nb);
  }
  asm volatile("s_waitcnt lgkmcnt(0)\n\ts_barrier" ::: "memory");
#pragma unroll
  for (int eh = 0; eh < 2; ++eh) {
    const unsigned lb = lds0 + stg((c.hs * 2 + eh - rotb) & 3) + c.blk0 * 1024 + lz * 16;
#pragma unroll
    for (int el = 0; el < 2; ++el)
#pragma unroll
      for (int nb = 0; nb < NB; ++nb) st128(lb + el * 10240 + nb * 1024, ow[nb][eh * 2 + el]);
  }
  {
    constexpr int NM = NB, M0 = NM == 3 ? 0 : 3;
    const int q = c.j, qm = q & 1, qn = q >> 1, zg = lz >> 4;
    unsigned vo3[5];
#pragma unroll
    for (int n = 0; n < 5; ++n) vo3[n] = NB == 3 ? wvo(n) - (n & 3) * 1024 : lz * 16 + (n >> 2) * 4096;
    const unsigned lb3 = lds0 + dks * 10240 + dh * 5120 + (NB == 3 ? 20480 : 0);
    auto so3 = [&](int i) { unsigned v = (NB == 3 ? wso(i) : (kpair(i) * 2 + dks) * 10240 + dh * 5120) + (unsigned)(i >> 4) * OOB; pins(v); return v; };
    auto dmal = [&](unsigned so, int u, int n) {
      constexpr int POL = NB == 3 ? 0 : 2;
      const rsrc_t r = NB == 3 ? rwo : rof;
      const unsigned vo = vo3[NB == 3 ? n : (n >> 2) * 4];
      if (n == 0) { setm0(lb3 + stg(u)); dmao<POL, 0>(vo, r, so); }
      else if (n == 1) dmao2<POL, 1024>(vo, r, so);
      else if (n == 2) dmao2<POL, 2048>(vo, r, so);
      else if (n == 3) dmao2<POL, 3072>(vo, r, so);
      else { setm0(lb3 + stg(u) + 4096); dmao<POL, 0>(vo, r, so); }
    };
    if constexpr (NB == 3) {
      const unsigned so = so3(3);
#pragma unroll
      for (int n = 0; n < 5; ++n) dmal(so, 3, n);
    }
    unsigned oa[2], wa[2][2];
    {
      oa[0] = lds0 + (qm * 5 + M0) * 1024 + lz * 16; oa[1] = oa[0] + 81920;
      const unsigned wb = lds0 + 20480 + (4 * zg + ((lz & 15) >> 2)) * 320 + (lz & 3) * 8 + qn * 160;
      const int sg = (qn ? -32 : 32) * (zg & 1);
      wa[0][0] = wb + sg; wa[0][1] = wb - sg; wa[1][0] = wa[0][0] + 81920; wa[1][1] = wa[0][1] + 81920;
      pin(oa[1]); pin(wa[1][0]); pin(wa[1][1]);
    }
    f32x4 acc[NM][5];
    bf16x8 h2, h3, h4, op[NM];
    auto interval = [&](auto UU, auto D, auto tail) {
      constexpr bool FIRST = decltype(UU)::value < 0;
      constexpr int u = FIRST ? 0 : decltype(UU)::value, so = (u & 1) * 40960;
      auto ro = [&](int ks, int m) { return ld128(oa[u >> 1] + so + ks * 10240 + m * 1024); };
      auto rw = [&](int ks, int n) { const unsigned a = wa[u >> 1][n & 1] + so + ks * 10240 + n * 32; return cat(ldtr(a), ldtr(a + 5120)); };
      auto mm = [&](auto Z, int n, bf16x8& wf, bf16x8 (&of)[NM], auto g0, auto g1, auto g2) {
        constexpr int z = decltype(Z)::value;
        if constexpr (z == 2) { g0(); g1(); g2(); SB(); }
        else {
          amf<z == 1>(acc[0][n], wf, of[0]); SB(); g0(); SB(); amf<z == 1>(acc[1][n], wf, of[1]); SB(); g1(); SB();
          if constexpr (NM == 3) { amf<z == 1>(acc[2][n], wf, of[2]); SB(); }
          g2(); SB();
        }
      };
      constexpr IC<FIRST ? 2 : 0> H{};
      constexpr IC<FIRST ? 1 : 0> C{};
      constexpr IC<0> P{};
      bf16x8 o0[NM], o1[NM], a0, a1, a2, a3, a4, b0, b1;
      mm(H, 2, h2, op, [&] { a0 = rw(0, 0); }, [&] { o0[0] = ro(0, 0); o0[1] = ro(0, 1); }, [&] { if constexpr (NM == 3) o0[2] = ro(0, 2); });
      mm(H, 3, h3, op, [&] { a1 = rw(0, 1); }, [&] { a2 = rw(0, 2); }, [&] { D(0); });
      mm(H, 4, h4, op, [&] { a3 = rw(0, 3); }, [&] { a4 = rw(0, 4); }, [&] { use(o0[NM - 1]); });
      mm(C, 0, a0, o0, [&] { o1[0] = ro(1, 0); o1[1] = ro(1, 1); }, [&] { use(a1); if constexpr (NM == 3) o1[2] = ro(1, 2); }, [&] { D(1); });
      mm(C, 1, a1, o0, [&] { b0 = rw(1, 0); }, [&] { use(a2); b1 = rw(1, 1); }, N0);
      mm(C, 2, a2, o0, [&] { h2 = rw(1, 2); }, [&] { use(a3); }, [&] { D(2); });
      mm(C, 3, a3, o0, [&] { h3 = rw(1, 3); }, [&] { use(a4); }, N0);
      mm(C, 4, a4, o0, [&] { h4 = rw(1, 4); }, [&] { use(b0); }, [&] { D(3); });
      mm(P, 0, b0, o1, N0, [&] { use(b1); }, [&] { D(4); });
      mm(P, 1, b1, o1, [&] { use(h4); }, N0, N0);
      tail();
#pragma unroll
      for (int m = 0; m < NM; ++m) op[m] = o1[m];
    };
    auto ND = [](int) {};
    SB();
    auto poll = [&](int base) {
      const int pid = c.id ^ (((lz & 3) ? (lz & 3) : 1) << 3);
      for (unsigned spins = 0; spins < SPINS; ++spins) {
        if (__all(__hip_atomic_load(sync + (base + pid) * FS, RLX_AGENT) == tag)) break;
        __builtin_amdgcn_s_sleep(1);
      }
    };
    wait_vm_bar<63>();
    interval(IC<-1>{}, [&](int n) {
#pragma unroll
      for (int k = 0; k < (4 - EPRE) * NB; ++k) if (k * 5 / (EPRE < 4 ? (4 - EPRE) * NB : 1) == n) ost(EPRE + k / NB, k % NB); }, N0);
    wait_vm_bar<63>();
    { const unsigned so = so3(4); interval(IC<1>{}, [&](int n) { if constexpr (NB == 3) dmal(so, 0, n); }, N0); }
    if constexpr (NB == 3) wait_vm_bar<5>(); else wait_vm_bar<0>();
    if (c.w == 3 && lz == 0) __hip_atomic_store(sync + (1 + c.id) * FS, tag, RLX_AGENT);
    const unsigned so5 = so3(5);
    interval(IC<2>{}, [&](int n) { if constexpr (NB == 3) dmal(so5, 1, n); }, [&] { if (c.w == 2) poll(1); });
    if constexpr (NB == 3) wait_vm_bar<10>(); else wait_vm_bar<63>();
    { const unsigned sa = so3(NB == 3 ? 6 : 4);
      interval(IC<3>{}, [&](int n) { if constexpr (NB == 3) dmal(sa, 2, n);
        else { if (n == 0) { dmal(sa, 0, 0); dmal(sa, 0, 1); } else if (n == 1) { dmal(sa, 0, 2); dmal(sa, 0, 3); } else if (n == 2) { dmal(sa, 0, 4); dmal(so5, 1, 0); }
               else if (n == 3) { dmal(so5, 1, 1); dmal(so5, 1, 2); } else { dmal(so5, 1, 3); dmal(so5, 1, 4); } } }, N0); }
    unsigned son = so3(NB == 3 ? 7 : 6);
#pragma unroll 1
    for (int i4 = 4; i4 < 16; i4 += 4)
      static_for(std::make_integer_sequence<int, 4>{}, [&](auto UU) {
        constexpr int u = decltype(UU)::value;
        if constexpr (NB == 3) wait_vm_bar<10>(); else wait_vm_bar<5>();
        interval(UU, [&](int n) { dmal(son, (u + (NB == 3 ? 3 : 2)) & 3, n); if (n == 4) son = so3(i4 + u + (NB == 3 ? 4 : 3)); }, N0);
      });
#pragma unroll
    for (int m = 0; m < NM; ++m) { amfma(acc[m][2], h2, op[m]); amfma(acc[m][3], h3, op[m]); amfma(acc[m][4], h4, op[m]); }
    asm volatile("s_nop 7");
    SB();
    if (q != hg) {
      const int so = (q * 3 + ((hg - q - 1) & 3)) * QB;
#pragma unroll
      for (int e = 0; e < NM * 5; e += 2) {
        const f32x4 a = acc[e / 5][e % 5], b = e + 1 < NM * 5 ? acc[(e + 1) / 5][(e + 1) % 5] : z4;
        __builtin_amdgcn_raw_buffer_store_b128(u32x4{cvtpk(a[0], a[1]), cvtpk(a[2], a[3]), cvtpk(b[0], b[1]), cvtpk(b[2], b[3])}, rslab, lz * 16 + ((M0 ? 8 : 0) + e / 2) * 1024 + so, 0, 16);
      }
    }
    asm volatile("s_waitcnt vmcnt(0) lgkmcnt(0)\n\ts_barrier" ::: "memory");
    if (c.w == 0 && lz == 0) __hip_atomic_store(sync + (257 + c.id) * FS, tag, RLX_AGENT);
    if (q == hg) {
#pragma unroll
      for (int e = 0; e < NM * 5; ++e) st128(lds0 + lz * 16 + (M0 * 5 + e) * 1024, __builtin_bit_cast(u32x4, acc[e / 5][e % 5]));
    }
    if (c.w == ((hg + 1) & 3)) {
      const int pid = c.id ^ ((lz & 3) ? (lz & 3) : 1);
      for (unsigned spins = 0; spins < SPINS; ++spins) {
        if (__all(__hip_atomic_load(sync + (257 + pid) * FS, RLX_AGENT) == tag)) break;
        __builtin_amdgcn_s_sleep(1);
      }
    }
    if (c.id == 0 && c.w == 7) {
      for (unsigned spins = 0; spins < SPINS; ++spins) {
        bool ok = true;
#pragma unroll
        for (int k = 0; k < 4; ++k) ok &= __hip_atomic_load(sync + (1 + lz + 64 * k) * FS, RLX_AGENT) == tag;
        if (__all(ok)) break;
        __builtin_amdgcn_s_sleep(1);
      }
      if (lz == 0) __hip_atomic_store(sync, tag, RLX_AGENT);
    }
    asm volatile("s_waitcnt vmcnt(0) lgkmcnt(0)\n\ts_barrier" ::: "memory");
    u16* op16 = ob + (size_t)(lz & 15) * 1280 + 4 * zg;
    u32x4 r[2][3];
#pragma unroll
    for (int k = 0; k < 2; ++k)
#pragma unroll
      for (int sl = 0; sl < 3; ++sl) r[k][sl] = __builtin_amdgcn_raw_buffer_load_b128(rslab, c.w + 8 * k < 13 ? lz * 16 + (c.w + 8 * k) * 1024 : OOB, (hg * 3 + sl) * QB, 16);
#pragma unroll
    for (int k = 0; k < 2; ++k) {
      const int un = c.w + 8 * k;
#pragma unroll
      for (int hh = 0; hh < 2; ++hh) {
        const int e = un < 8 ? 2 * un + hh : 15 + 2 * (un - 8) + hh;
        if (un < 13 && !(un == 7 && hh == 1)) {
          f32x4 v = *(const LDS3 f32x4*)(size_t)(lds0 + lz * 16 + e * 1024);
#pragma unroll
          for (int sl = 0; sl < 3; ++sl) { const unsigned lo = r[k][sl][2 * hh], hi = r[k][sl][2 * hh + 1];
            v += f32x4{__uint_as_float(lo << 16), __uint_as_float(lo & 0xffff0000u), __uint_as_float(hi << 16), __uint_as_float(hi & 0xffff0000u)}; }
          const int m = e / 5, n = e - m * 5;
          *(__attribute__((address_space(1))) u32x2*)(op16 + m * (16 * 1280) + n * 16) = u32x2{cvtpk(v[0], v[1]), cvtpk(v[2], v[3])};
        }
      }
    }
  }
}

struct Args { const void* p[16]; };
__global__ __launch_bounds__(512, 2) void fused(Args) {
  extern __shared__ __attribute__((aligned(16))) char smem[];
  typedef const __attribute__((address_space(4))) unsigned long long* kargp;
  kargp ka = (kargp)__builtin_amdgcn_kernarg_segment_ptr();
  const int id = blockIdx.x, xg = id & 7, cc = id >> 3, br = xg >> 2, hg = xg & 3, b = cc >> 2, pr = cc & 3, h0 = hg * 8 + pr * 2;
  ka += br * 8;
  const u16* x = (const u16*)ka[0]; const u16* k = (const u16*)ka[1]; const u16* v = (const u16*)ka[2]; const u16* wq = (const u16*)ka[3];
  const int w = __builtin_amdgcn_readfirstlane(threadIdx.x >> 6);
  asm volatile("s_nop 4");
  Ctx c;
  { unsigned long long pc; asm volatile("s_getpc_b64 %0" : "=s"(pc)); c.code = pc & ~255ull; }
  c.rx = mkr(x + (size_t)b * SQ * DM, SQ * DM * 2);
  c.rw = mkr(wq + h0 * DH, DM * NH * DH * 2 - h0 * DH * 2);
  c.rk = mkr(k + (size_t)(b * NH + h0) * SK * DH, 2 * SK * DH * 2);
  c.rv = mkr((const char*)(v + (size_t)(b * NH + h0) * SK * DH) - 1024, 2 * SK * DH * 2 + 1024);
  c.lds0 = (unsigned)(size_t)(LDS3 char*)smem;
  c.hs = w >> 2; c.j = w & 3; c.rot = (id * 5) & 15; c.krot = ((10 * pr + 5 * b) >> 2) % 10;
  c.w = w; c.id = id; c.wo = (const u16*)ka[4]; c.out = (u16*)ka[5]; c.ws = (char*)ka[6];
  const bool big = (c.j < 2) == (c.hs == 0);
  c.blk0 = (0x74208630u >> (4 * w)) & 15;
  if (big) body<3>(c); else body<2>(c);
}

extern "C" void kernel_launch(void* const* d_in, const int* in_sizes, int n_in, void* d_out, int out_size, void* d_ws, size_t ws_size, hipStream_t stream) {
  (void)hipFuncSetAttribute((const void*)fused, hipFuncAttributeMaxDynamicSharedMemorySize, LDS_BYTES);
  Args a;
  for (int br = 0; br < 2; ++br) {
    for (int i = 0; i < 5; ++i) a.p[br * 8 + i] = d_in[br * 5 + i];
    a.p[br * 8 + 5] = d_out; a.p[br * 8 + 6] = d_ws; a.p[br * 8 + 7] = d_ws;
  }
  fused<<<256, 512, LDS_BYTES, stream>>>(a);
}
```

```cpp
#include <hip/hip_runtime.h>
#include <hip/hip_bf16.h>
#include <type_traits>

typedef __bf16 bf16x8 __attribute__((ext_vector_type(8)));
typedef __bf16 bf16x4 __attribute__((ext_vector_type(4)));
typedef __bf16 bf16x2 __attribute__((ext_vector_type(2)));
typedef float f32x4 __attribute__((ext_vector_type(4)));
typedef float f32x2 __attribute__((ext_vector_type(2)));
typedef unsigned u32x4 __attribute__((ext_vector_type(4)));
typedef unsigned u32x2 __attribute__((ext_vector_type(2)));
typedef unsigned short u16;
typedef __attribute__((address_space(1))) unsigned long long gu64;
typedef __amdgpu_buffer_rsrc_t rsrc_t;
#define LDS3 __attribute__((address_space(3)))
#define DI __device__ __forceinline__
#define SB() __builtin_amdgcn_sched_barrier(0)
#define RLX_AGENT __ATOMIC_RELAXED, __HIP_MEMORY_SCOPE_AGENT
template <int N> using IC = std::integral_constant<int, N>;

constexpr int SQ = 160, DM = 640, NH = 32, SK = 512, DH = 128;
constexpr int LDS_BYTES = 163840;
__host__ __device__ constexpr int kslot(int sl, int hs) { return hs * 40960 + sl * 8192; }
__host__ __device__ constexpr int vslot(int sl, int hs) { return 81920 + hs * 40960 + sl * 8192; }
__host__ __device__ constexpr int xoff(int b) { return 16384 + b * 40960; }
__host__ __device__ constexpr int woff(int b) { return 90112 + (b & 1) * 16384 + (b >> 1) * 40960; }
constexpr float QSCALE = 0.08838834764831845f * 1.4426950408889634f;
constexpr float BIAS = 30.f;
constexpr unsigned OOB = 0x7ff00000u;
constexpr size_t WS_SLAB = 32u << 20, WS_SYNC = 64u << 20;
constexpr int FS = 32;
constexpr int QB = 13 * 1024;
constexpr int KVN0 = 1;
constexpr int NG = 8;
constexpr int PP2 = 15;
constexpr int THR = 3;
constexpr int EPRE = 2;
constexpr unsigned GOVQ = 55, GOVA = 110;
constexpr unsigned SPINS = 1u << 17;
constexpr int CODE_KB = 100;

DI unsigned cvtpk(float lo, float hi) { f32x2 v = {lo, hi}; bf16x2 b = __builtin_convertvector(v, bf16x2); return __builtin_bit_cast(unsigned, b); }
DI f32x4 mfma(bf16x8 a, bf16x8 b, f32x4 c) { return __builtin_amdgcn_mfma_f32_16x16x32_bf16(a, b, c, 0, 0, 0); }
DI bf16x8 ld128(unsigned addr) { return *(const LDS3 bf16x8*)(size_t)addr; }
DI void st128(unsigned addr, u32x4 v) { *(LDS3 u32x4*)(size_t)addr = v; }
DI bf16x4 ldtr(unsigned addr) { return __builtin_amdgcn_ds_read_tr16_b64_v4bf16((LDS3 bf16x4*)(size_t)addr); }
DI bf16x8 cat(bf16x4 a, bf16x4 b) { return __builtin_shufflevector(a, b, 0, 1, 2, 3, 4, 5, 6, 7); }
DI bf16x8 asb(u32x4 w) { return __builtin_bit_cast(bf16x8, w); }
DI rsrc_t mkr(const void* p, int bytes) { return __builtin_amdgcn_make_buffer_rsrc((void*)p, (short)0, bytes, 0x00020000); }
template <bool NT> DI void dma16(unsigned ldsaddr, unsigned voff, rsrc_t r, unsigned soff) {
  if constexpr (NT) asm volatile("s_mov_b32 m0, %0\n\ts_nop 0\n\tbuffer_load_dwordx4 %1, %2, %3 offen nt lds" :: "s"(ldsaddr), "v"(voff), "s"(r), "s"(soff) : "memory");
  else              asm volatile("s_mov_b32 m0, %0\n\ts_nop 0\n\tbuffer_load_dwordx4 %1, %2, %3 offen lds" :: "s"(ldsaddr), "v"(voff), "s"(r), "s"(soff) : "memory");
}
DI void setm0(unsigned v) { asm volatile("s_mov_b32 m0, %0" :: "s"(v) : "memory"); }
template <int POL, int OFF> DI void dmao(unsigned voff, rsrc_t r, unsigned soff) {
  if constexpr (POL == 1)      asm volatile("s_nop 0\n\tbuffer_load_dwordx4 %0, %1, %2 offen offset:%3 nt lds" :: "v"(voff), "s"(r), "s"(soff), "i"(OFF) : "memory");
  else if constexpr (POL == 2) asm volatile("s_nop 0\n\tbuffer_load_dwordx4 %0, %1, %2 offen offset:%3 sc1 lds" :: "v"(voff), "s"(r), "s"(soff), "i"(OFF) : "memory");
  else                         asm volatile("s_nop 0\n\tbuffer_load_dwordx4 %0, %1, %2 offen offset:%3 lds" :: "v"(voff), "s"(r), "s"(soff), "i"(OFF) : "memory");
}
template <int POL, int OFF> DI void dmao2(unsigned voff, rsrc_t r, unsigned soff) {
  if constexpr (POL == 1)      asm volatile("buffer_load_dwordx4 %0, %1, %2 offen offset:%3 nt lds" :: "v"(voff), "s"(r), "s"(soff), "i"(OFF) : "memory");
  else if constexpr (POL == 2) asm volatile("buffer_load_dwordx4 %0, %1, %2 offen offset:%3 sc1 lds" :: "v"(voff), "s"(r), "s"(soff), "i"(OFF) : "memory");
  else                         asm volatile("buffer_load_dwordx4 %0, %1, %2 offen offset:%3 lds" :: "v"(voff), "s"(r), "s"(soff), "i"(OFF) : "memory");
}
DI void pins(unsigned& v) { v = __builtin_amdgcn_readfirstlane(v); asm volatile("" : "+s"(v)); }
template <int N> DI void wait_vm() { asm volatile("s_waitcnt vmcnt(%0)" :: "i"(N) : "memory"); }
template <int N> DI void wait_vm_bar() { asm volatile("s_waitcnt vmcnt(%0) lgkmcnt(0)\n\ts_barrier" :: "i"(N) : "memory"); }
DI float xmax16(float v) { auto r = __builtin_amdgcn_permlane16_swap(__float_as_uint(v), __float_as_uint(v), false, false); return fmaxf(__uint_as_float(r[0]), __uint_as_float(r[1])); }
DI float xmax32(float v) { auto r = __builtin_amdgcn_permlane32_swap(__float_as_uint(v), __float_as_uint(v), false, false); return fmaxf(__uint_as_float(r[0]), __uint_as_float(r[1])); }
DI void amfma(f32x4& acc, bf16x8 a, bf16x8 b) { asm volatile("v_mfma_f32_16x16x32_bf16 %0, %1, %2, %0" : "+v"(acc) : "v"(a), "v"(b)); }
DI void amfma_c(f32x4& d, bf16x8 a, bf16x8 b, f32x4 cc) { asm volatile("v_mfma_f32_16x16x32_bf16 %0, %1, %2, %3" : "=&v"(d) : "v"(a), "v"(b), "v"(cc)); }
DI void amfma_z(f32x4& d, bf16x8 a, bf16x8 b) { asm volatile("v_mfma_f32_16x16x32_bf16 %0, %1, %2, 0" : "=&v"(d) : "v"(a), "v"(b)); }
template <bool Z> DI void amf(f32x4& d, bf16x8 a, bf16x8 b) { if constexpr (Z) amfma_z(d, a, b); else amfma(d, a, b); }
DI float aexp(float x) { asm volatile("v_exp_f32 %0, %0" : "+v"(x)); return x; }
DI float amul(float a, float b) { float r; asm("v_mul_f32 %0, %1, %2" : "=v"(r) : "s"(b), "v"(a)); return r; }
DI unsigned aor3(unsigned a, unsigned b, unsigned c) { unsigned r; asm volatile("v_or3_b32 %0, %1, %2, %3" : "=v"(r) : "v"(a), "v"(b), "v"(c)); return r; }
DI unsigned acvt(float lo, float hi) { unsigned r; asm volatile("v_cvt_pk_bf16_f32 %0, %1, %2" : "=v"(r) : "v"(lo), "v"(hi)); return r; }
DI void use(bf16x8 f) { asm volatile("" :: "v"(f)); }
DI void aaddu(unsigned& a, unsigned d) { asm volatile("v_add_u32 %0, %1, %0" : "+v"(a) : "s"(d)); }
DI unsigned laneid() { unsigned l; asm volatile("v_mbcnt_lo_u32_b32 %0, -1, 0\n\tv_mbcnt_hi_u32_b32 %0, -1, %0" : "=v"(l)); return l; }
template <class T> DI void pin(T& v) { asm volatile("" : "+v"(v)); }


struct Ctx {
  rsrc_t rx, rw, rk, rv;
  unsigned lds0;
  int j, hs, blk0, rot, krot, w, id;
  const u16* wo; u16* out; char* ws; unsigned long long code;
};
template <int NB> struct St {
  f32x4 o[NB][8], negm[NB], S0[NB], S1[NB], l[NB];
  bf16x8 qf[NB][4], ones;
  u32x4 pf[NB];
  unsigned orv;
};
template <int NB, int E> DI void exp1(f32x4 (&S)[NB]) { if constexpr (E >= 0 && E < 4 * NB) S[E % NB][E / NB] = aexp(S[E % NB][E / NB]); }
template <int NB, int LO, int HI> DI void exps(f32x4 (&S)[NB]) { if constexpr (LO < HI) { exp1<NB, LO>(S); exps<NB, LO + 1, HI>(S); } }
DI float max8(f32x4 a, f32x4 b) { return fmaxf(fmaxf(fmaxf(a[0], a[1]), fmaxf(a[2], a[3])), fmaxf(fmaxf(b[0], b[1]), fmaxf(b[2], b[3]))); }

template <int NB, bool HAS_NEXT> DI void refix(St<NB>& s, rsrc_t rk, int ksoff) {
  const unsigned ln = laneid();
  const int kvo = (ln & 15) * 256 + ((((ln >> 4) & 1) << 1) | (ln >> 5)) * 16;
  asm volatile("s_nop 7");
#pragma unroll
  for (int nb = 0; nb < NB; ++nb) {
    f32x4 a = s.negm[nb], b = s.negm[nb];
#pragma unroll
    for (int st = 0; st < 4; ++st) {
      a = mfma(asb(__builtin_amdgcn_raw_buffer_load_b128(rk, kvo + st * 64, ksoff, 0)), s.qf[nb][st], a);
      SB();
      b = mfma(asb(__builtin_amdgcn_raw_buffer_load_b128(rk, kvo + st * 64, ksoff + 4096, 0)), s.qf[nb][st], b);
      SB();
    }
    const float d = fmaxf(xmax32(xmax16(max8(a, b))) + BIAS, 0.f);
    const float f = __builtin_amdgcn_exp2f(-d);
    s.negm[nb] -= d; if (HAS_NEXT) s.S0[nb] -= d;
    s.l[nb] *= f;
#pragma unroll
    for (int db = 0; db < 8; ++db) s.o[nb][db] *= f;
#pragma unroll
    for (int r = 0; r < 4; ++r) { a[r] = __builtin_amdgcn_exp2f(a[r] - d); b[r] = __builtin_amdgcn_exp2f(b[r] - d); }
    u32x4 w = {cvtpk(a[0], a[1]), cvtpk(a[2], a[3]), cvtpk(b[0], b[1]), cvtpk(b[2], b[3])};
    s.pf[nb] = w;
    SB();
  }
  asm volatile("s_nop 1");
}
template <int NB, bool HAS_NEXT> DI void check(St<NB>& s, const Ctx& c, int t) {
  if (__builtin_expect(__any((s.orv & 0x40004000u) != 0u), 0)) { asm volatile("" : "+s"(t)); refix<NB, HAS_NEXT>(s, c.rk, c.hs * 131072 + ((t + c.rot) & 15) * 8192); }
}
template <int... I, class F> DI void static_for(std::integer_sequence<int, I...>, F f) { (f(IC<I>{}), ...); }

template <int NB> DI void body(const Ctx& c) {
  const int lane = threadIdx.x & 63, l15 = lane & 15, g = lane >> 4;
  const unsigned lds0 = c.lds0;
  const f32x4 z4 = {0.f, 0.f, 0.f, 0.f};
  auto N0 = [] {};
  unsigned voffK[2], voffV[2];
#pragma unroll
  for (int h = 0; h < 2; ++h) {
    voffK[h] = (lane >> 4) * 256 + (((lane & 15) ^ (8 * (c.j & 1) + 4 * h + (lane >> 4))) * 16);
    voffV[h] = ((lane >> 1) & 7) * 256 + (lane >> 4) * 32 + (lane & 1) * 16 + (h ? 128 : 1024);
  }
  auto tso = [&](int t) { return (unsigned)(((t + c.rot) & 15) * 8192 + c.j * 2048); };
  auto dma_kv = [&](bool isv, unsigned lb0, unsigned lb1, unsigned so, int q) {
    if (q & 1) { if (isv) dmao2<1, 1024>(voffV[1], c.rv, so + (q >> 1) * 131072); else dmao2<1, 1024>(voffK[1], c.rk, so + (q >> 1) * 131072); }
    else { setm0(q ? lb1 : lb0); if (isv) dmao<1, 0>(voffV[0], c.rv, so + (q >> 1) * 131072); else dmao<1, 0>(voffK[0], c.rk, so + (q >> 1) * 131072); }
  };
  const unsigned jb = lds0 + c.j * 2048;
  auto ld_k = [&](int t, int q) { return __builtin_amdgcn_raw_buffer_load_b128(c.rk, voffK[q & 1] + (q & 1) * 1024, tso(t) + (q >> 1) * 131072, 2); };
  auto ld_v = [&](int t, int q) { return __builtin_amdgcn_raw_buffer_load_b128(c.rv, voffV[q & 1] + (q & 1) * 1024, tso(t) + (q >> 1) * 131072, 2); };
  St<NB> s;
  unsigned gdl = 0;
  auto gov = [&](unsigned ts) {
    if (c.w == 2) {
      unsigned now = (unsigned)__builtin_amdgcn_s_memrealtime();
      while (now - gdl < ts) { __builtin_amdgcn_s_sleep(1); now = (unsigned)__builtin_amdgcn_s_memrealtime(); }
      gdl = now;
    }
  };
  const unsigned long long epoch = __hip_atomic_load((gu64*)(c.ws + WS_SYNC), RLX_AGENT);
  u32x4 kvr[(NG - 3) * 4];
  {
    unsigned voffS;
    if constexpr (NB == 3) { const int f = (2 * (c.j & 1) + (lane >> 5)) | ((c.j >> 1) << 2); voffS = (lane >> 5) * 8192 + (((lane & 31) ^ (f << 1)) * 16); }
    else voffS = (lane >> 3) * 1280 + (((lane & 7) ^ (4 * (c.j & 1) + g)) * 16);
    const int wi0 = (c.j & 1) + 4 * (c.j >> 1);
    const unsigned voffS2 = voffS + 4 * 8192 - 2048;
    unsigned wso0 = (2 * c.krot * 32 + 2 * wi0) * 8192;
    auto dma_w = [&](int buf, int n) {
      if (n == 0) { setm0(lds0 + woff(buf) + wi0 * 1024); dmao<1, 0>(voffS, c.rw, wso0); }
      else if (n == 1) dmao2<1, 2048>(voffS2, c.rw, wso0);
      else if (n == 2) { setm0(lds0 + woff(buf) + wi0 * 1024 + 8192); dmao<1, 0>(voffS, c.rw, wso0 + 16 * 8192); }
      else { dmao2<1, 2048>(voffS2, c.rw, wso0 + 16 * 8192); wso0 += 32 * 8192; wso0 = wso0 >= 640u * 8192u ? wso0 - 640u * 8192u : wso0; pins(wso0); }
    };
    auto dma_x = [&](int ch, int n) {
      int kc = ch + c.krot; kc = kc >= 10 ? kc - 10 : kc;
      const int i = c.j + 4 * n;
      dma16<false>(lds0 + xoff(ch & 1) + i * 1024, voffS, c.rx, i * (8 * 1280) + kc * 128);
    };
    auto extra = [&](int m, int q) {
      if (m == 0 || m == 1) dma_kv(false, jb + kslot(m, 0), jb + kslot(m, 1), tso(m), q);
      else if (m == 2) dma_kv(true, jb + vslot(0, 0), jb + vslot(0, 1), tso(0), q);
      else if (m > 2 && m < NG) kvr[(m - 3) * 4 + q] = (m & 1) ? ld_k((m + 1) / 2, q) : ld_v((m - 2) / 2, q);
    };
    if constexpr (NB == 3) {
#pragma unroll
      for (int st = 0; st < 3; ++st)
#pragma unroll
        for (int n = 0; n < 4; ++n) dma_w(st, n);
    } else {
#pragma unroll
      for (int n = 0; n < 5; ++n) dma_x(0, n);
      {
        const rsrc_t rc = mkr((const void*)c.code, CODE_KB * 1024);
        asm volatile("s_mov_b32 m0, %0\n\ts_nop 0\n\tbuffer_load_dword %1, %2, %3 offen lds" :: "s"(lds0 + 38912), "v"(lane < 7 ? (unsigned)lane * 128u : OOB), "s"(rc), "s"(((c.id >> 3) * 4 + c.j) * 896) : "memory");
      }
      __builtin_amdgcn_s_setprio(1);
    }
    unsigned wa[8], xa[2];
    const int qq = l15 >> 2, p = lane & 3, f = qq | ((g & 1) << 2);
#pragma unroll
    for (int db = 0; db < 8; ++db) wa[db] = lds0 + woff(0) + (8 * g + qq) * 512 + c.hs * 256 + ((db ^ f) * 32) + p * 8;
#pragma unroll
    for (int u = 0; u < 2; ++u) xa[u] = lds0 + xoff(0) + (c.blk0 * 16 + l15) * 128 + (((4 * u + g) ^ ((l15 >> 1) & 7)) * 16);
    bf16x8 w4, w5, w6, w7, xp[NB];
    auto qstep = [&](auto U, auto PH, auto D) {
      constexpr int u = decltype(U)::value, ph = decltype(PH)::value;
      auto rw = [&](int db) { const unsigned a = wa[db] + (woff(u) - woff(0)); return cat(ldtr(a), ldtr(a + 2048)); };
      auto rx = [&](int nb) { return ld128(xa[u & 1] + (u >> 1) * 40960 + nb * 2048); };
      auto mm = [&](auto Z, int db, bf16x8& wf, bf16x8 (&xf)[NB], auto g0, auto g1, auto g2) {
        constexpr int z = decltype(Z)::value;
        if constexpr (z == 2) { g0(); g1(); g2(); SB(); }
        else {
          amf<z == 1>(s.o[0][db], wf, xf[0]); SB(); g0(); SB();
          amf<z == 1>(s.o[1][db], wf, xf[1]); SB(); g1(); SB();
          if constexpr (NB == 3) { amf<z == 1>(s.o[2][db], wf, xf[2]); SB(); }
          g2(); SB();
        }
      };
      constexpr IC<ph == 0 ? 2 : ph == 1 ? 1 : 0> H{};
      constexpr IC<ph == 0 ? 1 : 0> C{};
      bf16x8 xf[NB], w0, w1, w2, w3;
      if constexpr (NB == 3) {
        mm(H, 4, w4, xp, [&] { w0 = rw(0); }, [&] { xf[0] = rx(0); xf[1] = rx(1); }, [&] { xf[2] = rx(2); });
        mm(H, 5, w5, xp, [&] { w1 = rw(1); }, [&] { D(0); }, N0);
        mm(H, 6, w6, xp, [&] { w2 = rw(2); }, [&] { w3 = rw(3); }, N0);
        mm(H, 7, w7, xp, [&] { w4 = rw(4); }, [&] { D(1); }, [&] { use(xf[NB - 1]); });
        mm(C, 0, w0, xf, [&] { w5 = rw(5); }, [&] { use(w1); w6 = rw(6); }, N0);
        mm(C, 1, w1, xf, [&] { w7 = rw(7); }, [&] { use(w2); D(2); }, N0);
        mm(C, 2, w2, xf, [&] { use(w3); }, [&] { D(3); }, N0);
        mm(C, 3, w3, xf, [&] { use(w7); }, N0, N0);
      } else {
        mm(H, 4, w4, xp, [&] { w0 = rw(0); }, [&] { xf[0] = rx(0); xf[1] = rx(1); }, N0);
        mm(H, 5, w5, xp, [&] { w1 = rw(1); }, [&] { D(0); }, N0);
        mm(H, 6, w6, xp, [&] { w2 = rw(2); }, [&] { w3 = rw(3); D(1); }, N0);
        mm(H, 7, w7, xp, [&] { w4 = rw(4); }, [&] { D(2); use(xf[NB - 1]); }, N0);
        mm(C, 0, w0, xf, [&] { w5 = rw(5); }, [&] { use(w1); w6 = rw(6); D(3); }, N0);
        mm(C, 1, w1, xf, [&] { w7 = rw(7); }, [&] { use(w2); D(4); }, N0);
        mm(C, 2, w2, xf, [&] { use(w3); }, N0, N0);
        mm(C, 3, w3, xf, [&] { use(w7); }, N0, N0);
      }
#pragma unroll
      for (int nb = 0; nb < NB; ++nb) xp[nb] = xf[nb];
    };
    SB();
    if constexpr (NB == 3) {
      static_for(std::make_integer_sequence<int, 4>{}, [&](auto U) {
        constexpr int u = decltype(U)::value;
        wait_vm_bar<8>(); qstep(U, IC<(u < 2 ? u : 2)>{}, [&](int n) { dma_w((u + 3) & 3, n); });
      });
#pragma unroll 1
      for (int s4 = 4; s4 < 16; s4 += 4)
        static_for(std::make_integer_sequence<int, 4>{}, [&](auto U) {
          constexpr int u = decltype(U)::value;
          wait_vm_bar<8>(); qstep(U, IC<2>{}, [&](int n) { dma_w((u + 3) & 3, n); });
        });
      wait_vm_bar<8>(); qstep(IC<0>{}, IC<2>{}, [&](int n) { dma_w(3, n); });
      wait_vm_bar<8>(); qstep(IC<1>{}, IC<2>{}, [&](int) {});
      wait_vm_bar<4>(); qstep(IC<2>{}, IC<2>{}, [&](int) {});
      wait_vm_bar<0>(); qstep(IC<3>{}, IC<2>{}, [&](int) {});
    } else {
      static_for(std::make_integer_sequence<int, 20>{}, [&](auto ST) {
        constexpr int st = decltype(ST)::value, mp = (st >> 1) - 1 - KVN0;
        gov(GOVQ);
        if constexpr (st & 1) wait_vm_bar<63>(); else wait_vm_bar<st == 0 ? 1 : (mp >= 0 && mp < NG) ? 4 : 0>();
        qstep(IC<(st & 3)>{}, IC<(st < 2 ? st : 2)>{}, [&](int n) {
          if constexpr (st & 1) { if (n < 4) extra((st >> 1) - KVN0, n); } else if constexpr ((st >> 1) + 1 < 10) dma_x((st >> 1) + 1, n); });
      });
    }
#pragma unroll
    for (int nb = 0; nb < NB; ++nb) { amfma(s.o[nb][4], w4, xp[nb]); amfma(s.o[nb][5], w5, xp[nb]); amfma(s.o[nb][6], w6, xp[nb]); amfma(s.o[nb][7], w7, xp[nb]); }
    asm volatile("s_nop 7");
    SB();
  }
  auto qconv = [&] {
#pragma unroll
    for (int nb = 0; nb < NB; ++nb)
#pragma unroll
      for (int e = 0; e < 4; ++e) {
        f32x4 a0 = s.o[nb][2 * e], a1 = s.o[nb][2 * e + 1];
#pragma unroll
        for (int r = 0; r < 4; ++r) { a0[r] = amul(a0[r], QSCALE); a1[r] = amul(a1[r], QSCALE); }
        auto r0 = __builtin_amdgcn_permlane16_swap(cvtpk(a0[0], a0[1]), cvtpk(a1[0], a1[1]), false, false);
        auto r1 = __builtin_amdgcn_permlane16_swap(cvtpk(a0[2], a0[3]), cvtpk(a1[2], a1[3]), false, false);
        u32x4 w = {r0[0], r1[0], r0[1], r1[1]};
        s.qf[nb][e] = asb(w); pin(s.qf[nb][e]);
      }
  };
  if constexpr (NB == 2) qconv();
  wait_vm_bar<0>();
  if constexpr (NB == 2) pin(kvr[(NG - 3) * 4 - 1]);
  const unsigned long long tag = (((unsigned long long)__builtin_amdgcn_readfirstlane((unsigned)(epoch >> 32)) << 32) | __builtin_amdgcn_readfirstlane((unsigned)epoch)) + 1ull;
  const int pg = ((g & 1) << 1) | (g >> 1);
  unsigned ka[4];
#pragma unroll
  for (int st = 0; st < 4; ++st) ka[st] = lds0 + kslot(0, c.hs) + l15 * 256 + (((4 * st + pg) ^ l15) * 16);
  unsigned va = lds0 + vslot(0, c.hs) + (g >> 1) * 2048 + (4 * (g & 1) + (l15 >> 2)) * 32 + (lane & 3) * 8;
  auto rks = [&](int sl, int kt, int st) { return ld128(ka[st] + sl * 8192 + kt * 4096); };
  auto rvs = [&](int sl, int db) { const unsigned a = va + sl * 8192 + (db >> 2) * 1024 + (db & 3) * 256; return cat(ldtr(a), ldtr(a + 4096)); };
  {
    bf16x8 kf0[4], kf1[4];
#pragma unroll
    for (int st = 0; st < 4; ++st) { kf0[st] = rks(0, 0, st); kf1[st] = rks(0, 1, st); }
    if constexpr (NB == 3) qconv();
#pragma unroll
    for (int st = 0; st < 4; ++st)
#pragma unroll
      for (int nb = 0; nb < NB; ++nb) {
        s.S0[nb] = mfma(kf0[st], s.qf[nb][st], st == 0 ? z4 : s.S0[nb]); s.S1[nb] = mfma(kf1[st], s.qf[nb][st], st == 0 ? z4 : s.S1[nb]);
      }
#pragma unroll
    for (int nb = 0; nb < NB; ++nb) {
#pragma unroll
      for (int db = (NB == 3 ? 6 : 5); db < 8; ++db) { s.o[nb][db] = z4; pin(s.o[nb][db]); }
      const float m = xmax32(xmax16(max8(s.S0[nb], s.S1[nb]))) + BIAS;
      s.negm[nb] = z4 - m; s.S0[nb] -= m; s.S1[nb] -= m;
      pin(s.negm[nb]);
    }
    s.ones = asb(u32x4{0x3f803f80u, 0x3f803f80u, 0x3f803f80u, 0x3f803f80u});
    pin(s.ones);
  }
  exps<NB, 0, 4 * NB>(s.S0); exps<NB, 0, 2 * NB>(s.S1);
  bf16x8 v5h, v6, v7;
  SB();
  auto us = [&](auto m, auto g0, auto g1, auto g2) {
    m(0); SB(); g0(); SB(); m(1); SB(); g1(); SB();
    if constexpr (NB == 3) { m(2); SB(); g2(); SB(); }
  };
  auto QK0 = [&](int st, bf16x8 fr) { return [&s, st, fr](int nb) { if (st == 0) amfma_c(s.S0[nb], fr, s.qf[nb][0], s.negm[nb]); else amfma(s.S0[nb], fr, s.qf[nb][st]); }; };
  auto QK1 = [&](int st, bf16x8 fr) { return [&s, st, fr](int nb) { if (st == 0) amfma_c(s.S1[nb], fr, s.qf[nb][0], s.negm[nb]); else amfma(s.S1[nb], fr, s.qf[nb][st]); }; };
  auto PVH = [&](int db, bf16x8& fr) { return [&s, db, &fr](int nb) { amfma(s.o[nb][db], fr, asb(s.pf[nb])); }; };
  auto N = [] {};
#define E0(e) [&] { exp1<NB, (e)>(s.S0); }
#define E1(e) [&] { exp1<NB, (e)>(s.S1); }
#define CLO(nb) [&] { if constexpr ((nb) < NB) { s.pf[nb][0] = acvt(s.S0[nb][0], s.S0[nb][1]); s.pf[nb][1] = acvt(s.S0[nb][2], s.S0[nb][3]); } }
#define CHI(nb) [&] { if constexpr ((nb) < NB) { s.pf[nb][2] = acvt(s.S1[nb][0], s.S1[nb][1]); s.pf[nb][3] = acvt(s.S1[nb][2], s.S1[nb][3]); } }
#define U(f, ...) [&] { use(f); SB(); (__VA_ARGS__)(); }
  unsigned dk = 8192, dv = 8192, cnt = 1;
  if constexpr (NB == 3) {
#pragma unroll
    for (int st = 0; st < 4; ++st) ka[st] += 8192;
  }
  auto astep = [&](auto FI, auto SL, int t, auto kvh) {
    constexpr bool FIRST = decltype(FI)::value;
    constexpr int sl = decltype(SL)::value;
    auto rk = [&](int kt, int st) { return rks(NB == 3 ? 0 : sl, kt, st); };
    auto rv = [&](int db) { return rvs(NB == 3 ? 0 : (sl + 4) % 5, db); };
    if constexpr (NB == 2) { gov(GOVA); wait_vm_bar<6>(); } else wait_vm_bar<63>();
    auto PH = [&](int db, bf16x8& fr) { return [&, db](int nb) { if constexpr (!FIRST) amfma(s.o[nb][db], fr, asb(s.pf[nb])); }; };
    auto PV = [&](int db, bf16x8 fr) { return [&s, db, fr](int nb) { amf<FIRST>(s.o[nb][db], fr, asb(s.pf[nb])); }; };
    auto SUM = [&](int nb) { amf<FIRST>(s.l[nb], s.ones, asb(s.pf[nb])); };
    bf16x8 a0, a1, a2, a3, b0, b1, b2, b3, v0, v1, v2, v3, v4;
    unsigned o1, o2;
    if constexpr (NB == 3) {
      bf16x8 v5;
      us(PH(6, v6), [&] { a0 = rk(0, 0); a1 = rk(0, 1); }, [&] { a2 = rk(0, 2); a3 = rk(0, 3); }, E1(6));
      us(PH(7, v7), CLO(0), CLO(1), U(a0, CLO(2)));
      us(QK0(0, a0), [&] { b0 = rk(1, 0); b1 = rk(1, 1); }, E1(7), U(a1, E1(8)));
      us(QK0(1, a1), E1(9), E1(10), U(a2, E1(11)));
      us(QK0(2, a2), [&] { o1 = aor3(s.pf[0][0], s.pf[0][1], s.pf[1][0]); o2 = aor3(s.pf[1][1], s.pf[2][0], s.pf[2][1]); }, CHI(0), U(a3, CHI(1)));
      us(QK0(3, a3), CHI(2), [&] { o1 = aor3(o1, s.pf[0][2], s.pf[0][3]); o2 = aor3(o2, s.pf[1][2], s.pf[1][3]); },
                     U(b0, [&] { o1 = aor3(o1, s.pf[2][2], s.pf[2][3]); s.orv = o1 | o2; }));
      if constexpr (!FIRST) check<NB, true>(s, c, t - 1);
      SB();
      us(QK1(0, b0), [&] { b2 = rk(1, 2); b3 = rk(1, 3); }, [&] { v0 = rv(0); }, U(b1, [&] { aaddu(ka[0], dk); aaddu(ka[1], dk); }));
      us(QK1(1, b1), [&] { v1 = rv(1); }, E0(0), U(b2, E0(1)));
      us(QK1(2, b2), [&] { v2 = rv(2); }, E0(2), U(b3, E0(3)));
      us(QK1(3, b3), [&] { v3 = rv(3); }, E0(4), E0(5));
      us(SUM, E0(6), E0(7), U(v0, E0(8)));
      us(PV(0, v0), [&] { v4 = rv(4); }, E0(9), U(v1, E0(10)));
      us(PV(1, v1), [&] { v5 = rv(5); }, E0(11), U(v2, E1(0)));
      us(PV(2, v2), [&] { v6 = rv(6); }, E1(1), U(v3, E1(2)));
      us(PV(3, v3), [&] { v7 = rv(7); }, E1(3), U(v4, E1(4)));
      us(PV(4, v4), E1(5), [&] { aaddu(ka[2], dk); aaddu(ka[3], dk); }, U(v5, [&] { aaddu(va, dv); }));
      us(PV(5, v5), [&] { dv = dk; cnt = cnt == 4 ? 0 : cnt + 1; pins(cnt); }, [&] { use(v7); dk = cnt == 4 ? -32768 : 8192; pins(dk); }, N);
    } else {
      us(PH(5, v5h), [&] { a0 = rk(0, 0); a1 = rk(0, 1); }, [&] { a2 = rk(0, 2); a3 = rk(0, 3); }, N);
      us(PH(6, v6), E1(4), [&] { kvh(0); }, N);
      us(PH(7, v7), CLO(0), U(a0, CLO(1)), N);
      us(QK0(0, a0), [&] { b0 = rk(1, 0); b1 = rk(1, 1); }, U(a1, E1(5)), N);
      us(QK0(1, a1), E1(6), U(a2, E1(7)), N);
      us(QK0(2, a2), [&] { o1 = aor3(s.pf[0][0], s.pf[0][1], s.pf[1][0]); kvh(1); }, U(a3, CHI(0)), N);
      us(QK0(3, a3), CHI(1), U(b0, [&] { o1 = aor3(o1, s.pf[1][1], s.pf[0][2]); o2 = aor3(s.pf[0][3], s.pf[1][2], s.pf[1][3]); s.orv = o1 | o2; }), N);
      if constexpr (!FIRST) check<NB, true>(s, c, t - 1);
      SB();
      us(QK1(0, b0), [&] { b2 = rk(1, 2); b3 = rk(1, 3); }, U(b1, [&] { v0 = rv(0); kvh(2); }), N);
      us(QK1(1, b1), [&] { v1 = rv(1); }, U(b2, [&] { kvh(3); }), N);
      us(QK1(2, b2), [&] { v2 = rv(2); }, U(b3, E0(0)), N);
      us(QK1(3, b3), [&] { v3 = rv(3); }, [&] { exp1<NB, 1>(s.S0); kvh(4); }, N);
      us(SUM, E0(2), U(v0, E0(3)), N);
      us(PV(0, v0), [&] { v4 = rv(4); }, U(v1, [&] { exp1<NB, 4>(s.S0); kvh(5); }), N);
      us(PV(1, v1), [&] { v5h = rv(5); }, U(v2, E0(5)), N);
      us(PV(2, v2), [&] { v6 = rv(6); }, U(v3, [&] { exp1<NB, 6>(s.S0); kvh(6); }), N);
      us(PV(3, v3), [&] { v7 = rv(7); exp1<NB, 7>(s.S0); }, U(v4, [&] { exp1<NB, 0>(s.S1); kvh(7); }), N);
      us(PV(4, v4), [&] { exp1<NB, 1>(s.S1); exp1<NB, 2>(s.S1); }, [&] { exp1<NB, 3>(s.S1); use(v7); }, N);
    }
  };
  auto piece = [&](int t, int n) {
    const int p0 = (PP2 * (t - 1) + 1) / 2, p = p0 + n;
    if (p < (PP2 * t + 1) / 2 && p < (32 - NG) * 4) {
      const int it = NG + (p >> 2), h = (p >> 1) & 1, v = it == 31 || !(it & 1), tn = it == 31 ? 15 : v ? (it - 2) / 2 : (it + 1) / 2;
      const unsigned so = tso(tn) + h * 131072;
      if (p & 1) { wait_vm<THR>(); dmao2<1, 1024>(v ? voffV[1] : voffK[1], v ? c.rv : c.rk, so); }
      else { setm0(jb + (v ? vslot(tn % 5, h) : kslot(tn % 5, h))); wait_vm<THR>(); dmao2<1, 0>(v ? voffV[0] : voffK[0], v ? c.rv : c.rk, so); }
    }
  };
  astep(std::true_type{}, IC<1>{}, 1, [&](int n) { if constexpr (NB == 2) {
    piece(1, n);
#pragma unroll
    for (int i = 0; i < (NG - 3) * 4; ++i) if (i * 8 / ((NG - 3) * 4) == n) { const int m = i >> 2, q = i & 3;
      st128(jb + lane * 16 + ((m & 1) ? vslot(1 + (m >> 1), q >> 1) : kslot(2 + (m >> 1), q >> 1)) + (q & 1) * 1024, kvr[i]); } } });
  constexpr std::false_type F{};
  if constexpr (NB == 3) {
    auto NH = [](int) {};
#pragma unroll 1
    for (int t = 2; t < 16; ++t) astep(F, IC<0>{}, t, NH);
  } else {
    static_for(std::make_integer_sequence<int, 12>{}, [&](auto TT) {
      constexpr int t = decltype(TT)::value + 2;
      astep(F, IC<t % 5>{}, t, [&](int n) { piece(t, n); });
    });
  }
  rsrc_t rof, rwo, rslab; int pr, hg, rotb; gu64* sync; u16* ob;
  {
    int id = c.id; asm volatile("" : "+s"(id));
    const int br = (id >> 2) & 1, b = id >> 5;
    hg = id & 3; pr = (id >> 3) & 3; rotb = b & 3;
    rof = mkr(c.ws + (size_t)((br * 8 + b) * 4 + hg) * 327680, 327680);
    rwo = mkr((const char*)(c.wo + (size_t)hg * 1024 * 640 + pr * 160) - 4096, 1024 * 1280 - pr * 320 + 4096);
    rslab = mkr(c.ws + WS_SLAB + (size_t)((br * 8 + b) * 4 + pr) * (12 * QB), 12 * QB);
    sync = (gu64*)(c.ws + WS_SYNC);
    ob = c.out + (size_t)(b * 160 + (hg & 1) * 80) * 1280 + br * 640 + pr * 160 + (hg >> 1) * 80;
  }
  unsigned lz = 0;
  const int dks = c.j >> 1, dh = c.j & 1;
  auto stg = [](int u) { return u * 40960; };
  auto kpair = [&](int i) { return ((pr + (i >> 2)) & 3) * 4 + ((i + rotb) & 3); };
  auto wvo = [&](int n) { const int e = 64 * n + (int)lz, row = e / 20, ch = e - row * 20; return row * 1280 + ((ch ^ (((row >> 2) & 1) << 1)) * 16) + 4096; };
  auto wso = [&](int i) { return (kpair(i) * 64 + dks * 32 + dh * 16) * 1280; };
  auto wld = [&](int i, int n) { return lds0 + stg(i & 3) + 20480 + dks * 10240 + dh * 5120 + n * 1024; };
  u32x4 wr[15];
  if constexpr (NB == 2) {
    lz = laneid();
    unsigned vw[5];
#pragma unroll
    for (int n = 0; n < 5; ++n) vw[n] = wvo(n);
    auto wl = [&](int p) { wait_vm<4>(); if (p < 15) wr[p] = __builtin_amdgcn_raw_buffer_load_b128(rwo, vw[p % 5], wso(p / 5), 0); };
    SB();
    astep(F, IC<4>{}, 14, [&](int n) { wl(n); });
    astep(F, IC<0>{}, 15, [&](int n) { wl(8 + n); });
  }
  if constexpr (NB == 3) wait_vm_bar<63>(); else wait_vm_bar<15>();
  u32x4 ow[NB][4];
  unsigned gb;
  auto ost = [&](int e, int nb) { __builtin_amdgcn_raw_buffer_store_b128(ow[nb][e], rof, gb + e * 10240 + nb * 1024, 0, 16); };
  {
    bf16x8 vf[8];
#pragma unroll
    for (int db = 0; db < 4; ++db) vf[db] = rvs(0, db);
    SB();
    if constexpr (NB == 2) us(PVH(5, v5h), N, N, N);
    us(PVH(6, v6), N, N, N);
    exps<NB, 2 * NB, 4 * NB>(s.S1);
    us(PVH(7, v7), CLO(0), CLO(1), CLO(2));
    CHI(0)(); CHI(1)(); CHI(2)();
    s.orv = 0;
#pragma unroll
    for (int nb = 0; nb < NB; ++nb) { s.orv = aor3(s.orv, s.pf[nb][0], s.pf[nb][1]); s.orv = aor3(s.orv, s.pf[nb][2], s.pf[nb][3]); }
    SB();
    check<NB, false>(s, c, 15);
    SB();
    if constexpr (NB == 3) lz = laneid();
#pragma unroll
    for (int db = 4; db < 8; ++db) vf[db] = rvs(0, db);
    gb = lz * 16 + (pr * 8 + c.hs * 4) * 10240 + c.blk0 * 1024;
#pragma unroll
    for (int nb = 0; nb < NB; ++nb) s.l[nb] = mfma(s.ones, asb(s.pf[nb]), s.l[nb]);
    float inv[NB];
    auto norm = [&](int e, int nb) {
      f32x4 a0 = s.o[nb][2 * e], a1 = s.o[nb][2 * e + 1];
#pragma unroll
      for (int r = 0; r < 4; ++r) { float x = a0[r] * inv[nb], y = a1[r] * inv[nb]; pin(x); pin(y); a0[r] = x; a1[r] = y; }
      ow[nb][e] = u32x4{cvtpk(a0[0], a0[1]), cvtpk(a0[2], a0[3]), cvtpk(a1[0], a1[1]), cvtpk(a1[2], a1[3])};
      if (e < EPRE) ost(e, nb);
    };
#pragma unroll
    for (int e = 0; e < 4; ++e) {
#pragma unroll
      for (int nb = 0; nb < NB; ++nb) {
        s.o[nb][2 * e] = mfma(vf[2 * e], asb(s.pf[nb]), s.o[nb][2 * e]);
        s.o[nb][2 * e + 1] = mfma(vf[2 * e + 1], asb(s.pf[nb]), s.o[nb][2 * e + 1]);
        if (e == 0) inv[nb] = __builtin_amdgcn_rcpf(s.l[nb][0]); else norm(e - 1, nb);
        if constexpr (NB == 2) { const int p = (e * 2 + nb) * 2; st128(wld(p / 5, p % 5) + lz * 16, wr[p]); if (p + 1 < 15) st128(wld((p + 1) / 5, (p + 1) % 5) + lz * 16, wr[p + 1]); }
        SB();
      }
    }
#pragma unroll
    for (int nb = 0; nb < NB; ++nb) norm(3, nb);
  }
  asm volatile("s_waitcnt lgkmcnt(0)\n\ts_barrier" ::: "memory");
#pragma unroll
  for (int eh = 0; eh < 2; ++eh) {
    const unsigned lb = lds0 + stg((c.hs * 2 + eh - rotb) & 3) + c.blk0 * 1024 + lz * 16;
#pragma unroll
    for (int el = 0; el < 2; ++el)
#pragma unroll
      for (int nb = 0; nb < NB; ++nb) st128(lb + el * 10240 + nb * 1024, ow[nb][eh * 2 + el]);
  }
  {
    constexpr int NM = NB, M0 = NM == 3 ? 0 : 3;
    const int q = c.j, qm = q & 1, qn = q >> 1, zg = lz >> 4;
    unsigned vo3[5];
#pragma unroll
    for (int n = 0; n < 5; ++n) vo3[n] = NB == 3 ? wvo(n) - (n & 3) * 1024 : lz * 16 + (n >> 2) * 4096;
    const unsigned lb3 = lds0 + dks * 10240 + dh * 5120 + (NB == 3 ? 20480 : 0);
    auto so3 = [&](int i) { unsigned v = (NB == 3 ? wso(i) : (kpair(i) * 2 + dks) * 10240 + dh * 5120) + (unsigned)(i >> 4) * OOB; pins(v); return v; };
    auto dmal = [&](unsigned so, int u, int n) {
      constexpr int POL = NB == 3 ? 0 : 2;
      const rsrc_t r = NB == 3 ? rwo : rof;
      const unsigned vo = vo3[NB == 3 ? n : (n >> 2) * 4];
      if (n == 0) { setm0(lb3 + stg(u)); dmao<POL, 0>(vo, r, so); }
      else if (n == 1) dmao2<POL, 1024>(vo, r, so);
      else if (n == 2) dmao2<POL, 2048>(vo, r, so);
      else if (n == 3) dmao2<POL, 3072>(vo, r, so);
      else { setm0(lb3 + stg(u) + 4096); dmao<POL, 0>(vo, r, so); }
    };
    if constexpr (NB == 3) {
      const unsigned so = so3(3);
#pragma unroll
      for (int n = 0; n < 5; ++n) dmal(so, 3, n);
    }
    unsigned oa[2], wa[2][2];
    {
      oa[0] = lds0 + (qm * 5 + M0) * 1024 + lz * 16; oa[1] = oa[0] + 81920;
      const unsigned wb = lds0 + 20480 + (4 * zg + ((lz & 15) >> 2)) * 320 + (lz & 3) * 8 + qn * 160;
      const int sg = (qn ? -32 : 32) * (zg & 1);
      wa[0][0] = wb + sg; wa[0][1] = wb - sg; wa[1][0] = wa[0][0] + 81920; wa[1][1] = wa[0][1] + 81920;
      pin(oa[1]); pin(wa[1][0]); pin(wa[1][1]);
    }
    f32x4 acc[NM][5];
    bf16x8 h2, h3, h4, op[NM];
    auto interval = [&](auto UU, auto D, auto tail) {
      constexpr bool FIRST = decltype(UU)::value < 0;
      constexpr int u = FIRST ? 0 : decltype(UU)::value, so = (u & 1) * 40960;
      auto ro = [&](int ks, int m) { return ld128(oa[u >> 1] + so + ks * 10240 + m * 1024); };
      auto rw = [&](int ks, int n) { const unsigned a = wa[u >> 1][n & 1] + so + ks * 10240 + n * 32; return cat(ldtr(a), ldtr(a + 5120)); };
      auto mm = [&](auto Z, int n, bf16x8& wf, bf16x8 (&of)[NM], auto g0, auto g1, auto g2) {
        constexpr int z = decltype(Z)::value;
        if constexpr (z == 2) { g0(); g1(); g2(); SB(); }
        else {
          amf<z == 1>(acc[0][n], wf, of[0]); SB(); g0(); SB(); amf<z == 1>(acc[1][n], wf, of[1]); SB(); g1(); SB();
          if constexpr (NM == 3) { amf<z == 1>(acc[2][n], wf, of[2]); SB(); }
          g2(); SB();
        }
      };
      constexpr IC<FIRST ? 2 : 0> H{};
      constexpr IC<FIRST ? 1 : 0> C{};
      constexpr IC<0> P{};
      bf16x8 o0[NM], o1[NM], a0, a1, a2, a3, a4, b0, b1;
      mm(H, 2, h2, op, [&] { a0 = rw(0, 0); }, [&] { o0[0] = ro(0, 0); o0[1] = ro(0, 1); }, [&] { if constexpr (NM == 3) o0[2] = ro(0, 2); });
      mm(H, 3, h3, op, [&] { a1 = rw(0, 1); }, [&] { a2 = rw(0, 2); }, [&] { D(0); });
      mm(H, 4, h4, op, [&] { a3 = rw(0, 3); }, [&] { a4 = rw(0, 4); }, [&] { use(o0[NM - 1]); });
      mm(C, 0, a0, o0, [&] { o1[0] = ro(1, 0); o1[1] = ro(1, 1); }, [&] { use(a1); if constexpr (NM == 3) o1[2] = ro(1, 2); }, [&] { D(1); });
      mm(C, 1, a1, o0, [&] { b0 = rw(1, 0); }, [&] { use(a2); b1 = rw(1, 1); }, N0);
      mm(C, 2, a2, o0, [&] { h2 = rw(1, 2); }, [&] { use(a3); }, [&] { D(2); });
      mm(C, 3, a3, o0, [&] { h3 = rw(1, 3); }, [&] { use(a4); }, N0);
      mm(C, 4, a4, o0, [&] { h4 = rw(1, 4); }, [&] { use(b0); }, [&] { D(3); });
      mm(P, 0, b0, o1, N0, [&] { use(b1); }, [&] { D(4); });
      mm(P, 1, b1, o1, [&] { use(h4); }, N0, N0);
      tail();
#pragma unroll
      for (int m = 0; m < NM; ++m) op[m] = o1[m];
    };
    auto ND = [](int) {};
    SB();
    auto poll = [&](int base) {
      const int pid = c.id ^ (((lz & 3) ? (lz & 3) : 1) << 3);
      for (unsigned spins = 0; spins < SPINS; ++spins) {
        if (__all(__hip_atomic_load(sync + (base + pid) * FS, RLX_AGENT) == tag)) break;
        __builtin_amdgcn_s_sleep(1);
      }
    };
    wait_vm_bar<63>();
    interval(IC<-1>{}, [&](int n) {
#pragma unroll
      for (int k = 0; k < (4 - EPRE) * NB; ++k) if (k * 5 / (EPRE < 4 ? (4 - EPRE) * NB : 1) == n) ost(EPRE + k / NB, k % NB); }, N0);
    wait_vm_bar<63>();
    { const unsigned so = so3(4); interval(IC<1>{}, [&](int n) { if constexpr (NB == 3) dmal(so, 0, n); }, N0); }
    if constexpr (NB == 3) wait_vm_bar<5>(); else wait_vm_bar<0>();
    if (c.w == 3 && lz == 0) __hip_atomic_store(sync + (1 + c.id) * FS, tag, RLX_AGENT);
    const unsigned so5 = so3(5);
    interval(IC<2>{}, [&](int n) { if constexpr (NB == 3) dmal(so5, 1, n); }, [&] { if (c.w == 2) poll(1); });
    if constexpr (NB == 3) wait_vm_bar<10>(); else wait_vm_bar<63>();
    { const unsigned sa = so3(NB == 3 ? 6 : 4);
      interval(IC<3>{}, [&](int n) { if constexpr (NB == 3) dmal(sa, 2, n);
        else { if (n == 0) { dmal(sa, 0, 0); dmal(sa, 0, 1); } else if (n == 1) { dmal(sa, 0, 2); dmal(sa, 0, 3); } else if (n == 2) { dmal(sa, 0, 4); dmal(so5, 1, 0); }
               else if (n == 3) { dmal(so5, 1, 1); dmal(so5, 1, 2); } else { dmal(so5, 1, 3); dmal(so5, 1, 4); } } }, N0); }
    unsigned son = so3(NB == 3 ? 7 : 6);
#pragma unroll 1
    for (int i4 = 4; i4 < 16; i4 += 4)
      static_for(std::make_integer_sequence<int, 4>{}, [&](auto UU) {
        constexpr int u = decltype(UU)::value;
        if constexpr (NB == 3) wait_vm_bar<10>(); else wait_vm_bar<5>();
        interval(UU, [&](int n) { dmal(son, (u + (NB == 3 ? 3 : 2)) & 3, n); if (n == 4) son = so3(i4 + u + (NB == 3 ? 4 : 3)); }, N0);
      });
#pragma unroll
    for (int m = 0; m < NM; ++m) { amfma(acc[m][2], h2, op[m]); amfma(acc[m][3], h3, op[m]); amfma(acc[m][4], h4, op[m]); }
    asm volatile("s_nop 7");
    SB();
    if (q != hg) {
      const int so = (q * 3 + ((hg - q - 1) & 3)) * QB;
#pragma unroll
      for (int e = 0; e < NM * 5; e += 2) {
        const f32x4 a = acc[e / 5][e % 5], b = e + 1 < NM * 5 ? acc[(e + 1) / 5][(e + 1) % 5] : z4;
        __builtin_amdgcn_raw_buffer_store_b128(u32x4{cvtpk(a[0], a[1]), cvtpk(a[2], a[3]), cvtpk(b[0], b[1]), cvtpk(b[2], b[3])}, rslab, lz * 16 + ((M0 ? 8 : 0) + e / 2) * 1024 + so, 0, 16);
      }
    }
    asm volatile("s_waitcnt vmcnt(0) lgkmcnt(0)\n\ts_barrier" ::: "memory");
    if (c.w == 0 && lz == 0) __hip_atomic_store(sync + (257 + c.id) * FS, tag, RLX_AGENT);
    if (q == hg) {
#pragma unroll
      for (int e = 0; e < NM * 5; ++e) st128(lds0 + lz * 16 + (M0 * 5 + e) * 1024, __builtin_bit_cast(u32x4, acc[e / 5][e % 5]));
    }
    if (c.w == ((hg + 1) & 3)) {
      const int pid = c.id ^ ((lz & 3) ? (lz & 3) : 1);
      for (unsigned spins = 0; spins < SPINS; ++spins) {
        if (__all(__hip_atomic_load(sync + (257 + pid) * FS, RLX_AGENT) == tag)) break;
        __builtin_amdgcn_s_sleep(1);
      }
    }
    if (c.id == 0 && c.w == 7) {
      for (unsigned spins = 0; spins < SPINS; ++spins) {
        bool ok = true;
#pragma unroll
        for (int k = 0; k < 4; ++k) ok &= __hip_atomic_load(sync + (1 + lz + 64 * k) * FS, RLX_AGENT) == tag;
        if (__all(ok)) break;
        __builtin_amdgcn_s_sleep(1);
      }
      if (lz == 0) __hip_atomic_store(sync, tag, RLX_AGENT);
    }
    asm volatile("s_waitcnt vmcnt(0) lgkmcnt(0)\n\ts_barrier" ::: "memory");
    u16* op16 = ob + (size_t)(lz & 15) * 1280 + 4 * zg;
    u32x4 r[2][3];
#pragma unroll
    for (int k = 0; k < 2; ++k)
#pragma unroll
      for (int sl = 0; sl < 3; ++sl) r[k][sl] = __builtin_amdgcn_raw_buffer_load_b128(rslab, c.w + 8 * k < 13 ? lz * 16 + (c.w + 8 * k) * 1024 : OOB, (hg * 3 + sl) * QB, 16);
#pragma unroll
    for (int k = 0; k < 2; ++k) {
      const int un = c.w + 8 * k;
#pragma unroll
      for (int hh = 0; hh < 2; ++hh) {
        const int e = un < 8 ? 2 * un + hh : 15 + 2 * (un - 8) + hh;
        if (un < 13 && !(un == 7 && hh == 1)) {
          f32x4 v = *(const LDS3 f32x4*)(size_t)(lds0 + lz * 16 + e * 1024);
#pragma unroll
          for (int sl = 0; sl < 3; ++sl) { const unsigned lo = r[k][sl][2 * hh], hi = r[k][sl][2 * hh + 1];
            v += f32x4{__uint_as_float(lo << 16), __uint_as_float(lo & 0xffff0000u), __uint_as_float(hi << 16), __uint_as_float(hi & 0xffff0000u)}; }
          const int m = e / 5, n = e - m * 5;
          *(__attribute__((address_space(1))) u32x2*)(op16 + m * (16 * 1280) + n * 16) = u32x2{cvtpk(v[0], v[1]), cvtpk(v[2], v[3])};
        }
      }
    }
  }
}

struct Args { const void* p[16]; };
__global__ __launch_bounds__(512, 2) void fused(Args) {
  extern __shared__ __attribute__((aligned(16))) char smem[];
  typedef const __attribute__((address_space(4))) unsigned long long* kargp;
  kargp ka = (kargp)__builtin_amdgcn_kernarg_segment_ptr();
  const int id = blockIdx.x, xg = id & 7, cc = id >> 3, br = xg >> 2, hg = xg & 3, b = cc >> 2, pr = cc & 3, h0 = hg * 8 + pr * 2;
  ka += br * 8;
  const u16* x = (const u16*)ka[0]; const u16* k = (const u16*)ka[1]; const u16* v = (const u16*)ka[2]; const u16* wq = (const u16*)ka[3];
  const int w = __builtin_amdgcn_readfirstlane(threadIdx.x >> 6);
  asm volatile("s_nop 4");
  Ctx c;
  { unsigned long long pc; asm volatile("s_getpc_b64 %0" : "=s"(pc)); c.code = pc & ~255ull; }
  c.rx = mkr(x + (size_t)b * SQ * DM, SQ * DM * 2);
  c.rw = mkr(wq + h0 * DH, DM * NH * DH * 2 - h0 * DH * 2);
  c.rk = mkr(k + (size_t)(b * NH + h0) * SK * DH, 2 * SK * DH * 2);
  c.rv = mkr((const char*)(v + (size_t)(b * NH + h0) * SK * DH) - 1024, 2 * SK * DH * 2 + 1024);
  c.lds0 = (unsigned)(size_t)(LDS3 char*)smem;
  c.hs = w >> 2; c.j = w & 3; c.rot = (id * 5) & 15; c.krot = ((10 * pr + 5 * b) >> 2) % 10;
  c.w = w; c.id = id; c.wo = (const u16*)ka[4]; c.out = (u16*)ka[5]; c.ws = (char*)ka[6];
  const bool big = (c.j < 2) == (c.hs == 0);
  c.blk0 = (0x74208630u >> (4 * w)) & 15;
  if (big) body<3>(c); else body<2>(c);
}

extern "C" void kernel_launch(void* const* d_in, const int* in_sizes, int n_in, void* d_out, int out_size, void* d_ws, size_t ws_size, hipStream_t stream) {
  (void)hipFuncSetAttribute((const void*)fused, hipFuncAttributeMaxDynamicSharedMemorySize, LDS_BYTES);
  Args a;
  for (int br = 0; br < 2; ++br) {
    for (int i = 0; i < 5; ++i) a.p[br * 8 + i] = d_in[br * 5 + i];
    a.p[br * 8 + 5] = d_out; a.p[br * 8 + 6] = d_ws; a.p[br * 8 + 7] = d_ws;
  }
  fused<<<256, 512, LDS_BYTES, stream>>>(a);
}
```

```cpp
#include <hip/hip_runtime.h>
#include <hip/hip_bf16.h>
#include <type_traits>

typedef __bf16 bf16x8 __attribute__((ext_vector_type(8)));
typedef __bf16 bf16x4 __attribute__((ext_vector_type(4)));
typedef __bf16 bf16x2 __attribute__((ext_vector_type(2)));
typedef float f32x4 __attribute__((ext_vector_type(4)));
typedef float f32x2 __attribute__((ext_vector_type(2)));
typedef unsigned u32x4 __attribute__((ext_vector_type(4)));
typedef unsigned u32x2 __attribute__((ext_vector_type(2)));
typedef unsigned short u16;
typedef __attribute__((address_space(1))) unsigned long long gu64;
typedef __amdgpu_buffer_rsrc_t rsrc_t;
#define LDS3 __attribute__((address_space(3)))
#define DI __device__ __forceinline__
#define SB() __builtin_amdgcn_sched_barrier(0)
#define RLX_AGENT __ATOMIC_RELAXED, __HIP_MEMORY_SCOPE_AGENT
template <int N> using IC = std::integral_constant<int, N>;

constexpr int SQ = 160, DM = 640, NH = 32, SK = 512, DH = 128;
constexpr int LDS_BYTES = 163840;
__host__ __device__ constexpr int kslot(int sl, int hs) { return hs * 40960 + sl * 8192; }
__host__ __device__ constexpr int vslot(int sl, int hs) { return 81920 + hs * 40960 + sl * 8192; }
__host__ __device__ constexpr int xoff(int b) { return 16384 + b * 40960; }
__host__ __device__ constexpr int woff(int b) { return 90112 + (b & 1) * 16384 + (b >> 1) * 40960; }
constexpr float QSCALE = 0.08838834764831845f * 1.4426950408889634f;
constexpr float BIAS = 30.f;
constexpr unsigned OOB = 0x7ff00000u;
constexpr size_t WS_SLAB = 32u << 20, WS_SYNC = 64u << 20;
constexpr int FS = 32;
constexpr int QB = 13 * 1024;
constexpr int KVN0 = 1;
constexpr int NG = 8;
constexpr int PP2 = 15;
constexpr int THR = 3;
constexpr int EPRE = 2;
constexpr unsigned GOVQ = 55, GOVA = 110;
constexpr unsigned SPINS = 1u << 17;
constexpr int CODE_KB = 100;

DI unsigned cvtpk(float lo, float hi) { f32x2 v = {lo, hi}; bf16x2 b = __builtin_convertvector(v, bf16x2); return __builtin_bit_cast(unsigned, b); }
DI f32x4 mfma(bf16x8 a, bf16x8 b, f32x4 c) { return __builtin_amdgcn_mfma_f32_16x16x32_bf16(a, b, c, 0, 0, 0); }
DI bf16x8 ld128(unsigned addr) { return *(const LDS3 bf16x8*)(size_t)addr; }
DI void st128(unsigned addr, u32x4 v) { *(LDS3 u32x4*)(size_t)addr = v; }
DI bf16x4 ldtr(unsigned addr) { return __builtin_amdgcn_ds_read_tr16_b64_v4bf16((LDS3 bf16x4*)(size_t)addr); }
DI bf16x8 cat(bf16x4 a, bf16x4 b) { return __builtin_shufflevector(a, b, 0, 1, 2, 3, 4, 5, 6, 7); }
DI bf16x8 asb(u32x4 w) { return __builtin_bit_cast(bf16x8, w); }
DI rsrc_t mkr(const void* p, int bytes) { return __builtin_amdgcn_make_buffer_rsrc((void*)p, (short)0, bytes, 0x00020000); }
template <bool NT> DI void dma16(unsigned ldsaddr, unsigned voff, rsrc_t r, unsigned soff) {
  if constexpr (NT) asm volatile("s_mov_b32 m0, %0\n\ts_nop 0\n\tbuffer_load_dwordx4 %1, %2, %3 offen nt lds" :: "s"(ldsaddr), "v"(voff), "s"(r), "s"(soff) : "memory");
  else              asm volatile("s_mov_b32 m0, %0\n\ts_nop 0\n\tbuffer_load_dwordx4 %1, %2, %3 offen lds" :: "s"(ldsaddr), "v"(voff), "s"(r), "s"(soff) : "memory");
}
DI void setm0(unsigned v) { asm volatile("s_mov_b32 m0, %0" :: "s"(v) : "memory"); }
template <int POL, int OFF> DI void dmao(unsigned voff, rsrc_t r, unsigned soff) {
  if constexpr (POL == 1)      asm volatile("s_nop 0\n\tbuffer_load_dwordx4 %0, %1, %2 offen offset:%3 nt lds" :: "v"(voff), "s"(r), "s"(soff), "i"(OFF) : "memory");
  else if constexpr (POL == 2) asm volatile("s_nop 0\n\tbuffer_load_dwordx4 %0, %1, %2 offen offset:%3 sc1 lds" :: "v"(voff), "s"(r), "s"(soff), "i"(OFF) : "memory");
  else                         asm volatile("s_nop 0\n\tbuffer_load_dwordx4 %0, %1, %2 offen offset:%3 lds" :: "v"(voff), "s"(r), "s"(soff), "i"(OFF) : "memory");
}
template <int POL, int OFF> DI void dmao2(unsigned voff, rsrc_t r, unsigned soff) {
  if constexpr (POL == 1)      asm volatile("buffer_load_dwordx4 %0, %1, %2 offen offset:%3 nt lds" :: "v"(voff), "s"(r), "s"(soff), "i"(OFF) : "memory");
  else if constexpr (POL == 2) asm volatile("buffer_load_dwordx4 %0, %1, %2 offen offset:%3 sc1 lds" :: "v"(voff), "s"(r), "s"(soff), "i"(OFF) : "memory");
  else                         asm volatile("buffer_load_dwordx4 %0, %1, %2 offen offset:%3 lds" :: "v"(voff), "s"(r), "s"(soff), "i"(OFF) : "memory");
}
DI void pins(unsigned& v) { v = __builtin_amdgcn_readfirstlane(v); asm volatile("" : "+s"(v)); }
template <int N> DI void wait_vm() { asm volatile("s_waitcnt vmcnt(%0)" :: "i"(N) : "memory"); }
template <int N> DI void wait_vm_bar() { asm volatile("s_waitcnt vmcnt(%0) lgkmcnt(0)\n\ts_barrier" :: "i"(N) : "memory"); }
DI float xmax16(float v) { auto r = __builtin_amdgcn_permlane16_swap(__float_as_uint(v), __float_as_uint(v), false, false); return fmaxf(__uint_as_float(r[0]), __uint_as_float(r[1])); }
DI float xmax32(float v) { auto r = __builtin_amdgcn_permlane32_swap(__float_as_uint(v), __float_as_uint(v), false, false); return fmaxf(__uint_as_float(r[0]), __uint_as_float(r[1])); }
DI void amfma(f32x4& acc, bf16x8 a, bf16x8 b) { asm volatile("v_mfma_f32_16x16x32_bf16 %0, %1, %2, %0" : "+v"(acc) : "v"(a), "v"(b)); }
DI void amfma_c(f32x4& d, bf16x8 a, bf16x8 b, f32x4 cc) { asm volatile("v_mfma_f32_16x16x32_bf16 %0, %1, %2, %3" : "=&v"(d) : "v"(a), "v"(b), "v"(cc)); }
DI void amfma_z(f32x4& d, bf16x8 a, bf16x8 b) { asm volatile("v_mfma_f32_16x16x32_bf16 %0, %1, %2, 0" : "=&v"(d) : "v"(a), "v"(b)); }
template <bool Z> DI void amf(f32x4& d, bf16x8 a, bf16x8 b) { if constexpr (Z) amfma_z(d, a, b); else amfma(d, a, b); }
DI float aexp(float x) { asm volatile("v_exp_f32 %0, %0" : "+v"(x)); return x; }
DI float amul(float a, float b) { float r; asm("v_mul_f32 %0, %1, %2" : "=v"(r) : "s"(b), "v"(a)); return r; }
DI unsigned aor3(unsigned a, unsigned b, unsigned c) { unsigned r; asm volatile("v_or3_b32 %0, %1, %2, %3" : "=v"(r) : "v"(a), "v"(b), "v"(c)); return r; }
DI unsigned acvt(float lo, float hi) { unsigned r; asm volatile("v_cvt_pk_bf16_f32 %0, %1, %2" : "=v"(r) : "v"(lo), "v"(hi)); return r; }
DI void use(bf16x8 f) { asm volatile("" :: "v"(f)); }
DI void aaddu(unsigned& a, unsigned d) { asm volatile("v_add_u32 %0, %1, %0" : "+v"(a) : "s"(d)); }
DI unsigned laneid() { unsigned l; asm volatile("v_mbcnt_lo_u32_b32 %0, -1, 0\n\tv_mbcnt_hi_u32_b32 %0, -1, %0" : "=v"(l)); return l; }
template <class T> DI void pin(T& v) { asm volatile("" : "+v"(v)); }


struct Ctx {
  rsrc_t rx, rw, rk, rv;
  unsigned lds0;
  int j, hs, blk0, rot, krot, w, id;
  const u16* wo; u16* out; char* ws; unsigned long long code;
};
template <int NB> struct St {
  f32x4 o[NB][8], negm[NB], S0[NB], S1[NB], l[NB];
  bf16x8 qf[NB][4], ones;
  u32x4 pf[NB];
  unsigned orv;
};
template <int NB, int E> DI void exp1(f32x4 (&S)[NB]) { if constexpr (E >= 0 && E < 4 * NB) S[E % NB][E / NB] = aexp(S[E % NB][E / NB]); }
template <int NB, int LO, int HI> DI void exps(f32x4 (&S)[NB]) { if constexpr (LO < HI) { exp1<NB, LO>(S); exps<NB, LO + 1, HI>(S); } }
DI float max8(f32x4 a, f32x4 b) { return fmaxf(fmaxf(fmaxf(a[0], a[1]), fmaxf(a[2], a[3])), fmaxf(fmaxf(b[0], b[1]), fmaxf(b[2], b[3]))); }

template <int NB, bool HAS_NEXT> DI void refix(St<NB>& s, rsrc_t rk, int ksoff) {
  const unsigned ln = laneid();
  const int kvo = (ln & 15) * 256 + ((((ln >> 4) & 1) << 1) | (ln >> 5)) * 16;
  asm volatile("s_nop 7");
#pragma unroll
  for (int nb = 0; nb < NB; ++nb) {
    f32x4 a = s.negm[nb], b = s.negm[nb];
#pragma unroll
    for (int st = 0; st < 4; ++st) {
      a = mfma(asb(__builtin_amdgcn_raw_buffer_load_b128(rk, kvo + st * 64, ksoff, 0)), s.qf[nb][st], a);
      SB();
      b = mfma(asb(__builtin_amdgcn_raw_buffer_load_b128(rk, kvo + st * 64, ksoff + 4096, 0)), s.qf[nb][st], b);
      SB();
    }
    const float d = fmaxf(xmax32(xmax16(max8(a, b))) + BIAS, 0.f);
    const float f = __builtin_amdgcn_exp2f(-d);
    s.negm[nb] -= d; if (HAS_NEXT) s.S0[nb] -= d;
    s.l[nb] *= f;
#pragma unroll
    for (int db = 0; db < 8; ++db) s.o[nb][db] *= f;
#pragma unroll
    for (int r = 0; r < 4; ++r) { a[r] = __builtin_amdgcn_exp2f(a[r] - d); b[r] = __builtin_amdgcn_exp2f(b[r] - d); }
    u32x4 w = {cvtpk(a[0], a[1]), cvtpk(a[2], a[3]), cvtpk(b[0], b[1]), cvtpk(b[2], b[3])};
    s.pf[nb] = w;
    SB();
  }
  asm volatile("s_nop 1");
}
template <int NB, bool HAS_NEXT> DI void check(St<NB>& s, const Ctx& c, int t) {
  if (__builtin_expect(__any((s.orv & 0x40004000u) != 0u), 0)) { asm volatile("" : "+s"(t)); refix<NB, HAS_NEXT>(s, c.rk, c.hs * 131072 + ((t + c.rot) & 15) * 8192); }
}
template <int... I, class F> DI void static_for(std::integer_sequence<int, I...>, F f) { (f(IC<I>{}), ...); }

template <int NB> DI void body(const Ctx& c) {
  const int lane = threadIdx.x & 63, l15 = lane & 15, g = lane >> 4;
  const unsigned lds0 = c.lds0;
  const f32x4 z4 = {0.f, 0.f, 0.f, 0.f};
  auto N0 = [] {};
  unsigned voffK[2], voffV[2];
#pragma unroll
  for (int h = 0; h < 2; ++h) {
    voffK[h] = (lane >> 4) * 256 + (((lane & 15) ^ (8 * (c.j & 1) + 4 * h + (lane >> 4))) * 16);
    voffV[h] = ((lane >> 1) & 7) * 256 + (lane >> 4) * 32 + (lane & 1) * 16 + (h ? 128 : 1024);
  }
  auto tso = [&](int t) { return (unsigned)(((t + c.rot) & 15) * 8192 + c.j * 2048); };
  auto dma_kv = [&](bool isv, unsigned lb0, unsigned lb1, unsigned so, int q) {
    if (q & 1) { if (isv) dmao2<1, 1024>(voffV[1], c.rv, so + (q >> 1) * 131072); else dmao2<1, 1024>(voffK[1], c.rk, so + (q >> 1) * 131072); }
    else { setm0(q ? lb1 : lb0); if (isv) dmao<1, 0>(voffV[0], c.rv, so + (q >> 1) * 131072); else dmao<1, 0>(voffK[0], c.rk, so + (q >> 1) * 131072); }
  };
  const unsigned jb = lds0 + c.j * 2048;
  auto ld_k = [&](int t, int q) { return __builtin_amdgcn_raw_buffer_load_b128(c.rk, voffK[q & 1] + (q & 1) * 1024, tso(t) + (q >> 1) * 131072, 2); };
  auto ld_v = [&](int t, int q) { return __builtin_amdgcn_raw_buffer_load_b128(c.rv, voffV[q & 1] + (q & 1) * 1024, tso(t) + (q >> 1) * 131072, 2); };
  St<NB> s;
  unsigned gdl = 0;
  auto gov = [&](unsigned ts) {
    if (c.w == 2) {
      unsigned now = (unsigned)__builtin_amdgcn_s_memrealtime();
      while (now - gdl < ts) { __builtin_amdgcn_s_sleep(1); now = (unsigned)__builtin_amdgcn_s_memrealtime(); }
      gdl = now;
    }
  };
  const unsigned long long epoch = __hip_atomic_load((gu64*)(c.ws + WS_SYNC), RLX_AGENT);
  unsigned tdst = 0;
  u32x4 kvr[(NG - 3) * 4];
  {
    unsigned voffS;
    if constexpr (NB == 3) { const int f = (2 * (c.j & 1) + (lane >> 5)) | ((c.j >> 1) << 2); voffS = (lane >> 5) * 8192 + (((lane & 31) ^ (f << 1)) * 16); }
    else voffS = (lane >> 3) * 1280 + (((lane & 7) ^ (4 * (c.j & 1) + g)) * 16);
    const int wi0 = (c.j & 1) + 4 * (c.j >> 1);
    const unsigned voffS2 = voffS + 4 * 8192 - 2048;
    unsigned wso0 = (2 * c.krot * 32 + 2 * wi0) * 8192;
    auto dma_w = [&](int buf, int n) {
      if (n == 0) { setm0(lds0 + woff(buf) + wi0 * 1024); dmao<0, 0>(voffS, c.rw, wso0); }
      else if (n == 1) dmao2<0, 2048>(voffS2, c.rw, wso0);
      else if (n == 2) { setm0(lds0 + woff(buf) + wi0 * 1024 + 8192); dmao<0, 0>(voffS, c.rw, wso0 + 16 * 8192); }
      else { dmao2<0, 2048>(voffS2, c.rw, wso0 + 16 * 8192); wso0 += 32 * 8192; wso0 = wso0 >= 640u * 8192u ? wso0 - 640u * 8192u : wso0; pins(wso0); }
    };
    auto dma_x = [&](int ch, int n) {
      int kc = ch + c.krot; kc = kc >= 10 ? kc - 10 : kc;
      const int i = c.j + 4 * n;
      dma16<false>(lds0 + xoff(ch & 1) + i * 1024, voffS, c.rx, i * (8 * 1280) + kc * 128);
    };
    auto extra = [&](int m, int q) {
      if (m == 0 || m == 1) dma_kv(false, jb + kslot(m, 0), jb + kslot(m, 1), tso(m), q);
      else if (m == 2) dma_kv(true, jb + vslot(0, 0), jb + vslot(0, 1), tso(0), q);
      else if (m > 2 && m < NG) kvr[(m - 3) * 4 + q] = (m & 1) ? ld_k((m + 1) / 2, q) : ld_v((m - 2) / 2, q);
    };
    if constexpr (NB == 3) {
#pragma unroll
      for (int st = 0; st < 3; ++st)
#pragma unroll
        for (int n = 0; n < 4; ++n) dma_w(st, n);
    } else {
#pragma unroll
      for (int n = 0; n < 5; ++n) dma_x(0, n);
      {
        const rsrc_t rc = mkr((const void*)c.code, CODE_KB * 1024);
        asm volatile("s_mov_b32 m0, %0\n\ts_nop 0\n\tbuffer_load_dword %1, %2, %3 offen lds" :: "s"(lds0 + 38912), "v"(lane < 7 ? (unsigned)lane * 128u : OOB), "s"(rc), "s"(((c.id >> 3) * 4 + c.j) * 896) : "memory");
      }
      __builtin_amdgcn_s_setprio(1);
    }
    unsigned wa[8], xa[2];
    const int qq = l15 >> 2, p = lane & 3, f = qq | ((g & 1) << 2);
#pragma unroll
    for (int db = 0; db < 8; ++db) wa[db] = lds0 + woff(0) + (8 * g + qq) * 512 + c.hs * 256 + ((db ^ f) * 32) + p * 8;
#pragma unroll
    for (int u = 0; u < 2; ++u) xa[u] = lds0 + xoff(0) + (c.blk0 * 16 + l15) * 128 + (((4 * u + g) ^ ((l15 >> 1) & 7)) * 16);
    bf16x8 w4, w5, w6, w7, xp[NB];
    auto qstep = [&](auto U, auto PH, auto D) {
      constexpr int u = decltype(U)::value, ph = decltype(PH)::value;
      auto rw = [&](int db) { const unsigned a = wa[db] + (woff(u) - woff(0)); return cat(ldtr(a), ldtr(a + 2048)); };
      auto rx = [&](int nb) { return ld128(xa[u & 1] + (u >> 1) * 40960 + nb * 2048); };
      auto mm = [&](auto Z, int db, bf16x8& wf, bf16x8 (&xf)[NB], auto g0, auto g1, auto g2) {
        constexpr int z = decltype(Z)::value;
        if constexpr (z == 2) { g0(); g1(); g2(); SB(); }
        else {
          amf<z == 1>(s.o[0][db], wf, xf[0]); SB(); g0(); SB();
          amf<z == 1>(s.o[1][db], wf, xf[1]); SB(); g1(); SB();
          if constexpr (NB == 3) { amf<z == 1>(s.o[2][db], wf, xf[2]); SB(); }
          g2(); SB();
        }
      };
      constexpr IC<ph == 0 ? 2 : ph == 1 ? 1 : 0> H{};
      constexpr IC<ph == 0 ? 1 : 0> C{};
      bf16x8 xf[NB], w0, w1, w2, w3;
      if constexpr (NB == 3) {
        mm(H, 4, w4, xp, [&] { w0 = rw(0); }, [&] { xf[0] = rx(0); xf[1] = rx(1); }, [&] { xf[2] = rx(2); });
        mm(H, 5, w5, xp, [&] { w1 = rw(1); }, [&] { D(0); }, N0);
        mm(H, 6, w6, xp, [&] { w2 = rw(2); }, [&] { w3 = rw(3); }, N0);
        mm(H, 7, w7, xp, [&] { w4 = rw(4); }, [&] { D(1); }, [&] { use(xf[NB - 1]); });
        mm(C, 0, w0, xf, [&] { w5 = rw(5); }, [&] { use(w1); w6 = rw(6); }, N0);
        mm(C, 1, w1, xf, [&] { w7 = rw(7); }, [&] { use(w2); D(2); }, N0);
        mm(C, 2, w2, xf, [&] { use(w3); }, [&] { D(3); }, N0);
        mm(C, 3, w3, xf, [&] { use(w7); }, N0, N0);
      } else {
        mm(H, 4, w4, xp, [&] { w0 = rw(0); }, [&] { xf[0] = rx(0); xf[1] = rx(1); }, N0);
        mm(H, 5, w5, xp, [&] { w1 = rw(1); }, [&] { D(0); }, N0);
        mm(H, 6, w6, xp, [&] { w2 = rw(2); }, [&] { w3 = rw(3); D(1); }, N0);
        mm(H, 7, w7, xp, [&] { w4 = rw(4); }, [&] { D(2); use(xf[NB - 1]); }, N0);
        mm(C, 0, w0, xf, [&] { w5 = rw(5); }, [&] { use(w1); w6 = rw(6); D(3); }, N0);
        mm(C, 1, w1, xf, [&] { w7 = rw(7); }, [&] { use(w2); D(4); }, N0);
        mm(C, 2, w2, xf, [&] { use(w3); }, N0, N0);
        mm(C, 3, w3, xf, [&] { use(w7); }, N0, N0);
      }
#pragma unroll
      for (int nb = 0; nb < NB; ++nb) xp[nb] = xf[nb];
    };
    SB();
    if constexpr (NB == 3) {
      static_for(std::make_integer_sequence<int, 4>{}, [&](auto U) {
        constexpr int u = decltype(U)::value;
        wait_vm_bar<8>(); qstep(U, IC<(u < 2 ? u : 2)>{}, [&](int n) { dma_w((u + 3) & 3, n); });
      });
#pragma unroll 1
      for (int s4 = 4; s4 < 16; s4 += 4)
        static_for(std::make_integer_sequence<int, 4>{}, [&](auto U) {
          constexpr int u = decltype(U)::value;
          wait_vm_bar<8>(); qstep(U, IC<2>{}, [&](int n) { dma_w((u + 3) & 3, n); });
        });
      wait_vm_bar<8>(); qstep(IC<0>{}, IC<2>{}, [&](int n) { dma_w(3, n); });
      wait_vm_bar<8>(); qstep(IC<1>{}, IC<2>{}, [&](int) {});
      wait_vm_bar<4>(); qstep(IC<2>{}, IC<2>{}, [&](int) {});
      wait_vm_bar<0>(); qstep(IC<3>{}, IC<2>{}, [&](int) {});
    } else {
      static_for(std::make_integer_sequence<int, 20>{}, [&](auto ST) {
        constexpr int st = decltype(ST)::value, mp = (st >> 1) - 1 - KVN0;
        gov(GOVQ);
        if constexpr (st & 1) wait_vm_bar<63>(); else wait_vm_bar<st == 0 ? 1 : (mp >= 0 && mp < NG) ? 4 : 0>();
        qstep(IC<(st & 3)>{}, IC<(st < 2 ? st : 2)>{}, [&](int n) {
          if constexpr (st & 1) { if (n < 4) extra((st >> 1) - KVN0, n); } else if constexpr ((st >> 1) + 1 < 10) dma_x((st >> 1) + 1, n); });
      });
    }
#pragma unroll
    for (int nb = 0; nb < NB; ++nb) { amfma(s.o[nb][4], w4, xp[nb]); amfma(s.o[nb][5], w5, xp[nb]); amfma(s.o[nb][6], w6, xp[nb]); amfma(s.o[nb][7], w7, xp[nb]); }
    asm volatile("s_nop 7");
    SB();
  }
  auto qconv = [&] {
#pragma unroll
    for (int nb = 0; nb < NB; ++nb)
#pragma unroll
      for (int e = 0; e < 4; ++e) {
        f32x4 a0 = s.o[nb][2 * e], a1 = s.o[nb][2 * e + 1];
#pragma unroll
        for (int r = 0; r < 4; ++r) { a0[r] = amul(a0[r], QSCALE); a1[r] = amul(a1[r], QSCALE); }
        auto r0 = __builtin_amdgcn_permlane16_swap(cvtpk(a0[0], a0[1]), cvtpk(a1[0], a1[1]), false, false);
        auto r1 = __builtin_amdgcn_permlane16_swap(cvtpk(a0[2], a0[3]), cvtpk(a1[2], a1[3]), false, false);
        u32x4 w = {r0[0], r1[0], r0[1], r1[1]};
        s.qf[nb][e] = asb(w); pin(s.qf[nb][e]);
      }
  };
  if constexpr (NB == 2) qconv();
  wait_vm_bar<0>();
  if constexpr (NB == 2) pin(kvr[(NG - 3) * 4 - 1]);
  const unsigned long long tag = (((unsigned long long)__builtin_amdgcn_readfirstlane((unsigned)(epoch >> 32)) << 32) | __builtin_amdgcn_readfirstlane((unsigned)epoch)) + 1ull;
  const int pg = ((g & 1) << 1) | (g >> 1);
  unsigned ka[4];
#pragma unroll
  for (int st = 0; st < 4; ++st) ka[st] = lds0 + kslot(0, c.hs) + l15 * 256 + (((4 * st + pg) ^ l15) * 16);
  unsigned va = lds0 + vslot(0, c.hs) + (g >> 1) * 2048 + (4 * (g & 1) + (l15 >> 2)) * 32 + (lane & 3) * 8;
  auto rks = [&](int sl, int kt, int st) { return ld128(ka[st] + sl * 8192 + kt * 4096); };
  auto rvs = [&](int sl, int db) { const unsigned a = va + sl * 8192 + (db >> 2) * 1024 + (db & 3) * 256; return cat(ldtr(a), ldtr(a + 4096)); };
  {
    bf16x8 kf0[4], kf1[4];
#pragma unroll
    for (int st = 0; st < 4; ++st) { kf0[st] = rks(0, 0, st); kf1[st] = rks(0, 1, st); }
    if constexpr (NB == 3) {
      const rsrc_t rwt = mkr(c.wo + (size_t)((c.id & 3) * 1024 + (c.id >> 3) * 32) * 640, 40960);
      asm volatile("buffer_load_dword %0, %1, %2, %3 offen\n\tbuffer_load_dword %0, %1, %2, %4 offen" : "+v"(tdst) : "v"(lane * 128), "s"(rwt), "s"(c.j * 8192), "s"(c.j * 8192 + 32768) : "memory");
      SB();
      qconv();
    }
#pragma unroll
    for (int st = 0; st < 4; ++st)
#pragma unroll
      for (int nb = 0; nb < NB; ++nb) {
        s.S0[nb] = mfma(kf0[st], s.qf[nb][st], st == 0 ? z4 : s.S0[nb]); s.S1[nb] = mfma(kf1[st], s.qf[nb][st], st == 0 ? z4 : s.S1[nb]);
      }
#pragma unroll
    for (int nb = 0; nb < NB; ++nb) {
#pragma unroll
      for (int db = (NB == 3 ? 6 : 5); db < 8; ++db) { s.o[nb][db] = z4; pin(s.o[nb][db]); }
      const float m = xmax32(xmax16(max8(s.S0[nb], s.S1[nb]))) + BIAS;
      s.negm[nb] = z4 - m; s.S0[nb] -= m; s.S1[nb] -= m;
      pin(s.negm[nb]);
    }
    s.ones = asb(u32x4{0x3f803f80u, 0x3f803f80u, 0x3f803f80u, 0x3f803f80u});
    pin(s.ones);
  }
  exps<NB, 0, 4 * NB>(s.S0); exps<NB, 0, 2 * NB>(s.S1);
  bf16x8 v5h, v6, v7;
  SB();
  auto us = [&](auto m, auto g0, auto g1, auto g2) {
    m(0); SB(); g0(); SB(); m(1); SB(); g1(); SB();
    if constexpr (NB == 3) { m(2); SB(); g2(); SB(); }
  };
  auto QK0 = [&](int st, bf16x8 fr) { return [&s, st, fr](int nb) { if (st == 0) amfma_c(s.S0[nb], fr, s.qf[nb][0], s.negm[nb]); else amfma(s.S0[nb], fr, s.qf[nb][st]); }; };
  auto QK1 = [&](int st, bf16x8 fr) { return [&s, st, fr](int nb) { if (st == 0) amfma_c(s.S1[nb], fr, s.qf[nb][0], s.negm[nb]); else amfma(s.S1[nb], fr, s.qf[nb][st]); }; };
  auto PVH = [&](int db, bf16x8& fr) { return [&s, db, &fr](int nb) { amfma(s.o[nb][db], fr, asb(s.pf[nb])); }; };
  auto N = [] {};
#define E0(e) [&] { exp1<NB, (e)>(s.S0); }
#define E1(e) [&] { exp1<NB, (e)>(s.S1); }
#define CLO(nb) [&] { if constexpr ((nb) < NB) { s.pf[nb][0] = acvt(s.S0[nb][0], s.S0[nb][1]); s.pf[nb][1] = acvt(s.S0[nb][2], s.S0[nb][3]); } }
#define CHI(nb) [&] { if constexpr ((nb) < NB) { s.pf[nb][2] = acvt(s.S1[nb][0], s.S1[nb][1]); s.pf[nb][3] = acvt(s.S1[nb][2], s.S1[nb][3]); } }
#define U(f, ...) [&] { use(f); SB(); (__VA_ARGS__)(); }
  unsigned dk = 8192, dv = 8192, cnt = 1;
  if constexpr (NB == 3) {
#pragma unroll
    for (int st = 0; st < 4; ++st) ka[st] += 8192;
  }
  auto astep = [&](auto FI, auto SL, int t, auto kvh) {
    constexpr bool FIRST = decltype(FI)::value;
    constexpr int sl = decltype(SL)::value;
    auto rk = [&](int kt, int st) { return rks(NB == 3 ? 0 : sl, kt, st); };
    auto rv = [&](int db) { return rvs(NB == 3 ? 0 : (sl + 4) % 5, db); };
    if constexpr (NB == 2) { gov(GOVA); wait_vm_bar<6>(); } else wait_vm_bar<63>();
    auto PH = [&](int db, bf16x8& fr) { return [&, db](int nb) { if constexpr (!FIRST) amfma(s.o[nb][db], fr, asb(s.pf[nb])); }; };
    auto PV = [&](int db, bf16x8 fr) { return [&s, db, fr](int nb) { amf<FIRST>(s.o[nb][db], fr, asb(s.pf[nb])); }; };
    auto SUM = [&](int nb) { amf<FIRST>(s.l[nb], s.ones, asb(s.pf[nb])); };
    bf16x8 a0, a1, a2, a3, b0, b1, b2, b3, v0, v1, v2, v3, v4;
    unsigned o1, o2;
    if constexpr (NB == 3) {
      bf16x8 v5;
      us(PH(6, v6), [&] { a0 = rk(0, 0); a1 = rk(0, 1); }, [&] { a2 = rk(0, 2); a3 = rk(0, 3); }, E1(6));
      us(PH(7, v7), CLO(0), CLO(1), U(a0, CLO(2)));
      us(QK0(0, a0), [&] { b0 = rk(1, 0); b1 = rk(1, 1); }, E1(7), U(a1, E1(8)));
      us(QK0(1, a1), E1(9), E1(10), U(a2, E1(11)));
      us(QK0(2, a2), [&] { o1 = aor3(s.pf[0][0], s.pf[0][1], s.pf[1][0]); o2 = aor3(s.pf[1][1], s.pf[2][0], s.pf[2][1]); }, CHI(0), U(a3, CHI(1)));
      us(QK0(3, a3), CHI(2), [&] { o1 = aor3(o1, s.pf[0][2], s.pf[0][3]); o2 = aor3(o2, s.pf[1][2], s.pf[1][3]); },
                     U(b0, [&] { o1 = aor3(o1, s.pf[2][2], s.pf[2][3]); s.orv = o1 | o2; }));
      if constexpr (!FIRST) check<NB, true>(s, c, t - 1);
      SB();
      us(QK1(0, b0), [&] { b2 = rk(1, 2); b3 = rk(1, 3); }, [&] { v0 = rv(0); }, U(b1, [&] { aaddu(ka[0], dk); aaddu(ka[1], dk); }));
      us(QK1(1, b1), [&] { v1 = rv(1); }, E0(0), U(b2, E0(1)));
      us(QK1(2, b2), [&] { v2 = rv(2); }, E0(2), U(b3, E0(3)));
      us(QK1(3, b3), [&] { v3 = rv(3); }, E0(4), E0(5));
      us(SUM, E0(6), E0(7), U(v0, E0(8)));
      us(PV(0, v0), [&] { v4 = rv(4); }, E0(9), U(v1, E0(10)));
      us(PV(1, v1), [&] { v5 = rv(5); }, E0(11), U(v2, E1(0)));
      us(PV(2, v2), [&] { v6 = rv(6); }, E1(1), U(v3, E1(2)));
      us(PV(3, v3), [&] { v7 = rv(7); }, E1(3), U(v4, E1(4)));
      us(PV(4, v4), E1(5), [&] { aaddu(ka[2], dk); aaddu(ka[3], dk); }, U(v5, [&] { aaddu(va, dv); }));
      us(PV(5, v5), [&] { dv = dk; cnt = cnt == 4 ? 0 : cnt + 1; pins(cnt); }, [&] { use(v7); dk = cnt == 4 ? -32768 : 8192; pins(dk); }, N);
    } else {
      us(PH(5, v5h), [&] { a0 = rk(0, 0); a1 = rk(0, 1); }, [&] { a2 = rk(0, 2); a3 = rk(0, 3); }, N);
      us(PH(6, v6), E1(4), [&] { kvh(0); }, N);
      us(PH(7, v7), CLO(0), U(a0, CLO(1)), N);
      us(QK0(0, a0), [&] { b0 = rk(1, 0); b1 = rk(1, 1); }, U(a1, E1(5)), N);
      us(QK0(1, a1), E1(6), U(a2, E1(7)), N);
      us(QK0(2, a2), [&] { o1 = aor3(s.pf[0][0], s.pf[0][1], s.pf[1][0]); kvh(1); }, U(a3, CHI(0)), N);
      us(QK0(3, a3), CHI(1), U(b0, [&] { o1 = aor3(o1, s.pf[1][1], s.pf[0][2]); o2 = aor3(s.pf[0][3], s.pf[1][2], s.pf[1][3]); s.orv = o1 | o2; }), N);
      if constexpr (!FIRST) check<NB, true>(s, c, t - 1);
      SB();
      us(QK1(0, b0), [&] { b2 = rk(1, 2); b3 = rk(1, 3); }, U(b1, [&] { v0 = rv(0); kvh(2); }), N);
      us(QK1(1, b1), [&] { v1 = rv(1); }, U(b2, [&] { kvh(3); }), N);
      us(QK1(2, b2), [&] { v2 = rv(2); }, U(b3, E0(0)), N);
      us(QK1(3, b3), [&] { v3 = rv(3); }, [&] { exp1<NB, 1>(s.S0); kvh(4); }, N);
      us(SUM, E0(2), U(v0, E0(3)), N);
      us(PV(0, v0), [&] { v4 = rv(4); }, U(v1, [&] { exp1<NB, 4>(s.S0); kvh(5); }), N);
      us(PV(1, v1), [&] { v5h = rv(5); }, U(v2, E0(5)), N);
      us(PV(2, v2), [&] { v6 = rv(6); }, U(v3, [&] { exp1<NB, 6>(s.S0); kvh(6); }), N);
      us(PV(3, v3), [&] { v7 = rv(7); exp1<NB, 7>(s.S0); }, U(v4, [&] { exp1<NB, 0>(s.S1); kvh(7); }), N);
      us(PV(4, v4), [&] { exp1<NB, 1>(s.S1); exp1<NB, 2>(s.S1); }, [&] { exp1<NB, 3>(s.S1); use(v7); }, N);
    }
  };
  auto piece = [&](int t, int n) {
    const int p0 = (PP2 * (t - 1) + 1) / 2, p = p0 + n;
    if (p < (PP2 * t + 1) / 2 && p < (32 - NG) * 4) {
      const int it = NG + (p >> 2), h = (p >> 1) & 1, v = it == 31 || !(it & 1), tn = it == 31 ? 15 : v ? (it - 2) / 2 : (it + 1) / 2;
      const unsigned so = tso(tn) + h * 131072;
      if (p & 1) { wait_vm<THR>(); dmao2<1, 1024>(v ? voffV[1] : voffK[1], v ? c.rv : c.rk, so); }
      else { setm0(jb + (v ? vslot(tn % 5, h) : kslot(tn % 5, h))); wait_vm<THR>(); dmao2<1, 0>(v ? voffV[0] : voffK[0], v ? c.rv : c.rk, so); }
    }
  };
  astep(std::true_type{}, IC<1>{}, 1, [&](int n) { if constexpr (NB == 2) {
    piece(1, n);
#pragma unroll
    for (int i = 0; i < (NG - 3) * 4; ++i) if (i * 8 / ((NG - 3) * 4) == n) { const int m = i >> 2, q = i & 3;
      st128(jb + lane * 16 + ((m & 1) ? vslot(1 + (m >> 1), q >> 1) : kslot(2 + (m >> 1), q >> 1)) + (q & 1) * 1024, kvr[i]); } } });
  constexpr std::false_type F{};
  if constexpr (NB == 3) {
    auto NH = [](int) {};
#pragma unroll 1
    for (int t = 2; t < 16; ++t) astep(F, IC<0>{}, t, NH);
  } else {
    static_for(std::make_integer_sequence<int, 12>{}, [&](auto TT) {
      constexpr int t = decltype(TT)::value + 2;
      astep(F, IC<t % 5>{}, t, [&](int n) { piece(t, n); });
    });
  }
  rsrc_t rof, rwo, rslab; int pr, hg, rotb; gu64* sync; u16* ob;
  {
    int id = c.id; asm volatile("" : "+s"(id));
    const int br = (id >> 2) & 1, b = id >> 5;
    hg = id & 3; pr = (id >> 3) & 3; rotb = b & 3;
    rof = mkr(c.ws + (size_t)((br * 8 + b) * 4 + hg) * 327680, 327680);
    rwo = mkr((const char*)(c.wo + (size_t)hg * 1024 * 640 + pr * 160) - 4096, 1024 * 1280 - pr * 320 + 4096);
    rslab = mkr(c.ws + WS_SLAB + (size_t)((br * 8 + b) * 4 + pr) * (12 * QB), 12 * QB);
    sync = (gu64*)(c.ws + WS_SYNC);
    ob = c.out + (size_t)(b * 160 + (hg & 1) * 80) * 1280 + br * 640 + pr * 160 + (hg >> 1) * 80;
  }
  unsigned lz = 0;
  const int dks = c.j >> 1, dh = c.j & 1;
  auto stg = [](int u) { return u * 40960; };
  auto kpair = [&](int i) { return ((pr + (i >> 2)) & 3) * 4 + ((i + rotb) & 3); };
  auto wvo = [&](int n) { const int e = 64 * n + (int)lz, row = e / 20, ch = e - row * 20; return row * 1280 + ((ch ^ (((row >> 2) & 1) << 1)) * 16) + 4096; };
  auto wso = [&](int i) { return (kpair(i) * 64 + dks * 32 + dh * 16) * 1280; };
  auto wld = [&](int i, int n) { return lds0 + stg(i & 3) + 20480 + dks * 10240 + dh * 5120 + n * 1024; };
  u32x4 wr[15];
  if constexpr (NB == 2) {
    lz = laneid();
    unsigned vw[5];
#pragma unroll
    for (int n = 0; n < 5; ++n) vw[n] = wvo(n);
    auto wl = [&](int p) { wait_vm<4>(); if (p < 15) wr[p] = __builtin_amdgcn_raw_buffer_load_b128(rwo, vw[p % 5], wso(p / 5), 0); };
    SB();
    astep(F, IC<4>{}, 14, [&](int n) { wl(n); });
    astep(F, IC<0>{}, 15, [&](int n) { wl(8 + n); });
  }
  if constexpr (NB == 3) { wait_vm_bar<0>(); pin(tdst); } else wait_vm_bar<15>();
  u32x4 ow[NB][4];
  unsigned gb;
  auto ost = [&](int e, int nb) { __builtin_amdgcn_raw_buffer_store_b128(ow[nb][e], rof, gb + e * 10240 + nb * 1024, 0, 16); };
  {
    bf16x8 vf[8];
#pragma unroll
    for (int db = 0; db < 4; ++db) vf[db] = rvs(0, db);
    SB();
    if constexpr (NB == 2) us(PVH(5, v5h), N, N, N);
    us(PVH(6, v6), N, N, N);
    exps<NB, 2 * NB, 4 * NB>(s.S1);
    us(PVH(7, v7), CLO(0), CLO(1), CLO(2));
    CHI(0)(); CHI(1)(); CHI(2)();
    s.orv = 0;
#pragma unroll
    for (int nb = 0; nb < NB; ++nb) { s.orv = aor3(s.orv, s.pf[nb][0], s.pf[nb][1]); s.orv = aor3(s.orv, s.pf[nb][2], s.pf[nb][3]); }
    SB();
    check<NB, false>(s, c, 15);
    SB();
    if constexpr (NB == 3) lz = laneid();
#pragma unroll
    for (int db = 4; db < 8; ++db) vf[db] = rvs(0, db);
    gb = lz * 16 + (pr * 8 + c.hs * 4) * 10240 + c.blk0 * 1024;
#pragma unroll
    for (int nb = 0; nb < NB; ++nb) s.l[nb] = mfma(s.ones, asb(s.pf[nb]), s.l[nb]);
    float inv[NB];
    auto norm = [&](int e, int nb) {
      f32x4 a0 = s.o[nb][2 * e], a1 = s.o[nb][2 * e + 1];
#pragma unroll
      for (int r = 0; r < 4; ++r) { float x = a0[r] * inv[nb], y = a1[r] * inv[nb]; pin(x); pin(y); a0[r] = x; a1[r] = y; }
      ow[nb][e] = u32x4{cvtpk(a0[0], a0[1]), cvtpk(a0[2], a0[3]), cvtpk(a1[0], a1[1]), cvtpk(a1[2], a1[3])};
      if (e < EPRE) ost(e, nb);
    };
#pragma unroll
    for (int e = 0; e < 4; ++e) {
#pragma unroll
      for (int nb = 0; nb < NB; ++nb) {
        s.o[nb][2 * e] = mfma(vf[2 * e], asb(s.pf[nb]), s.o[nb][2 * e]);
        s.o[nb][2 * e + 1] = mfma(vf[2 * e + 1], asb(s.pf[nb]), s.o[nb][2 * e + 1]);
        if (e == 0) inv[nb] = __builtin_amdgcn_rcpf(s.l[nb][0]); else norm(e - 1, nb);
        if constexpr (NB == 2) { const int p = (e * 2 + nb) * 2; st128(wld(p / 5, p % 5) + lz * 16, wr[p]); if (p + 1 < 15) st128(wld((p + 1) / 5, (p + 1) % 5) + lz * 16, wr[p + 1]); }
        SB();
      }
    }
#pragma unroll
    for (int nb = 0; nb < NB; ++nb) norm(3, nb);
  }
  asm volatile("s_waitcnt lgkmcnt(0)\n\ts_barrier" ::: "memory");
#pragma unroll
  for (int eh = 0; eh < 2; ++eh) {
    const unsigned lb = lds0 + stg((c.hs * 2 + eh - rotb) & 3) + c.blk0 * 1024 + lz * 16;
#pragma unroll
    for (int el = 0; el < 2; ++el)
#pragma unroll
      for (int nb = 0; nb < NB; ++nb) st128(lb + el * 10240 + nb * 1024, ow[nb][eh * 2 + el]);
  }
  {
    constexpr int NM = NB, M0 = NM == 3 ? 0 : 3;
    const int q = c.j, qm = q & 1, qn = q >> 1, zg = lz >> 4;
    unsigned vo3[5];
#pragma unroll
    for (int n = 0; n < 5; ++n) vo3[n] = NB == 3 ? wvo(n) - (n & 3) * 1024 : lz * 16 + (n >> 2) * 4096;
    const unsigned lb3 = lds0 + dks * 10240 + dh * 5120 + (NB == 3 ? 20480 : 0);
    auto so3 = [&](int i) { unsigned v = (NB == 3 ? wso(i) : (kpair(i) * 2 + dks) * 10240 + dh * 5120) + (unsigned)(i >> 4) * OOB; pins(v); return v; };
    auto dmal = [&](unsigned so, int u, int n) {
      constexpr int POL = NB == 3 ? 0 : 2;
      const rsrc_t r = NB == 3 ? rwo : rof;
      const unsigned vo = vo3[NB == 3 ? n : (n >> 2) * 4];
      if (n == 0) { setm0(lb3 + stg(u)); dmao<POL, 0>(vo, r, so); }
      else if (n == 1) dmao2<POL, 1024>(vo, r, so);
      else if (n == 2) dmao2<POL, 2048>(vo, r, so);
      else if (n == 3) dmao2<POL, 3072>(vo, r, so);
      else { setm0(lb3 + stg(u) + 4096); dmao<POL, 0>(vo, r, so); }
    };
    if constexpr (NB == 3) {
      const unsigned so = so3(3);
#pragma unroll
      for (int n = 0; n < 5; ++n) dmal(so, 3, n);
    }
    unsigned oa[2], wa[2][2];
    {
      oa[0] = lds0 + (qm * 5 + M0) * 1024 + lz * 16; oa[1] = oa[0] + 81920;
      const unsigned wb = lds0 + 20480 + (4 * zg + ((lz & 15) >> 2)) * 320 + (lz & 3) * 8 + qn * 160;
      const int sg = (qn ? -32 : 32) * (zg & 1);
      wa[0][0] = wb + sg; wa[0][1] = wb - sg; wa[1][0] = wa[0][0] + 81920; wa[1][1] = wa[0][1] + 81920;
      pin(oa[1]); pin(wa[1][0]); pin(wa[1][1]);
    }
    f32x4 acc[NM][5];
    bf16x8 h2, h3, h4, op[NM];
    auto interval = [&](auto UU, auto D, auto tail) {
      constexpr bool FIRST = decltype(UU)::value < 0;
      constexpr int u = FIRST ? 0 : decltype(UU)::value, so = (u & 1) * 40960;
      auto ro = [&](int ks, int m) { return ld128(oa[u >> 1] + so + ks * 10240 + m * 1024); };
      auto rw = [&](int ks, int n) { const unsigned a = wa[u >> 1][n & 1] + so + ks * 10240 + n * 32; return cat(ldtr(a), ldtr(a + 5120)); };
      auto mm = [&](auto Z, int n, bf16x8& wf, bf16x8 (&of)[NM], auto g0, auto g1, auto g2) {
        constexpr int z = decltype(Z)::value;
        if constexpr (z == 2) { g0(); g1(); g2(); SB(); }
        else {
          amf<z == 1>(acc[0][n], wf, of[0]); SB(); g0(); SB(); amf<z == 1>(acc[1][n], wf, of[1]); SB(); g1(); SB();
          if constexpr (NM == 3) { amf<z == 1>(acc[2][n], wf, of[2]); SB(); }
          g2(); SB();
        }
      };
      constexpr IC<FIRST ? 2 : 0> H{};
      constexpr IC<FIRST ? 1 : 0> C{};
      constexpr IC<0> P{};
      bf16x8 o0[NM], o1[NM], a0, a1, a2, a3, a4, b0, b1;
      mm(H, 2, h2, op, [&] { a0 = rw(0, 0); }, [&] { o0[0] = ro(0, 0); o0[1] = ro(0, 1); }, [&] { if constexpr (NM == 3) o0[2] = ro(0, 2); });
      mm(H, 3, h3, op, [&] { a1 = rw(0, 1); }, [&] { a2 = rw(0, 2); }, [&] { D(0); });
      mm(H, 4, h4, op, [&] { a3 = rw(0, 3); }, [&] { a4 = rw(0, 4); }, [&] { use(o0[NM - 1]); });
      mm(C, 0, a0, o0, [&] { o1[0] = ro(1, 0); o1[1] = ro(1, 1); }, [&] { use(a1); if constexpr (NM == 3) o1[2] = ro(1, 2); }, [&] { D(1); });
      mm(C, 1, a1, o0, [&] { b0 = rw(1, 0); }, [&] { use(a2); b1 = rw(1, 1); }, N0);
      mm(C, 2, a2, o0, [&] { h2 = rw(1, 2); }, [&] { use(a3); }, [&] { D(2); });
      mm(C, 3, a3, o0, [&] { h3 = rw(1, 3); }, [&] { use(a4); }, N0);
      mm(C, 4, a4, o0, [&] { h4 = rw(1, 4); }, [&] { use(b0); }, [&] { D(3); });
      mm(P, 0, b0, o1, N0, [&] { use(b1); }, [&] { D(4); });
      mm(P, 1, b1, o1, [&] { use(h4); }, N0, N0);
      tail();
#pragma unroll
      for (int m = 0; m < NM; ++m) op[m] = o1[m];
    };
    auto ND = [](int) {};
    SB();
    auto poll = [&](int base) {
      const int pid = c.id ^ (((lz & 3) ? (lz & 3) : 1) << 3);
      for (unsigned spins = 0; spins < SPINS; ++spins) {
        if (__all(__hip_atomic_load(sync + (base + pid) * FS, RLX_AGENT) == tag)) break;
        __builtin_amdgcn_s_sleep(1);
      }
    };
    wait_vm_bar<63>();
    interval(IC<-1>{}, [&](int n) {
#pragma unroll
      for (int k = 0; k < (4 - EPRE) * NB; ++k) if (k * 5 / (EPRE < 4 ? (4 - EPRE) * NB : 1) == n) ost(EPRE + k / NB, k % NB); }, N0);
    wait_vm_bar<63>();
    { const unsigned so = so3(4); interval(IC<1>{}, [&](int n) { if constexpr (NB == 3) dmal(so, 0, n); }, N0); }
    if constexpr (NB == 3) wait_vm_bar<5>(); else wait_vm_bar<0>();
    if (c.w == 3 && lz == 0) __hip_atomic_store(sync + (1 + c.id) * FS, tag, RLX_AGENT);
    const unsigned so5 = so3(5);
    interval(IC<2>{}, [&](int n) { if constexpr (NB == 3) dmal(so5, 1, n); }, [&] { if (c.w == 2) poll(1); });
    if constexpr (NB == 3) wait_vm_bar<10>(); else wait_vm_bar<63>();
    { const unsigned sa = so3(NB == 3 ? 6 : 4);
      interval(IC<3>{}, [&](int n) { if constexpr (NB == 3) dmal(sa, 2, n);
        else { if (n == 0) { dmal(sa, 0, 0); dmal(sa, 0, 1); } else if (n == 1) { dmal(sa, 0, 2); dmal(sa, 0, 3); } else if (n == 2) { dmal(sa, 0, 4); dmal(so5, 1, 0); }
               else if (n == 3) { dmal(so5, 1, 1); dmal(so5, 1, 2); } else { dmal(so5, 1, 3); dmal(so5, 1, 4); } } }, N0); }
    unsigned son = so3(NB == 3 ? 7 : 6);
#pragma unroll 1
    for (int i4 = 4; i4 < 16; i4 += 4)
      static_for(std::make_integer_sequence<int, 4>{}, [&](auto UU) {
        constexpr int u = decltype(UU)::value;
        if constexpr (NB == 3) wait_vm_bar<10>(); else wait_vm_bar<5>();
        interval(UU, [&](int n) { dmal(son, (u + (NB == 3 ? 3 : 2)) & 3, n); if (n == 4) son = so3(i4 + u + (NB == 3 ? 4 : 3)); }, N0);
      });
#pragma unroll
    for (int m = 0; m < NM; ++m) { amfma(acc[m][2], h2, op[m]); amfma(acc[m][3], h3, op[m]); amfma(acc[m][4], h4, op[m]); }
    asm volatile("s_nop 7");
    SB();
    if (q != hg) {
      const int so = (q * 3 + ((hg - q - 1) & 3)) * QB;
#pragma unroll
      for (int e = 0; e < NM * 5; e += 2) {
        const f32x4 a = acc[e / 5][e % 5], b = e + 1 < NM * 5 ? acc[(e + 1) / 5][(e + 1) % 5] : z4;
        __builtin_amdgcn_raw_buffer_store_b128(u32x4{cvtpk(a[0], a[1]), cvtpk(a[2], a[3]), cvtpk(b[0], b[1]), cvtpk(b[2], b[3])}, rslab, lz * 16 + ((M0 ? 8 : 0) + e / 2) * 1024 + so, 0, 16);
      }
    }
    asm volatile("s_waitcnt vmcnt(0) lgkmcnt(0)\n\ts_barrier" ::: "memory");
    if (c.w == 0 && lz == 0) __hip_atomic_store(sync + (257 + c.id) * FS, tag, RLX_AGENT);
    if (q == hg) {
#pragma unroll
      for (int e = 0; e < NM * 5; ++e) st128(lds0 + lz * 16 + (M0 * 5 + e) * 1024, __builtin_bit_cast(u32x4, acc[e / 5][e % 5]));
    }
    if (c.w == ((hg + 1) & 3)) {
      const int pid = c.id ^ ((lz & 3) ? (lz & 3) : 1);
      for (unsigned spins = 0; spins < SPINS; ++spins) {
        if (__all(__hip_atomic_load(sync + (257 + pid) * FS, RLX_AGENT) == tag)) break;
        __builtin_amdgcn_s_sleep(1);
      }
    }
    if (c.id == 0 && c.w == 7) {
      for (unsigned spins = 0; spins < SPINS; ++spins) {
        bool ok = true;
#pragma unroll
        for (int k = 0; k < 4; ++k) ok &= __hip_atomic_load(sync + (1 + lz + 64 * k) * FS, RLX_AGENT) == tag;
        if (__all(ok)) break;
        __builtin_amdgcn_s_sleep(1);
      }
      if (lz == 0) __hip_atomic_store(sync, tag, RLX_AGENT);
    }
    asm volatile("s_waitcnt vmcnt(0) lgkmcnt(0)\n\ts_barrier" ::: "memory");
    u16* op16 = ob + (size_t)(lz & 15) * 1280 + 4 * zg;
    u32x4 r[2][3];
#pragma unroll
    for (int k = 0; k < 2; ++k)
#pragma unroll
      for (int sl = 0; sl < 3; ++sl) r[k][sl] = __builtin_amdgcn_raw_buffer_load_b128(rslab, c.w + 8 * k < 13 ? lz * 16 + (c.w + 8 * k) * 1024 : OOB, (hg * 3 + sl) * QB, 16);
#pragma unroll
    for (int k = 0; k < 2; ++k) {
      const int un = c.w + 8 * k;
#pragma unroll
      for (int hh = 0; hh < 2; ++hh) {
        const int e = un < 8 ? 2 * un + hh : 15 + 2 * (un - 8) + hh;
        if (un < 13 && !(un == 7 && hh == 1)) {
          f32x4 v = *(const LDS3 f32x4*)(size_t)(lds0 + lz * 16 + e * 1024);
#pragma unroll
          for (int sl = 0; sl < 3; ++sl) { const unsigned lo = r[k][sl][2 * hh], hi = r[k][sl][2 * hh + 1];
            v += f32x4{__uint_as_float(lo << 16), __uint_as_float(lo & 0xffff0000u), __uint_as_float(hi << 16), __uint_as_float(hi & 0xffff0000u)}; }
          const int m = e / 5, n = e - m * 5;
          *(__attribute__((address_space(1))) u32x2*)(op16 + m * (16 * 1280) + n * 16) = u32x2{cvtpk(v[0], v[1]), cvtpk(v[2], v[3])};
        }
      }
    }
  }
}

struct Args { const void* p[16]; };
__global__ __launch_bounds__(512, 2) void fused(Args) {
  extern __shared__ __attribute__((aligned(16))) char smem[];
  typedef const __attribute__((address_space(4))) unsigned long long* kargp;
  kargp ka = (kargp)__builtin_amdgcn_kernarg_segment_ptr();
  const int id = blockIdx.x, xg = id & 7, cc = id >> 3, br = xg >> 2, hg = xg & 3, b = cc >> 2, pr = cc & 3, h0 = hg * 8 + pr * 2;
  ka += br * 8;
  const u16* x = (const u16*)ka[0]; const u16* k = (const u16*)ka[1]; const u16* v = (const u16*)ka[2]; const u16* wq = (const u16*)ka[3];
  const int w = __builtin_amdgcn_readfirstlane(threadIdx.x >> 6);
  asm volatile("s_nop 4");
  Ctx c;
  { unsigned long long pc; asm volatile("s_getpc_b64 %0" : "=s"(pc)); c.code = pc & ~255ull; }
  c.rx = mkr(x + (size_t)b * SQ * DM, SQ * DM * 2);
  c.rw = mkr(wq + h0 * DH, DM * NH * DH * 2 - h0 * DH * 2);
  c.rk = mkr(k + (size_t)(b * NH + h0) * SK * DH, 2 * SK * DH * 2);
  c.rv = mkr((const char*)(v + (size_t)(b * NH + h0) * SK * DH) - 1024, 2 * SK * DH * 2 + 1024);
  c.lds0 = (unsigned)(size_t)(LDS3 char*)smem;
  c.hs = w >> 2; c.j = w & 3; c.rot = (id * 5) & 15; c.krot = 0;
  c.w = w; c.id = id; c.wo = (const u16*)ka[4]; c.out = (u16*)ka[5]; c.ws = (char*)ka[6];
  const bool big = (c.j < 2) == (c.hs == 0);
  c.blk0 = (0x74208630u >> (4 * w)) & 15;
  if (big) body<3>(c); else body<2>(c);
}

extern "C" void kernel_launch(void* const* d_in, const int* in_sizes, int n_in, void* d_out, int out_size, void* d_ws, size_t ws_size, hipStream_t stream) {
  (void)hipFuncSetAttribute((const void*)fused, hipFuncAttributeMaxDynamicSharedMemorySize, LDS_BYTES);
  Args a;
  for (int br = 0; br < 2; ++br) {
    for (int i = 0; i < 5; ++i) a.p[br * 8 + i] = d_in[br * 5 + i];
    a.p[br * 8 + 5] = d_out; a.p[br * 8 + 6] = d_ws; a.p[br * 8 + 7] = d_ws;
  }
  fused<<<256, 512, LDS_BYTES, stream>>>(a);
}
```

```cpp
#include <hip/hip_runtime.h>
#include <hip/hip_bf16.h>
#include <type_traits>

typedef __bf16 bf16x8 __attribute__((ext_vector_type(8)));
typedef __bf16 bf16x4 __attribute__((ext_vector_type(4)));
typedef __bf16 bf16x2 __attribute__((ext_vector_type(2)));
typedef float f32x4 __attribute__((ext_vector_type(4)));
typedef float f32x2 __attribute__((ext_vector_type(2)));
typedef unsigned u32x4 __attribute__((ext_vector_type(4)));
typedef unsigned u32x2 __attribute__((ext_vector_type(2)));
typedef unsigned short u16;
typedef __attribute__((address_space(1))) unsigned long long gu64;
typedef __amdgpu_buffer_rsrc_t rsrc_t;
#define LDS3 __attribute__((address_space(3)))
#define DI __device__ __forceinline__
#define SB() __builtin_amdgcn_sched_barrier(0)
#define RLX_AGENT __ATOMIC_RELAXED, __HIP_MEMORY_SCOPE_AGENT
template <int N> using IC = std::integral_constant<int, N>;

constexpr int SQ = 160, DM = 640, NH = 32, SK = 512, DH = 128;
constexpr int LDS_BYTES = 163840;
__host__ __device__ constexpr int kslot(int sl, int hs) { return hs * 40960 + sl * 8192; }
__host__ __device__ constexpr int vslot(int sl, int hs) { return 81920 + hs * 40960 + sl * 8192; }
__host__ __device__ constexpr int xoff(int b) { return 16384 + b * 40960; }
__host__ __device__ constexpr int woff(int b) { return 90112 + (b & 1) * 16384 + (b >> 1) * 40960; }
constexpr float QSCALE = 0.08838834764831845f * 1.4426950408889634f;
constexpr float BIAS = 30.f;
constexpr unsigned OOB = 0x7ff00000u;
constexpr size_t WS_SLAB = 32u << 20, WS_SYNC = 64u << 20;
constexpr int FS = 32;
constexpr int QB = 13 * 1024;
constexpr int KVN0 = 1;
constexpr int NG = 8;
constexpr int PP2 = 15;
constexpr int THR = 3;
constexpr int EPRE = 2;
constexpr unsigned GOVQ = 55, GOVA = 110;
constexpr unsigned SPINS = 1u << 17;
constexpr int CODE_KB = 100;

DI unsigned cvtpk(float lo, float hi) { f32x2 v = {lo, hi}; bf16x2 b = __builtin_convertvector(v, bf16x2); return __builtin_bit_cast(unsigned, b); }
DI f32x4 mfma(bf16x8 a, bf16x8 b, f32x4 c) { return __builtin_amdgcn_mfma_f32_16x16x32_bf16(a, b, c, 0, 0, 0); }
DI bf16x8 ld128(unsigned addr) { return *(const LDS3 bf16x8*)(size_t)addr; }
DI void st128(unsigned addr, u32x4 v) { *(LDS3 u32x4*)(size_t)addr = v; }
DI bf16x4 ldtr(unsigned addr) { return __builtin_amdgcn_ds_read_tr16_b64_v4bf16((LDS3 bf16x4*)(size_t)addr); }
DI bf16x8 cat(bf16x4 a, bf16x4 b) { return __builtin_shufflevector(a, b, 0, 1, 2, 3, 4, 5, 6, 7); }
DI bf16x8 asb(u32x4 w) { return __builtin_bit_cast(bf16x8, w); }
DI rsrc_t mkr(const void* p, int bytes) { return __builtin_amdgcn_make_buffer_rsrc((void*)p, (short)0, bytes, 0x00020000); }
template <bool NT> DI void dma16(unsigned ldsaddr, unsigned voff, rsrc_t r, unsigned soff) {
  if constexpr (NT) asm volatile("s_mov_b32 m0, %0\n\ts_nop 0\n\tbuffer_load_dwordx4 %1, %2, %3 offen nt lds" :: "s"(ldsaddr), "v"(voff), "s"(r), "s"(soff) : "memory");
  else              asm volatile("s_mov_b32 m0, %0\n\ts_nop 0\n\tbuffer_load_dwordx4 %1, %2, %3 offen lds" :: "s"(ldsaddr), "v"(voff), "s"(r), "s"(soff) : "memory");
}
DI void setm0(unsigned v) { asm volatile("s_mov_b32 m0, %0" :: "s"(v) : "memory"); }
template <int POL, int OFF> DI void dmao(unsigned voff, rsrc_t r, unsigned soff) {
  if constexpr (POL == 1)      asm volatile("s_nop 0\n\tbuffer_load_dwordx4 %0, %1, %2 offen offset:%3 nt lds" :: "v"(voff), "s"(r), "s"(soff), "i"(OFF) : "memory");
  else if constexpr (POL == 2) asm volatile("s_nop 0\n\tbuffer_load_dwordx4 %0, %1, %2 offen offset:%3 sc1 lds" :: "v"(voff), "s"(r), "s"(soff), "i"(OFF) : "memory");
  else                         asm volatile("s_nop 0\n\tbuffer_load_dwordx4 %0, %1, %2 offen offset:%3 lds" :: "v"(voff), "s"(r), "s"(soff), "i"(OFF) : "memory");
}
template <int POL, int OFF> DI void dmao2(unsigned voff, rsrc_t r, unsigned soff) {
  if constexpr (POL == 1)      asm volatile("buffer_load_dwordx4 %0, %1, %2 offen offset:%3 nt lds" :: "v"(voff), "s"(r), "s"(soff), "i"(OFF) : "memory");
  else if constexpr (POL == 2) asm volatile("buffer_load_dwordx4 %0, %1, %2 offen offset:%3 sc1 lds" :: "v"(voff), "s"(r), "s"(soff), "i"(OFF) : "memory");
  else                         asm volatile("buffer_load_dwordx4 %0, %1, %2 offen offset:%3 lds" :: "v"(voff), "s"(r), "s"(soff), "i"(OFF) : "memory");
}
DI void pins(unsigned& v) { v = __builtin_amdgcn_readfirstlane(v); asm volatile("" : "+s"(v)); }
template <int N> DI void wait_vm() { asm volatile("s_waitcnt vmcnt(%0)" :: "i"(N) : "memory"); }
template <int N> DI void wait_vm_bar() { asm volatile("s_waitcnt vmcnt(%0) lgkmcnt(0)\n\ts_barrier" :: "i"(N) : "memory"); }
DI float xmax16(float v) { auto r = __builtin_amdgcn_permlane16_swap(__float_as_uint(v), __float_as_uint(v), false, false); return fmaxf(__uint_as_float(r[0]), __uint_as_float(r[1])); }
DI float xmax32(float v) { auto r = __builtin_amdgcn_permlane32_swap(__float_as_uint(v), __float_as_uint(v), false, false); return fmaxf(__uint_as_float(r[0]), __uint_as_float(r[1])); }
DI void amfma(f32x4& acc, bf16x8 a, bf16x8 b) { asm volatile("v_mfma_f32_16x16x32_bf16 %0, %1, %2, %0" : "+v"(acc) : "v"(a), "v"(b)); }
DI void amfma_c(f32x4& d, bf16x8 a, bf16x8 b, f32x4 cc) { asm volatile("v_mfma_f32_16x16x32_bf16 %0, %1, %2, %3" : "=&v"(d) : "v"(a), "v"(b), "v"(cc)); }
DI void amfma_z(f32x4& d, bf16x8 a, bf16x8 b) { asm volatile("v_mfma_f32_16x16x32_bf16 %0, %1, %2, 0" : "=&v"(d) : "v"(a), "v"(b)); }
template <bool Z> DI void amf(f32x4& d, bf16x8 a, bf16x8 b) { if constexpr (Z) amfma_z(d, a, b); else amfma(d, a, b); }
DI float aexp(float x) { asm volatile("v_exp_f32 %0, %0" : "+v"(x)); return x; }
DI float amul(float a, float b) { float r; asm("v_mul_f32 %0, %1, %2" : "=v"(r) : "s"(b), "v"(a)); return r; }
DI unsigned aor3(unsigned a, unsigned b, unsigned c) { unsigned r; asm volatile("v_or3_b32 %0, %1, %2, %3" : "=v"(r) : "v"(a), "v"(b), "v"(c)); return r; }
DI unsigned acvt(float lo, float hi) { unsigned r; asm volatile("v_cvt_pk_bf16_f32 %0, %1, %2" : "=v"(r) : "v"(lo), "v"(hi)); return r; }
DI void use(bf16x8 f) { asm volatile("" :: "v"(f)); }
DI void aaddu(unsigned& a, unsigned d) { asm volatile("v_add_u32 %0, %1, %0" : "+v"(a) : "s"(d)); }
DI unsigned laneid() { unsigned l; asm volatile("v_mbcnt_lo_u32_b32 %0, -1, 0\n\tv_mbcnt_hi_u32_b32 %0, -1, %0" : "=v"(l)); return l; }
template <class T> DI void pin(T& v) { asm volatile("" : "+v"(v)); }


struct Ctx {
  rsrc_t rx, rw, rk, rv;
  unsigned lds0;
  int j, hs, blk0, rot, krot, w, id;
  const u16* wo; u16* out; char* ws; unsigned long long code;
};
template <int NB> struct St {
  f32x4 o[NB][8], negm[NB], S0[NB], S1[NB], l[NB];
  bf16x8 qf[NB][4], ones;
  u32x4 pf[NB];
  unsigned orv;
};
template <int NB, int E> DI void exp1(f32x4 (&S)[NB]) { if constexpr (E >= 0 && E < 4 * NB) S[E % NB][E / NB] = aexp(S[E % NB][E / NB]); }
template <int NB, int LO, int HI> DI void exps(f32x4 (&S)[NB]) { if constexpr (LO < HI) { exp1<NB, LO>(S); exps<NB, LO + 1, HI>(S); } }
DI float max8(f32x4 a, f32x4 b) { return fmaxf(fmaxf(fmaxf(a[0], a[1]), fmaxf(a[2], a[3])), fmaxf(fmaxf(b[0], b[1]), fmaxf(b[2], b[3]))); }

template <int NB, bool HAS_NEXT> DI void refix(St<NB>& s, rsrc_t rk, int ksoff) {
  const unsigned ln = laneid();
  const int kvo = (ln & 15) * 256 + ((((ln >> 4) & 1) << 1) | (ln >> 5)) * 16;
  asm volatile("s_nop 7");
#pragma unroll
  for (int nb = 0; nb < NB; ++nb) {
    f32x4 a = s.negm[nb], b = s.negm[nb];
#pragma unroll
    for (int st = 0; st < 4; ++st) {
      a = mfma(asb(__builtin_amdgcn_raw_buffer_load_b128(rk, kvo + st * 64, ksoff, 0)), s.qf[nb][st], a);
      SB();
      b = mfma(asb(__builtin_amdgcn_raw_buffer_load_b128(rk, kvo + st * 64, ksoff + 4096, 0)), s.qf[nb][st], b);
      SB();
    }
    const float d = fmaxf(xmax32(xmax16(max8(a, b))) + BIAS, 0.f);
    const float f = __builtin_amdgcn_exp2f(-d);
    s.negm[nb] -= d; if (HAS_NEXT) s.S0[nb] -= d;
    s.l[nb] *= f;
#pragma unroll
    for (int db = 0; db < 8; ++db) s.o[nb][db] *= f;
#pragma unroll
    for (int r = 0; r < 4; ++r) { a[r] = __builtin_amdgcn_exp2f(a[r] - d); b[r] = __builtin_amdgcn_exp2f(b[r] - d); }
    u32x4 w = {cvtpk(a[0], a[1]), cvtpk(a[2], a[3]), cvtpk(b[0], b[1]), cvtpk(b[2], b[3])};
    s.pf[nb] = w;
    SB();
  }
  asm volatile("s_nop 1");
}
template <int NB, bool HAS_NEXT> DI void check(St<NB>& s, const Ctx& c, int t) {
  if (__builtin_expect(__any((s.orv & 0x40004000u) != 0u), 0)) { asm volatile("" : "+s"(t)); refix<NB, HAS_NEXT>(s, c.rk, c.hs * 131072 + ((t + c.rot) & 15) * 8192); }
}
template <int... I, class F> DI void static_for(std::integer_sequence<int, I...>, F f) { (f(IC<I>{}), ...); }

template <int NB> DI void body(const Ctx& c) {
  const int lane = threadIdx.x & 63, l15 = lane & 15, g = lane >> 4;
  const unsigned lds0 = c.lds0;
  const f32x4 z4 = {0.f, 0.f, 0.f, 0.f};
  auto N0 = [] {};
  unsigned voffK[2], voffV[2];
#pragma unroll
  for (int h = 0; h < 2; ++h) {
    voffK[h] = (lane >> 4) * 256 + (((lane & 15) ^ (8 * (c.j & 1) + 4 * h + (lane >> 4))) * 16);
    voffV[h] = ((lane >> 1) & 7) * 256 + (lane >> 4) * 32 + (lane & 1) * 16 + (h ? 128 : 1024);
  }
  auto tso = [&](int t) { return (unsigned)(((t + c.rot) & 15) * 8192 + c.j * 2048); };
  auto dma_kv = [&](bool isv, unsigned lb0, unsigned lb1, unsigned so, int q) {
    if (q & 1) { if (isv) dmao2<1, 1024>(voffV[1], c.rv, so + (q >> 1) * 131072); else dmao2<1, 1024>(voffK[1], c.rk, so + (q >> 1) * 131072); }
    else { setm0(q ? lb1 : lb0); if (isv) dmao<1, 0>(voffV[0], c.rv, so + (q >> 1) * 131072); else dmao<1, 0>(voffK[0], c.rk, so + (q >> 1) * 131072); }
  };
  const unsigned jb = lds0 + c.j * 2048;
  auto ld_k = [&](int t, int q) { return __builtin_amdgcn_raw_buffer_load_b128(c.rk, voffK[q & 1] + (q & 1) * 1024, tso(t) + (q >> 1) * 131072, 2); };
  auto ld_v = [&](int t, int q) { return __builtin_amdgcn_raw_buffer_load_b128(c.rv, voffV[q & 1] + (q & 1) * 1024, tso(t) + (q >> 1) * 131072, 2); };
  St<NB> s;
  unsigned gdl = 0;
  auto gov = [&](unsigned ts) {
    if (c.w == 2) {
      unsigned now = (unsigned)__builtin_amdgcn_s_memrealtime();
      while (now - gdl < ts) { __builtin_amdgcn_s_sleep(1); now = (unsigned)__builtin_amdgcn_s_memrealtime(); }
      gdl = now;
    }
  };
  const unsigned long long epoch = __hip_atomic_load((gu64*)(c.ws + WS_SYNC), RLX_AGENT);
  u32x4 kvr[(NG - 3) * 4];
  {
    unsigned voffS;
    if constexpr (NB == 3) { const int f = (2 * (c.j & 1) + (lane >> 5)) | ((c.j >> 1) << 2); voffS = (lane >> 5) * 8192 + (((lane & 31) ^ (f << 1)) * 16); }
    else voffS = (lane >> 3) * 1280 + (((lane & 7) ^ (4 * (c.j & 1) + g)) * 16);
    const int wi0 = (c.j & 1) + 4 * (c.j >> 1);
    const unsigned voffS2 = voffS + 4 * 8192 - 2048;
    unsigned wso0 = (2 * c.krot * 32 + 2 * wi0) * 8192;
    auto dma_w = [&](int buf, int n) {
      if (n == 0) { setm0(lds0 + woff(buf) + wi0 * 1024); dmao<0, 0>(voffS, c.rw, wso0); }
      else if (n == 1) dmao2<0, 2048>(voffS2, c.rw, wso0);
      else if (n == 2) { setm0(lds0 + woff(buf) + wi0 * 1024 + 8192); dmao<0, 0>(voffS, c.rw, wso0 + 16 * 8192); }
      else { dmao2<0, 2048>(voffS2, c.rw, wso0 + 16 * 8192); wso0 += 32 * 8192; wso0 = wso0 >= 640u * 8192u ? wso0 - 640u * 8192u : wso0; pins(wso0); }
    };
    auto dma_x = [&](int ch, int n) {
      int kc = ch + c.krot; kc = kc >= 10 ? kc - 10 : kc;
      const int i = c.j + 4 * n;
      dma16<false>(lds0 + xoff(ch & 1) + i * 1024, voffS, c.rx, i * (8 * 1280) + kc * 128);
    };
    auto extra = [&](int m, int q) {
      if (m == 0 || m == 1) dma_kv(false, jb + kslot(m, 0), jb + kslot(m, 1), tso(m), q);
      else if (m == 2) dma_kv(true, jb + vslot(0, 0), jb + vslot(0, 1), tso(0), q);
      else if (m > 2 && m < NG) kvr[(m - 3) * 4 + q] = (m & 1) ? ld_k((m + 1) / 2, q) : ld_v((m - 2) / 2, q);
    };
    if constexpr (NB == 3) {
#pragma unroll
      for (int st = 0; st < 3; ++st)
#pragma unroll
        for (int n = 0; n < 4; ++n) dma_w(st, n);
    } else {
#pragma unroll
      for (int n = 0; n < 5; ++n) dma_x(0, n);
      {
        const rsrc_t rc = mkr((const void*)c.code, CODE_KB * 1024);
        asm volatile("s_mov_b32 m0, %0\n\ts_nop 0\n\tbuffer_load_dword %1, %2, %3 offen lds" :: "s"(lds0 + 38912), "v"(lane < 7 ? (unsigned)lane * 128u : OOB), "s"(rc), "s"(((c.id >> 3) * 4 + c.j) * 896) : "memory");
      }
      __builtin_amdgcn_s_setprio(1);
    }
    unsigned wa[8], xa[2];
    const int qq = l15 >> 2, p = lane & 3, f = qq | ((g & 1) << 2);
#pragma unroll
    for (int db = 0; db < 8; ++db) wa[db] = lds0 + woff(0) + (8 * g + qq) * 512 + c.hs * 256 + ((db ^ f) * 32) + p * 8;
#pragma unroll
    for (int u = 0; u < 2; ++u) xa[u] = lds0 + xoff(0) + (c.blk0 * 16 + l15) * 128 + (((4 * u + g) ^ ((l15 >> 1) & 7)) * 16);
    bf16x8 w4, w5, w6, w7, xp[NB];
    auto qstep = [&](auto U, auto PH, auto D) {
      constexpr int u = decltype(U)::value, ph = decltype(PH)::value;
      auto rw = [&](int db) { const unsigned a = wa[db] + (woff(u) - woff(0)); return cat(ldtr(a), ldtr(a + 2048)); };
      auto rx = [&](int nb) { return ld128(xa[u & 1] + (u >> 1) * 40960 + nb * 2048); };
      auto mm = [&](auto Z, int db, bf16x8& wf, bf16x8 (&xf)[NB], auto g0, auto g1, auto g2) {
        constexpr int z = decltype(Z)::value;
        if constexpr (z == 2) { g0(); g1(); g2(); SB(); }
        else {
          amf<z == 1>(s.o[0][db], wf, xf[0]); SB(); g0(); SB();
          amf<z == 1>(s.o[1][db], wf, xf[1]); SB(); g1(); SB();
          if constexpr (NB == 3) { amf<z == 1>(s.o[2][db], wf, xf[2]); SB(); }
          g2(); SB();
        }
      };
      constexpr IC<ph == 0 ? 2 : ph == 1 ? 1 : 0> H{};
      constexpr IC<ph == 0 ? 1 : 0> C{};
      bf16x8 xf[NB], w0, w1, w2, w3;
      if constexpr (NB == 3) {
        mm(H, 4, w4, xp, [&] { w0 = rw(0); }, [&] { xf[0] = rx(0); xf[1] = rx(1); }, [&] { xf[2] = rx(2); });
        mm(H, 5, w5, xp, [&] { w1 = rw(1); }, [&] { D(0); }, N0);
        mm(H, 6, w6, xp, [&] { w2 = rw(2); }, [&] { w3 = rw(3); }, N0);
        mm(H, 7, w7, xp, [&] { w4 = rw(4); }, [&] { D(1); }, [&] { use(xf[NB - 1]); });
        mm(C, 0, w0, xf, [&] { w5 = rw(5); }, [&] { use(w1); w6 = rw(6); }, N0);
        mm(C, 1, w1, xf, [&] { w7 = rw(7); }, [&] { use(w2); D(2); }, N0);
        mm(C, 2, w2, xf, [&] { use(w3); }, [&] { D(3); }, N0);
        mm(C, 3, w3, xf, [&] { use(w7); }, N0, N0);
      } else {
        mm(H, 4, w4, xp, [&] { w0 = rw(0); }, [&] { xf[0] = rx(0); xf[1] = rx(1); }, N0);
        mm(H, 5, w5, xp, [&] { w1 = rw(1); }, [&] { D(0); }, N0);
        mm(H, 6, w6, xp, [&] { w2 = rw(2); }, [&] { w3 = rw(3); D(1); }, N0);
        mm(H, 7, w7, xp, [&] { w4 = rw(4); }, [&] { D(2); use(xf[NB - 1]); }, N0);
        mm(C, 0, w0, xf, [&] { w5 = rw(5); }, [&] { use(w1); w6 = rw(6); D(3); }, N0);
        mm(C, 1, w1, xf, [&] { w7 = rw(7); }, [&] { use(w2); D(4); }, N0);
        mm(C, 2, w2, xf, [&] { use(w3); }, N0, N0);
        mm(C, 3, w3, xf, [&] { use(w7); }, N0, N0);
      }
#pragma unroll
      for (int nb = 0; nb < NB; ++nb) xp[nb] = xf[nb];
    };
    SB();
    if constexpr (NB == 3) {
      static_for(std::make_integer_sequence<int, 4>{}, [&](auto U) {
        constexpr int u = decltype(U)::value;
        wait_vm_bar<8>(); qstep(U, IC<(u < 2 ? u : 2)>{}, [&](int n) { dma_w((u + 3) & 3, n); });
      });
#pragma unroll 1
      for (int s4 = 4; s4 < 16; s4 += 4)
        static_for(std::make_integer_sequence<int, 4>{}, [&](auto U) {
          constexpr int u = decltype(U)::value;
          wait_vm_bar<8>(); qstep(U, IC<2>{}, [&](int n) { dma_w((u + 3) & 3, n); });
        });
      wait_vm_bar<8>(); qstep(IC<0>{}, IC<2>{}, [&](int n) { dma_w(3, n); });
      wait_vm_bar<8>(); qstep(IC<1>{}, IC<2>{}, [&](int) {});
      wait_vm_bar<4>(); qstep(IC<2>{}, IC<2>{}, [&](int) {});
      wait_vm_bar<0>(); qstep(IC<3>{}, IC<2>{}, [&](int) {});
    } else {
      static_for(std::make_integer_sequence<int, 20>{}, [&](auto ST) {
        constexpr int st = decltype(ST)::value, mp = (st >> 1) - 1 - KVN0;
        gov(GOVQ);
        if constexpr (st & 1) wait_vm_bar<63>(); else wait_vm_bar<st == 0 ? 1 : (mp >= 0 && mp < NG) ? 4 : 0>();
        qstep(IC<(st & 3)>{}, IC<(st < 2 ? st : 2)>{}, [&](int n) {
          if constexpr (st & 1) { if (n < 4) extra((st >> 1) - KVN0, n); } else if constexpr ((st >> 1) + 1 < 10) dma_x((st >> 1) + 1, n); });
      });
    }
#pragma unroll
    for (int nb = 0; nb < NB; ++nb) { amfma(s.o[nb][4], w4, xp[nb]); amfma(s.o[nb][5], w5, xp[nb]); amfma(s.o[nb][6], w6, xp[nb]); amfma(s.o[nb][7], w7, xp[nb]); }
    asm volatile("s_nop 7");
    SB();
  }
  auto qconv = [&] {
#pragma unroll
    for (int nb = 0; nb < NB; ++nb)
#pragma unroll
      for (int e = 0; e < 4; ++e) {
        f32x4 a0 = s.o[nb][2 * e], a1 = s.o[nb][2 * e + 1];
#pragma unroll
        for (int r = 0; r < 4; ++r) { a0[r] = amul(a0[r], QSCALE); a1[r] = amul(a1[r], QSCALE); }
        auto r0 = __builtin_amdgcn_permlane16_swap(cvtpk(a0[0], a0[1]), cvtpk(a1[0], a1[1]), false, false);
        auto r1 = __builtin_amdgcn_permlane16_swap(cvtpk(a0[2], a0[3]), cvtpk(a1[2], a1[3]), false, false);
        u32x4 w = {r0[0], r1[0], r0[1], r1[1]};
        s.qf[nb][e] = asb(w); pin(s.qf[nb][e]);
      }
  };
  if constexpr (NB == 2) qconv();
  wait_vm_bar<0>();
  if constexpr (NB == 2) pin(kvr[(NG - 3) * 4 - 1]);
  const unsigned long long tag = (((unsigned long long)__builtin_amdgcn_readfirstlane((unsigned)(epoch >> 32)) << 32) | __builtin_amdgcn_readfirstlane((unsigned)epoch)) + 1ull;
  const int pg = ((g & 1) << 1) | (g >> 1);
  unsigned ka[4];
#pragma unroll
  for (int st = 0; st < 4; ++st) ka[st] = lds0 + kslot(0, c.hs) + l15 * 256 + (((4 * st + pg) ^ l15) * 16);
  unsigned va = lds0 + vslot(0, c.hs) + (g >> 1) * 2048 + (4 * (g & 1) + (l15 >> 2)) * 32 + (lane & 3) * 8;
  auto rks = [&](int sl, int kt, int st) { return ld128(ka[st] + sl * 8192 + kt * 4096); };
  auto rvs = [&](int sl, int db) { const unsigned a = va + sl * 8192 + (db >> 2) * 1024 + (db & 3) * 256; return cat(ldtr(a), ldtr(a + 4096)); };
  {
    bf16x8 kf0[4], kf1[4];
#pragma unroll
    for (int st = 0; st < 4; ++st) { kf0[st] = rks(0, 0, st); kf1[st] = rks(0, 1, st); }
    if constexpr (NB == 3) qconv();
#pragma unroll
    for (int st = 0; st < 4; ++st)
#pragma unroll
      for (int nb = 0; nb < NB; ++nb) {
        s.S0[nb] = mfma(kf0[st], s.qf[nb][st], st == 0 ? z4 : s.S0[nb]); s.S1[nb] = mfma(kf1[st], s.qf[nb][st], st == 0 ? z4 : s.S1[nb]);
      }
#pragma unroll
    for (int nb = 0; nb < NB; ++nb) {
#pragma unroll
      for (int db = (NB == 3 ? 6 : 5); db < 8; ++db) { s.o[nb][db] = z4; pin(s.o[nb][db]); }
      const float m = xmax32(xmax16(max8(s.S0[nb], s.S1[nb]))) + BIAS;
      s.negm[nb] = z4 - m; s.S0[nb] -= m; s.S1[nb] -= m;
      pin(s.negm[nb]);
    }
    s.ones = asb(u32x4{0x3f803f80u, 0x3f803f80u, 0x3f803f80u, 0x3f803f80u});
    pin(s.ones);
  }
  exps<NB, 0, 4 * NB>(s.S0); exps<NB, 0, 2 * NB>(s.S1);
  bf16x8 v5h, v6, v7;
  SB();
  auto us = [&](auto m, auto g0, auto g1, auto g2) {
    m(0); SB(); g0(); SB(); m(1); SB(); g1(); SB();
    if constexpr (NB == 3) { m(2); SB(); g2(); SB(); }
  };
  auto QK0 = [&](int st, bf16x8 fr) { return [&s, st, fr](int nb) { if (st == 0) amfma_c(s.S0[nb], fr, s.qf[nb][0], s.negm[nb]); else amfma(s.S0[nb], fr, s.qf[nb][st]); }; };
  auto QK1 = [&](int st, bf16x8 fr) { return [&s, st, fr](int nb) { if (st == 0) amfma_c(s.S1[nb], fr, s.qf[nb][0], s.negm[nb]); else amfma(s.S1[nb], fr, s.qf[nb][st]); }; };
  auto PVH = [&](int db, bf16x8& fr) { return [&s, db, &fr](int nb) { amfma(s.o[nb][db], fr, asb(s.pf[nb])); }; };
  auto N = [] {};
#define E0(e) [&] { exp1<NB, (e)>(s.S0); }
#define E1(e) [&] { exp1<NB, (e)>(s.S1); }
#define CLO(nb) [&] { if constexpr ((nb) < NB) { s.pf[nb][0] = acvt(s.S0[nb][0], s.S0[nb][1]); s.pf[nb][1] = acvt(s.S0[nb][2], s.S0[nb][3]); } }
#define CHI(nb) [&] { if constexpr ((nb) < NB) { s.pf[nb][2] = acvt(s.S1[nb][0], s.S1[nb][1]); s.pf[nb][3] = acvt(s.S1[nb][2], s.S1[nb][3]); } }
#define U(f, ...) [&] { use(f); SB(); (__VA_ARGS__)(); }
  unsigned dk = 8192, dv = 8192, cnt = 1;
  if constexpr (NB == 3) {
#pragma unroll
    for (int st = 0; st < 4; ++st) ka[st] += 8192;
  }
  auto astep = [&](auto FI, auto SL, int t, auto kvh) {
    constexpr bool FIRST = decltype(FI)::value;
    constexpr int sl = decltype(SL)::value;
    auto rk = [&](int kt, int st) { return rks(NB == 3 ? 0 : sl, kt, st); };
    auto rv = [&](int db) { return rvs(NB == 3 ? 0 : (sl + 4) % 5, db); };
    if constexpr (NB == 2) { gov(GOVA); wait_vm_bar<6>(); } else wait_vm_bar<63>();
    auto PH = [&](int db, bf16x8& fr) { return [&, db](int nb) { if constexpr (!FIRST) amfma(s.o[nb][db], fr, asb(s.pf[nb])); }; };
    auto PV = [&](int db, bf16x8 fr) { return [&s, db, fr](int nb) { amf<FIRST>(s.o[nb][db], fr, asb(s.pf[nb])); }; };
    auto SUM = [&](int nb) { amf<FIRST>(s.l[nb], s.ones, asb(s.pf[nb])); };
    bf16x8 a0, a1, a2, a3, b0, b1, b2, b3, v0, v1, v2, v3, v4;
    unsigned o1, o2;
    if constexpr (NB == 3) {
      bf16x8 v5;
      us(PH(6, v6), [&] { a0 = rk(0, 0); a1 = rk(0, 1); }, [&] { a2 = rk(0, 2); a3 = rk(0, 3); }, E1(6));
      us(PH(7, v7), CLO(0), CLO(1), U(a0, CLO(2)));
      us(QK0(0, a0), [&] { b0 = rk(1, 0); b1 = rk(1, 1); }, E1(7), U(a1, E1(8)));
      us(QK0(1, a1), E1(9), E1(10), U(a2, E1(11)));
      us(QK0(2, a2), [&] { o1 = aor3(s.pf[0][0], s.pf[0][1], s.pf[1][0]); o2 = aor3(s.pf[1][1], s.pf[2][0], s.pf[2][1]); }, CHI(0), U(a3, CHI(1)));
      us(QK0(3, a3), CHI(2), [&] { o1 = aor3(o1, s.pf[0][2], s.pf[0][3]); o2 = aor3(o2, s.pf[1][2], s.pf[1][3]); },
                     U(b0, [&] { o1 = aor3(o1, s.pf[2][2], s.pf[2][3]); s.orv = o1 | o2; }));
      if constexpr (!FIRST) check<NB, true>(s, c, t - 1);
      SB();
      us(QK1(0, b0), [&] { b2 = rk(1, 2); b3 = rk(1, 3); }, [&] { v0 = rv(0); }, U(b1, [&] { aaddu(ka[0], dk); aaddu(ka[1], dk); }));
      us(QK1(1, b1), [&] { v1 = rv(1); }, E0(0), U(b2, E0(1)));
      us(QK1(2, b2), [&] { v2 = rv(2); }, E0(2), U(b3, E0(3)));
      us(QK1(3, b3), [&] { v3 = rv(3); }, E0(4), E0(5));
      us(SUM, E0(6), E0(7), U(v0, E0(8)));
      us(PV(0, v0), [&] { v4 = rv(4); }, E0(9), U(v1, E0(10)));
      us(PV(1, v1), [&] { v5 = rv(5); }, E0(11), U(v2, E1(0)));
      us(PV(2, v2), [&] { v6 = rv(6); }, E1(1), U(v3, E1(2)));
      us(PV(3, v3), [&] { v7 = rv(7); }, E1(3), U(v4, E1(4)));
      us(PV(4, v4), E1(5), [&] { aaddu(ka[2], dk); aaddu(ka[3], dk); }, U(v5, [&] { aaddu(va, dv); }));
      us(PV(5, v5), [&] { dv = dk; cnt = cnt == 4 ? 0 : cnt + 1; pins(cnt); }, [&] { use(v7); dk = cnt == 4 ? -32768 : 8192; pins(dk); }, N);
    } else {
      us(PH(5, v5h), [&] { a0 = rk(0, 0); a1 = rk(0, 1); }, [&] { a2 = rk(0, 2); a3 = rk(0, 3); }, N);
      us(PH(6, v6), E1(4), [&] { kvh(0); }, N);
      us(PH(7, v7), CLO(0), U(a0, CLO(1)), N);
      us(QK0(0, a0), [&] { b0 = rk(1, 0); b1 = rk(1, 1); }, U(a1, E1(5)), N);
      us(QK0(1, a1), E1(6), U(a2, E1(7)), N);
      us(QK0(2, a2), [&] { o1 = aor3(s.pf[0][0], s.pf[0][1], s.pf[1][0]); kvh(1); }, U(a3, CHI(0)), N);
      us(QK0(3, a3), CHI(1), U(b0, [&] { o1 = aor3(o1, s.pf[1][1], s.pf[0][2]); o2 = aor3(s.pf[0][3], s.pf[1][2], s.pf[1][3]); s.orv = o1 | o2; }), N);
      if constexpr (!FIRST) check<NB, true>(s, c, t - 1);
      SB();
      us(QK1(0, b0), [&] { b2 = rk(1, 2); b3 = rk(1, 3); }, U(b1, [&] { v0 = rv(0); kvh(2); }), N);
      us(QK1(1, b1), [&] { v1 = rv(1); }, U(b2, [&] { kvh(3); }), N);
      us(QK1(2, b2), [&] { v2 = rv(2); }, U(b3, E0(0)), N);
      us(QK1(3, b3), [&] { v3 = rv(3); }, [&] { exp1<NB, 1>(s.S0); kvh(4); }, N);
      us(SUM, E0(2), U(v0, E0(3)), N);
      us(PV(0, v0), [&] { v4 = rv(4); }, U(v1, [&] { exp1<NB, 4>(s.S0); kvh(5); }), N);
      us(PV(1, v1), [&] { v5h = rv(5); }, U(v2, E0(5)), N);
      us(PV(2, v2), [&] { v6 = rv(6); }, U(v3, [&] { exp1<NB, 6>(s.S0); kvh(6); }), N);
      us(PV(3, v3), [&] { v7 = rv(7); exp1<NB, 7>(s.S0); }, U(v4, [&] { exp1<NB, 0>(s.S1); kvh(7); }), N);
      us(PV(4, v4), [&] { exp1<NB, 1>(s.S1); exp1<NB, 2>(s.S1); }, [&] { exp1<NB, 3>(s.S1); use(v7); }, N);
    }
  };
  auto piece = [&](int t, int n) {
    const int p0 = (PP2 * (t - 1) + 1) / 2, p = p0 + n;
    if (p < (PP2 * t + 1) / 2 && p < (32 - NG) * 4) {
      const int it = NG + (p >> 2), h = (p >> 1) & 1, v = it == 31 || !(it & 1), tn = it == 31 ? 15 : v ? (it - 2) / 2 : (it + 1) / 2;
      const unsigned so = tso(tn) + h * 131072;
      if (p & 1) { wait_vm<THR>(); dmao2<1, 1024>(v ? voffV[1] : voffK[1], v ? c.rv : c.rk, so); }
      else { setm0(jb + (v ? vslot(tn % 5, h) : kslot(tn % 5, h))); wait_vm<THR>(); dmao2<1, 0>(v ? voffV[0] : voffK[0], v ? c.rv : c.rk, so); }
    }
  };
  astep(std::true_type{}, IC<1>{}, 1, [&](int n) { if constexpr (NB == 2) {
    piece(1, n);
#pragma unroll
    for (int i = 0; i < (NG - 3) * 4; ++i) if (i * 8 / ((NG - 3) * 4) == n) { const int m = i >> 2, q = i & 3;
      st128(jb + lane * 16 + ((m & 1) ? vslot(1 + (m >> 1), q >> 1) : kslot(2 + (m >> 1), q >> 1)) + (q & 1) * 1024, kvr[i]); } } });
  constexpr std::false_type F{};
  if constexpr (NB == 3) {
    auto NH = [](int) {};
#pragma unroll 1
    for (int t = 2; t < 16; ++t) astep(F, IC<0>{}, t, NH);
  } else {
    static_for(std::make_integer_sequence<int, 12>{}, [&](auto TT) {
      constexpr int t = decltype(TT)::value + 2;
      astep(F, IC<t % 5>{}, t, [&](int n) { piece(t, n); });
    });
  }
  rsrc_t rof, rwo, rslab; int pr, hg, rotb; gu64* sync; u16* ob;
  {
    int id = c.id; asm volatile("" : "+s"(id));
    const int br = (id >> 2) & 1, b = id >> 5;
    hg = id & 3; pr = (id >> 3) & 3; rotb = b & 3;
    rof = mkr(c.ws + (size_t)((br * 8 + b) * 4 + hg) * 327680, 327680);
    rwo = mkr((const char*)(c.wo + (size_t)hg * 1024 * 640 + pr * 160) - 4096, 1024 * 1280 - pr * 320 + 4096);
    rslab = mkr(c.ws + WS_SLAB + (size_t)((br * 8 + b) * 4 + pr) * (12 * QB), 12 * QB);
    sync = (gu64*)(c.ws + WS_SYNC);
    ob = c.out + (size_t)(b * 160 + (hg & 1) * 80) * 1280 + br * 640 + pr * 160 + (hg >> 1) * 80;
  }
  unsigned lz = 0;
  const int dks = c.j >> 1, dh = c.j & 1;
  auto stg = [](int u) { return u * 40960; };
  auto kpair = [&](int i) { return ((pr + (i >> 2)) & 3) * 4 + ((i + rotb) & 3); };
  auto wvo = [&](int n) { const int e = 64 * n + (int)lz, row = e / 20, ch = e - row * 20; return row * 1280 + ((ch ^ (((row >> 2) & 1) << 1)) * 16) + 4096; };
  auto wso = [&](int i) { return (kpair(i) * 64 + dks * 32 + dh * 16) * 1280; };
  auto wld = [&](int i, int n) { return lds0 + stg(i & 3) + 20480 + dks * 10240 + dh * 5120 + n * 1024; };
  u32x4 wr[15];
  if constexpr (NB == 2) {
    lz = laneid();
    unsigned vw[5];
#pragma unroll
    for (int n = 0; n < 5; ++n) vw[n] = wvo(n);
    auto wl = [&](int p) { wait_vm<4>(); if (p < 15) wr[p] = __builtin_amdgcn_raw_buffer_load_b128(rwo, vw[p % 5], wso(p / 5), 0); };
    SB();
    astep(F, IC<4>{}, 14, [&](int n) { wl(n); });
    astep(F, IC<0>{}, 15, [&](int n) { wl(8 + n); });
  }
  if constexpr (NB == 3) wait_vm_bar<63>(); else wait_vm_bar<15>();
  u32x4 ow[NB][4];
  unsigned gb;
  auto ost = [&](int e, int nb) { __builtin_amdgcn_raw_buffer_store_b128(ow[nb][e], rof, gb + e * 10240 + nb * 1024, 0, 16); };
  {
    bf16x8 vf[8];
#pragma unroll
    for (int db = 0; db < 4; ++db) vf[db] = rvs(0, db);
    SB();
    if constexpr (NB == 2) us(PVH(5, v5h), N, N, N);
    us(PVH(6, v6), N, N, N);
    exps<NB, 2 * NB, 4 * NB>(s.S1);
    us(PVH(7, v7), CLO(0), CLO(1), CLO(2));
    CHI(0)(); CHI(1)(); CHI(2)();
    s.orv = 0;
#pragma unroll
    for (int nb = 0; nb < NB; ++nb) { s.orv = aor3(s.orv, s.pf[nb][0], s.pf[nb][1]); s.orv = aor3(s.orv, s.pf[nb][2], s.pf[nb][3]); }
    SB();
    check<NB, false>(s, c, 15);
    SB();
    if constexpr (NB == 3) lz = laneid();
#pragma unroll
    for (int db = 4; db < 8; ++db) vf[db] = rvs(0, db);
    gb = lz * 16 + (pr * 8 + c.hs * 4) * 10240 + c.blk0 * 1024;
#pragma unroll
    for (int nb = 0; nb < NB; ++nb) s.l[nb] = mfma(s.ones, asb(s.pf[nb]), s.l[nb]);
    float inv[NB];
    auto norm = [&](int e, int nb) {
      f32x4 a0 = s.o[nb][2 * e], a1 = s.o[nb][2 * e + 1];
#pragma unroll
      for (int r = 0; r < 4; ++r) { float x = a0[r] * inv[nb], y = a1[r] * inv[nb]; pin(x); pin(y); a0[r] = x; a1[r] = y; }
      ow[nb][e] = u32x4{cvtpk(a0[0], a0[1]), cvtpk(a0[2], a0[3]), cvtpk(a1[0], a1[1]), cvtpk(a1[2], a1[3])};
      if (e < EPRE) ost(e, nb);
    };
#pragma unroll
    for (int e = 0; e < 4; ++e) {
#pragma unroll
      for (int nb = 0; nb < NB; ++nb) {
        s.o[nb][2 * e] = mfma(vf[2 * e], asb(s.pf[nb]), s.o[nb][2 * e]);
        s.o[nb][2 * e + 1] = mfma(vf[2 * e + 1], asb(s.pf[nb]), s.o[nb][2 * e + 1]);
        if (e == 0) inv[nb] = __builtin_amdgcn_rcpf(s.l[nb][0]); else norm(e - 1, nb);
        if constexpr (NB == 2) { const int p = (e * 2 + nb) * 2; st128(wld(p / 5, p % 5) + lz * 16, wr[p]); if (p + 1 < 15) st128(wld((p + 1) / 5, (p + 1) % 5) + lz * 16, wr[p + 1]); }
        SB();
      }
    }
#pragma unroll
    for (int nb = 0; nb < NB; ++nb) norm(3, nb);
  }
  asm volatile("s_waitcnt lgkmcnt(0)\n\ts_barrier" ::: "memory");
#pragma unroll
  for (int eh = 0; eh < 2; ++eh) {
    const unsigned lb = lds0 + stg((c.hs * 2 + eh - rotb) & 3) + c.blk0 * 1024 + lz * 16;
#pragma unroll
    for (int el = 0; el < 2; ++el)
#pragma unroll
      for (int nb = 0; nb < NB; ++nb) st128(lb + el * 10240 + nb * 1024, ow[nb][eh * 2 + el]);
  }
  {
    constexpr int NM = NB, M0 = NM == 3 ? 0 : 3;
    const int q = c.j, qm = q & 1, qn = q >> 1, zg = lz >> 4;
    unsigned vo3[5];
#pragma unroll
    for (int n = 0; n < 5; ++n) vo3[n] = NB == 3 ? wvo(n) - (n & 3) * 1024 : lz * 16 + (n >> 2) * 4096;
    const unsigned lb3 = lds0 + dks * 10240 + dh * 5120 + (NB == 3 ? 20480 : 0);
    auto so3 = [&](int i) { unsigned v = (NB == 3 ? wso(i) : (kpair(i) * 2 + dks) * 10240 + dh * 5120) + (unsigned)(i >> 4) * OOB; pins(v); return v; };
    auto dmal = [&](unsigned so, int u, int n) {
      constexpr int POL = NB == 3 ? 0 : 2;
      const rsrc_t r = NB == 3 ? rwo : rof;
      const unsigned vo = vo3[NB == 3 ? n : (n >> 2) * 4];
      if (n == 0) { setm0(lb3 + stg(u)); dmao<POL, 0>(vo, r, so); }
      else if (n == 1) dmao2<POL, 1024>(vo, r, so);
      else if (n == 2) dmao2<POL, 2048>(vo, r, so);
      else if (n == 3) dmao2<POL, 3072>(vo, r, so);
      else { setm0(lb3 + stg(u) + 4096); dmao<POL, 0>(vo, r, so); }
    };
    if constexpr (NB == 3) {
      const unsigned so = so3(3);
#pragma unroll
      for (int n = 0; n < 5; ++n) dmal(so, 3, n);
    }
    unsigned oa[2], wa[2][2];
    {
      oa[0] = lds0 + (qm * 5 + M0) * 1024 + lz * 16; oa[1] = oa[0] + 81920;
      const unsigned wb = lds0 + 20480 + (4 * zg + ((lz & 15) >> 2)) * 320 + (lz & 3) * 8 + qn * 160;
      const int sg = (qn ? -32 : 32) * (zg & 1);
      wa[0][0] = wb + sg; wa[0][1] = wb - sg; wa[1][0] = wa[0][0] + 81920; wa[1][1] = wa[0][1] + 81920;
      pin(oa[1]); pin(wa[1][0]); pin(wa[1][1]);
    }
    f32x4 acc[NM][5];
    bf16x8 h2, h3, h4, op[NM];
    auto interval = [&](auto UU, auto D, auto tail) {
      constexpr bool FIRST = decltype(UU)::value < 0;
      constexpr int u = FIRST ? 0 : decltype(UU)::value, so = (u & 1) * 40960;
      auto ro = [&](int ks, int m) { return ld128(oa[u >> 1] + so + ks * 10240 + m * 1024); };
      auto rw = [&](int ks, int n) { const unsigned a = wa[u >> 1][n & 1] + so + ks * 10240 + n * 32; return cat(ldtr(a), ldtr(a + 5120)); };
      auto mm = [&](auto Z, int n, bf16x8& wf, bf16x8 (&of)[NM], auto g0, auto g1, auto g2) {
        constexpr int z = decltype(Z)::value;
        if constexpr (z == 2) { g0(); g1(); g2(); SB(); }
        else {
          amf<z == 1>(acc[0][n], wf, of[0]); SB(); g0(); SB(); amf<z == 1>(acc[1][n], wf, of[1]); SB(); g1(); SB();
          if constexpr (NM == 3) { amf<z == 1>(acc[2][n], wf, of[2]); SB(); }
          g2(); SB();
        }
      };
      constexpr IC<FIRST ? 2 : 0> H{};
      constexpr IC<FIRST ? 1 : 0> C{};
      constexpr IC<0> P{};
      bf16x8 o0[NM], o1[NM], a0, a1, a2, a3, a4, b0, b1;
      mm(H, 2, h2, op, [&] { a0 = rw(0, 0); }, [&] { o0[0] = ro(0, 0); o0[1] = ro(0, 1); }, [&] { if constexpr (NM == 3) o0[2] = ro(0, 2); });
      mm(H, 3, h3, op, [&] { a1 = rw(0, 1); }, [&] { a2 = rw(0, 2); }, [&] { D(0); });
      mm(H, 4, h4, op, [&] { a3 = rw(0, 3); }, [&] { a4 = rw(0, 4); }, [&] { use(o0[NM - 1]); });
      mm(C, 0, a0, o0, [&] { o1[0] = ro(1, 0); o1[1] = ro(1, 1); }, [&] { use(a1); if constexpr (NM == 3) o1[2] = ro(1, 2); }, [&] { D(1); });
      mm(C, 1, a1, o0, [&] { b0 = rw(1, 0); }, [&] { use(a2); b1 = rw(1, 1); }, N0);
      mm(C, 2, a2, o0, [&] { h2 = rw(1, 2); }, [&] { use(a3); }, [&] { D(2); });
      mm(C, 3, a3, o0, [&] { h3 = rw(1, 3); }, [&] { use(a4); }, N0);
      mm(C, 4, a4, o0, [&] { h4 = rw(1, 4); }, [&] { use(b0); }, [&] { D(3); });
      mm(P, 0, b0, o1, N0, [&] { use(b1); }, [&] { D(4); });
      mm(P, 1, b1, o1, [&] { use(h4); }, N0, N0);
      tail();
#pragma unroll
      for (int m = 0; m < NM; ++m) op[m] = o1[m];
    };
    auto ND = [](int) {};
    SB();
    auto poll = [&](int base) {
      const int pid = c.id ^ (((lz & 3) ? (lz & 3) : 1) << 3);
      for (unsigned spins = 0; spins < SPINS; ++spins) {
        if (__all(__hip_atomic_load(sync + (base + pid) * FS, RLX_AGENT) == tag)) break;
        __builtin_amdgcn_s_sleep(1);
      }
    };
    wait_vm_bar<63>();
    interval(IC<-1>{}, [&](int n) {
#pragma unroll
      for (int k = 0; k < (4 - EPRE) * NB; ++k) if (k * 5 / (EPRE < 4 ? (4 - EPRE) * NB : 1) == n) ost(EPRE + k / NB, k % NB); }, N0);
    wait_vm_bar<63>();
    { const unsigned so = so3(4); interval(IC<1>{}, [&](int n) { if constexpr (NB == 3) dmal(so, 0, n); }, N0); }
    if constexpr (NB == 3) wait_vm_bar<5>(); else wait_vm_bar<0>();
    if (c.w == 3 && lz == 0) __hip_atomic_store(sync + (1 + c.id) * FS, tag, RLX_AGENT);
    const unsigned so5 = so3(5);
    interval(IC<2>{}, [&](int n) { if constexpr (NB == 3) dmal(so5, 1, n); }, [&] { if (c.w == 2) poll(1); });
    if constexpr (NB == 3) wait_vm_bar<10>(); else wait_vm_bar<63>();
    { const unsigned sa = so3(NB == 3 ? 6 : 4);
      interval(IC<3>{}, [&](int n) { if constexpr (NB == 3) dmal(sa, 2, n);
        else { if (n == 0) { dmal(sa, 0, 0); dmal(sa, 0, 1); } else if (n == 1) { dmal(sa, 0, 2); dmal(sa, 0, 3); } else if (n == 2) { dmal(sa, 0, 4); dmal(so5, 1, 0); }
               else if (n == 3) { dmal(so5, 1, 1); dmal(so5, 1, 2); } else { dmal(so5, 1, 3); dmal(so5, 1, 4); } } }, N0); }
    unsigned son = so3(NB == 3 ? 7 : 6);
#pragma unroll 1
    for (int i4 = 4; i4 < 16; i4 += 4)
      static_for(std::make_integer_sequence<int, 4>{}, [&](auto UU) {
        constexpr int u = decltype(UU)::value;
        if constexpr (NB == 3) wait_vm_bar<10>(); else wait_vm_bar<5>();
        interval(UU, [&](int n) { dmal(son, (u + (NB == 3 ? 3 : 2)) & 3, n); if (n == 4) son = so3(i4 + u + (NB == 3 ? 4 : 3)); }, N0);
      });
#pragma unroll
    for (int m = 0; m < NM; ++m) { amfma(acc[m][2], h2, op[m]); amfma(acc[m][3], h3, op[m]); amfma(acc[m][4], h4, op[m]); }
    asm volatile("s_nop 7");
    SB();
    if (q != hg) {
      const int so = (q * 3 + ((hg - q - 1) & 3)) * QB;
#pragma unroll
      for (int e = 0; e < NM * 5; e += 2) {
        const f32x4 a = acc[e / 5][e % 5], b = e + 1 < NM * 5 ? acc[(e + 1) / 5][(e + 1) % 5] : z4;
        __builtin_amdgcn_raw_buffer_store_b128(u32x4{cvtpk(a[0], a[1]), cvtpk(a[2], a[3]), cvtpk(b[0], b[1]), cvtpk(b[2], b[3])}, rslab, lz * 16 + ((M0 ? 8 : 0) + e / 2) * 1024 + so, 0, 16);
      }
    }
    asm volatile("s_waitcnt vmcnt(0) lgkmcnt(0)\n\ts_barrier" ::: "memory");
    if (c.w == 0 && lz == 0) __hip_atomic_store(sync + (257 + c.id) * FS, tag, RLX_AGENT);
    if (q == hg) {
#pragma unroll
      for (int e = 0; e < NM * 5; ++e) st128(lds0 + lz * 16 + (M0 * 5 + e) * 1024, __builtin_bit_cast(u32x4, acc[e / 5][e % 5]));
    }
    if (c.w == ((hg + 1) & 3)) {
      const int pid = c.id ^ ((lz & 3) ? (lz & 3) : 1);
      for (unsigned spins = 0; spins < SPINS; ++spins) {
        if (__all(__hip_atomic_load(sync + (257 + pid) * FS, RLX_AGENT) == tag)) break;
        __builtin_amdgcn_s_sleep(1);
      }
    }
    if (c.id == 0 && c.w == 7) {
      for (unsigned spins = 0; spins < SPINS; ++spins) {
        bool ok = true;
#pragma unroll
        for (int k = 0; k < 4; ++k) ok &= __hip_atomic_load(sync + (1 + lz + 64 * k) * FS, RLX_AGENT) == tag;
        if (__all(ok)) break;
        __builtin_amdgcn_s_sleep(1);
      }
      if (lz == 0) __hip_atomic_store(sync, tag, RLX_AGENT);
    }
    asm volatile("s_waitcnt vmcnt(0) lgkmcnt(0)\n\ts_barrier" ::: "memory");
    u16* op16 = ob + (size_t)(lz & 15) * 1280 + 4 * zg;
    u32x4 r[2][3];
#pragma unroll
    for (int k = 0; k < 2; ++k)
#pragma unroll
      for (int sl = 0; sl < 3; ++sl) r[k][sl] = __builtin_amdgcn_raw_buffer_load_b128(rslab, c.w + 8 * k < 13 ? lz * 16 + (c.w + 8 * k) * 1024 : OOB, (hg * 3 + sl) * QB, 16);
#pragma unroll
    for (int k = 0; k < 2; ++k) {
      const int un = c.w + 8 * k;
#pragma unroll
      for (int hh = 0; hh < 2; ++hh) {
        const int e = un < 8 ? 2 * un + hh : 15 + 2 * (un - 8) + hh;
        if (un < 13 && !(un == 7 && hh == 1)) {
          f32x4 v = *(const LDS3 f32x4*)(size_t)(lds0 + lz * 16 + e * 1024);
#pragma unroll
          for (int sl = 0; sl < 3; ++sl) { const unsigned lo = r[k][sl][2 * hh], hi = r[k][sl][2 * hh + 1];
            v += f32x4{__uint_as_float(lo << 16), __uint_as_float(lo & 0xffff0000u), __uint_as_float(hi << 16), __uint_as_float(hi & 0xffff0000u)}; }
          const int m = e / 5, n = e - m * 5;
          *(__attribute__((address_space(1))) u32x2*)(op16 + m * (16 * 1280) + n * 16) = u32x2{cvtpk(v[0], v[1]), cvtpk(v[2], v[3])};
        }
      }
    }
  }
}

struct Args { const void* p[16]; };
__global__ __launch_bounds__(512, 2) void fused(Args) {
  extern __shared__ __attribute__((aligned(16))) char smem[];
  typedef const __attribute__((address_space(4))) unsigned long long* kargp;
  kargp ka = (kargp)__builtin_amdgcn_kernarg_segment_ptr();
  const int id = blockIdx.x, xg = id & 7, cc = id >> 3, br = xg >> 2, hg = xg & 3, b = cc >> 2, pr = cc & 3, h0 = hg * 8 + pr * 2;
  ka += br * 8;
  const u16* x = (const u16*)ka[0]; const u16* k = (const u16*)ka[1]; const u16* v = (const u16*)ka[2]; const u16* wq = (const u16*)ka[3];
  const int w = __builtin_amdgcn_readfirstlane(threadIdx.x >> 6);
  asm volatile("s_nop 4");
  Ctx c;
  { unsigned long long pc; asm volatile("s_getpc_b64 %0" : "=s"(pc)); c.code = pc & ~255ull; }
  c.rx = mkr(x + (size_t)b * SQ * DM, SQ * DM * 2);
  c.rw = mkr(wq + h0 * DH, DM * NH * DH * 2 - h0 * DH * 2);
  c.rk = mkr(k + (size_t)(b * NH + h0) * SK * DH, 2 * SK * DH * 2);
  c.rv = mkr((const char*)(v + (size_t)(b * NH + h0) * SK * DH) - 1024, 2 * SK * DH * 2 + 1024);
  c.lds0 = (unsigned)(size_t)(LDS3 char*)smem;
  c.hs = w >> 2; c.j = w & 3; c.rot = (id * 5) & 15; c.krot = 0;
  c.w = w; c.id = id; c.wo = (const u16*)ka[4]; c.out = (u16*)ka[5]; c.ws = (char*)ka[6];
  const bool big = (c.j < 2) == (c.hs == 0);
  c.blk0 = (0x74208630u >> (4 * w)) & 15;
  if (big) body<3>(c); else body<2>(c);
}

extern "C" void kernel_launch(void* const* d_in, const int* in_sizes, int n_in, void* d_out, int out_size, void* d_ws, size_t ws_size, hipStream_t stream) {
  (void)hipFuncSetAttribute((const void*)fused, hipFuncAttributeMaxDynamicSharedMemorySize, LDS_BYTES);
  Args a;
  for (int br = 0; br < 2; ++br) {
    for (int i = 0; i < 5; ++i) a.p[br * 8 + i] = d_in[br * 5 + i];
    a.p[br * 8 + 5] = d_out; a.p[br * 8 + 6] = d_ws; a.p[br * 8 + 7] = d_ws;
  }
  fused<<<256, 512, LDS_BYTES, stream>>>(a);
}
```
